```python
import torch

def _softmax(x, axis=-1):
    return torch.softmax(x, dim=axis)
def _gelu(x):
    return torch.nn.functional.gelu(x, approximate="tanh")
def _silu(x):
    return torch.nn.functional.silu(x)
def _sigmoid(x):
    return torch.sigmoid(x)
def _layernorm(x, axis=-1, eps=1e-5):
    m = x.mean(dim=axis, keepdim=True)
    v = x.var(dim=axis, keepdim=True, unbiased=False)
    return (x - m) / torch.sqrt(v + eps)
def _scatter_add(tgt, idx, src, axis=0):
    return tgt.clone().index_add_(axis, idx, src)
def _segment_sum(src, seg, n):
    out = torch.zeros((n,) + src.shape[1:], dtype=src.dtype)
    return out.index_add_(0, seg, src)
def _pad(x, pw):
    flat = tuple(v for pair in reversed(pw) for v in pair)
    return torch.nn.functional.pad(x, flat)

kernel_name = 'interleave_layers__sliding_block_cached__glu_gated_swiglu__decode'

def setup_inputs():

    _w = (lambda a: (lambda t: t.to(torch.bfloat16) if t.is_floating_point() else t)(torch.as_tensor(a)))
    def _A_setup():
        B, Sq, D = 1, 1, 640
        Hq, Hkv, Sk, Dh = 32, 32, 32768, 192
        assert Hq % Hkv == 0, (Hq, Hkv)
        return {
            "x": _w((lambda *s: torch.randn(*s, dtype=torch.bfloat16) * 0.05)(B, Sq, D)),
            "k_cache": _w((lambda *s: torch.randn(*s, dtype=torch.bfloat16) * 0.8)(B, Hkv, Sk, Dh)),
            "v_cache": _w((lambda *s: torch.randn(*s, dtype=torch.bfloat16) * 1.6)(B, Hkv, Sk, Dh)),
            "wq": _w((lambda *s: torch.randn(*s, dtype=torch.bfloat16) * 0.8)(D, Hq * Dh) * (D ** -0.5)),
            "wo": _w((lambda *s: torch.randn(*s, dtype=torch.bfloat16) * 1.6)(Hq * Dh, D) * ((Hq * Dh) ** -0.5)),
        }
    _A_in = _A_setup()

    def _B_setup():
        B, S, D, F = 1, 1, 640, 1536
        return {
            "x": _w((lambda *s: torch.randn(*s, dtype=torch.bfloat16) * 0.05)(B, S, D)),
            "w1": _w((lambda *s: torch.randn(*s, dtype=torch.bfloat16) * 0.4)(D, F) * (2.0 / D) ** 0.5),
            "w3": _w((lambda *s: torch.randn(*s, dtype=torch.bfloat16) * 0.4)(D, F) * (2.0 / D) ** 0.5),
            "w2": _w((lambda *s: torch.randn(*s, dtype=torch.bfloat16) * 0.4)(F, D) * (2.0 / F) ** 0.5),
        }
    _B_in = _B_setup()

    return {
        "A_x": _A_in["x"],
        "A_k_cache": _A_in["k_cache"],
        "A_v_cache": _A_in["v_cache"],
        "A_wq": _A_in["wq"],
        "A_wo": _A_in["wo"],
        "B_x": _B_in["x"],
        "B_w1": _B_in["w1"],
        "B_w3": _B_in["w3"],
        "B_w2": _B_in["w2"],
    }

def reference(A_x, A_k_cache, A_v_cache, A_wq, A_wo, B_x, B_w1, B_w3, B_w2):
    def _A_ref(x, k_cache, v_cache, wq, wo):
        B, Sq, D = x.shape
        Hq, Dh = 32, 192
        Hkv, Sk = k_cache.shape[1], k_cache.shape[2]
        W = min(4096, max(256, Sk // 4))
        rep = Hq // Hkv
        q = (lambda x, *d: x.permute(*d))((x @ wq).reshape(B, Sq, Hq, Dh), 0, 2, 1, 3)
        k = (lambda x, n, axis: torch.repeat_interleave(x, n, dim=axis))(k_cache[:, :, Sk - W:, :], rep, axis=1)
        v = (lambda x, n, axis: torch.repeat_interleave(x, n, dim=axis))(v_cache[:, :, Sk - W:, :], rep, axis=1)
        scores = torch.einsum("bhqd,bhkd->bhqk", q, k) * (Dh ** -0.5)
        p = _softmax(scores, axis=-1)
        o = torch.einsum("bhqk,bhkd->bhqd", p, v)
        return (lambda x, *d: x.permute(*d))(o, 0, 2, 1, 3).reshape(B, Sq, Hq * Dh) @ wo

    def _B_ref(x, w1, w3, w2):
        gate = torch.einsum("bsd,df->bsf", x, w1)
        up = torch.einsum("bsd,df->bsf", x, w3)
        h = _silu(gate) * up
        out = torch.einsum("bsf,fd->bsd", h, w2)
        return out

    x = A_x
    x = x + _A_ref(_layernorm(x, axis=-1), A_k_cache, A_v_cache, A_wq, A_wo)
    x = x + _B_ref(_layernorm(x, axis=-1), B_w1, B_w3, B_w2)
    x = x + _A_ref(_layernorm(x, axis=-1), A_k_cache, A_v_cache, A_wq, A_wo)
    x = x + _B_ref(_layernorm(x, axis=-1), B_w1, B_w3, B_w2)
    return x
```

```cpp
#include <hip/hip_runtime.h>
#include <hip/hip_bf16.h>
#include <stdint.h>

namespace {

typedef unsigned int u32;
typedef unsigned short u16;
typedef unsigned long long u64;
using u32x2 = __attribute__((ext_vector_type(2))) unsigned;
using u32x4 = __attribute__((ext_vector_type(4))) unsigned;
using f32x2 = __attribute__((ext_vector_type(2))) float;
using f32x4 = __attribute__((ext_vector_type(4))) float;
using s16x4 = __attribute__((ext_vector_type(4))) short;
using bf16x2 = __attribute__((ext_vector_type(2))) __bf16;
using bf16x8 = __attribute__((ext_vector_type(8))) __bf16;
#define LAS __attribute__((address_space(3)))

constexpr int D = 640, H = 32, DH = 192, WIN = 4096, SK = 32768, F = 1536;
constexpr int NWG = 256, NT = 512;
constexpr u32 SPIN_CAP = 1u << 16;
constexpr float SCL = 0.07216878364870322f * 1.4426950408889634f;

#define T_O 40
#define T_YF 122
#define T_H 120
#define T_AN 35
#define PTHR 60
#define T_LAZY 60

#define A_E 122
#define A_Q (A_E + 84)
#define A_O (A_Q + 146)
#define A_Y (A_O + 96)
#define A_H (A_Y + 94)
#define A_F (A_H + 58)


constexpr u32 SIDE = 8192, AREA = 16 * 16384;
constexpr u32 A_GO = 0;
constexpr u32 A_GQ = 4 * 16384;
constexpr u32 A_GH = A_GQ + 7168;
constexpr u32 A_GY = 8 * 16384;
constexpr u32 GO_SZ = 32768, GY_PAIR = 65536, GY_INST = 32768;
constexpr u32 WS_CLR = 4 * AREA;
constexpr u32 WS_PB = WS_CLR;
#define HBS 4352
constexpr u32 WS_GOF = WS_PB + (128 << 10), WS_HB = WS_GOF + 8 * HBS;
constexpr u32 WS_GQ1 = WS_GOF + (192 << 10);
constexpr u32 WS_END = WS_GQ1 + 256 * 2048;
static_assert(WS_END <= (2u << 20) && WS_CLR == 256 * 4096, "layout");
__device__ __forceinline__ u32 go_slot(int w) { return (u32)(w >> 2) * 16384u + (u32)(w & 3) * 2048u; }
__device__ __forceinline__ u32 gy_row(int i) { return go_slot(i); }
__device__ __forceinline__ u32 gy_chunk(int u) {
  const int i = (u * 205) >> 11;
  return gy_row(i) + (u32)(u - 10 * i) * 16u;
}
__device__ __forceinline__ u32 st_line(int g) { return A_GH + 768u + (u32)(g >> 1) * 16384u + (u32)(g & 1) * 128u; }
__device__ __forceinline__ u32 gy_rep(int r) { return (u32)(r & 3) * AREA + (u32)(r >> 2) * SIDE; }

constexpr int L_ZERO = 0;
constexpr int L_CNT = 16;
constexpr int L_RI = 28;
constexpr int L_ONE = 32;
constexpr int L_TAG = 48;
constexpr int L_ST = 64;
constexpr int L_VOTE = L_ST;
constexpr int L_ANC = 192;
constexpr int L_Q = 256;
constexpr int L_CS = 640;
constexpr int L_HB = L_CS + 768;
constexpr int L_ACT = L_HB + D * 2;
constexpr int L_RED = L_ACT + 2 * 384;
constexpr int L_P = L_RED + 32 * 144;
constexpr int P_W = 1280, P_U = 592, P_PAR = 288;
constexpr int OM_S = 400, OM_P = 200;
constexpr int L_OM = L_P + 8 * P_W;
constexpr int L_LN = L_OM;
constexpr int L_LAND = (L_OM + 8 * OM_S * 4 + 255) / 256 * 256;
constexpr int L_WQ = L_LAND + 8 * 2048;
constexpr int L_WB = L_WQ + 6 * 5120;
constexpr int L_W2 = L_WB + 768 * 64;
constexpr int L_PF = L_W2 + 192 * 64;
constexpr int L_TAB = L_PF + 256;
constexpr int L_TOTAL = L_TAB + 8 * 2048;
static_assert(L_LAND % 256 == 0 && L_P % 16 == 0 && L_WQ % 16 == 0 && L_HB % 16 == 0 && L_RED % 16 == 0, "alignment");


struct Params {
  const u16* x0;
  const u16* kc;
  const u16* vc;
  const u16* wq;
  const u16* wo;
  const u16* w1;
  const u16* w3;
  const u16* w2;
  u16* out;
  u32* ws;
};

__device__ __forceinline__ u32 cvt2(float lo, float hi) {
  return __builtin_bit_cast(u32, __builtin_convertvector(f32x2{lo, hi}, bf16x2));
}
__device__ __forceinline__ float bflo(u32 w) { return __uint_as_float(w << 16); }
__device__ __forceinline__ float bfhi(u32 w) { return __uint_as_float(w & 0xffff0000u); }
__device__ __forceinline__ float bf16r(float x) { return bflo(cvt2(x, x)); }

template <int CTRL>
__device__ __forceinline__ float dpp(float x) {
  return __builtin_bit_cast(float, __builtin_amdgcn_mov_dpp(__builtin_bit_cast(int, x), CTRL, 0xf, 0xf, true));
}
constexpr int XOR1 = 0xB1, XOR2 = 0x4E, XOR7 = 0x141, ROR8 = 0x128;
__device__ __forceinline__ float fmx(float a, float b) {
  float r;
  asm("v_max_f32 %0, %1, %2" : "=v"(r) : "v"(a), "v"(b));
  return r;
}
__device__ __forceinline__ float xrow16_max(float x) {
  auto s = __builtin_amdgcn_permlane16_swap(__float_as_uint(x), __float_as_uint(x), false, false);
  x = fmx(__uint_as_float(s[0]), __uint_as_float(s[1]));
  auto t = __builtin_amdgcn_permlane32_swap(__float_as_uint(x), __float_as_uint(x), false, false);
  return fmx(__uint_as_float(t[0]), __uint_as_float(t[1]));
}
__device__ __forceinline__ float xrow16_sum(float x) {
  auto s = __builtin_amdgcn_permlane16_swap(__float_as_uint(x), __float_as_uint(x), false, false);
  x = __uint_as_float(s[0]) + __uint_as_float(s[1]);
  auto t = __builtin_amdgcn_permlane32_swap(__float_as_uint(x), __float_as_uint(x), false, false);
  return __uint_as_float(t[0]) + __uint_as_float(t[1]);
}
__device__ __forceinline__ float sum8(float v) {
  v += dpp<XOR1>(v);
  v += dpp<XOR2>(v);
  v += dpp<XOR7>(v);
  return v;
}
__device__ __forceinline__ float max8(float v) {
  asm("s_nop 1\n\tv_max_f32_dpp %0, %0, %0 quad_perm:[1,0,3,2] row_mask:0xf bank_mask:0xf\n\t"
      "s_nop 1\n\tv_max_f32_dpp %0, %0, %0 quad_perm:[2,3,0,1] row_mask:0xf bank_mask:0xf\n\t"
      "s_nop 1\n\tv_max_f32_dpp %0, %0, %0 row_half_mirror row_mask:0xf bank_mask:0xf"
      : "+v"(v));
  return v;
}
__device__ __forceinline__ float max_ror8(float v) {
  asm("s_nop 1\n\tv_max_f32_dpp %0, %0, %0 row_ror:8 row_mask:0xf bank_mask:0xf" : "+v"(v));
  return v;
}
__device__ __forceinline__ f32x4 sum4x4(f32x4 v) {
  float a = v[0], b = v[1], c = v[2], d = v[3];
#define S4(CTL)                                               \
  "v_add_f32_dpp %0, %0, %0 " CTL " row_mask:0xf bank_mask:0xf\n\t" \
  "v_add_f32_dpp %1, %1, %1 " CTL " row_mask:0xf bank_mask:0xf\n\t" \
  "v_add_f32_dpp %2, %2, %2 " CTL " row_mask:0xf bank_mask:0xf\n\t" \
  "v_add_f32_dpp %3, %3, %3 " CTL " row_mask:0xf bank_mask:0xf\n\t"
  asm("s_nop 1\n\t" S4("quad_perm:[1,0,3,2]") S4("quad_perm:[2,3,0,1]") : "+v"(a), "+v"(b), "+v"(c), "+v"(d));
#undef S4
  return f32x4{a, b, c, d};
}
__device__ __forceinline__ float wave_sum(float v) {
  v = sum8(v);
  v += dpp<ROR8>(v);
  return xrow16_sum(v);
}
__device__ __forceinline__ float tsum8(f32x4 a) {
  auto s0 = __builtin_amdgcn_permlane32_swap(__float_as_uint(a[0]), __float_as_uint(a[1]), false, false);
  auto s1 = __builtin_amdgcn_permlane32_swap(__float_as_uint(a[2]), __float_as_uint(a[3]), false, false);
  const float r0 = __uint_as_float(s0[0]) + __uint_as_float(s0[1]);
  const float r1 = __uint_as_float(s1[0]) + __uint_as_float(s1[1]);
  auto t = __builtin_amdgcn_permlane16_swap(__float_as_uint(r0), __float_as_uint(r1), false, false);
  float r = __uint_as_float(t[0]) + __uint_as_float(t[1]);
  r += dpp<ROR8>(r);
  return r;
}

__device__ __forceinline__ u32 tagv(float v) { return __float_as_uint(v) | 1u; }
template <int AUX = 16>
__device__ __forceinline__ void store_quad(__amdgpu_buffer_rsrc_t rs, u32 off, f32x4 v) {
  const u32x4 s = {tagv(v[0]), tagv(v[1]), tagv(v[2]), tagv(v[3])};
  __builtin_amdgcn_raw_buffer_store_b128(s, rs, off, 0, AUX);
}
template <int N>
__device__ __forceinline__ void gpoll4(u32x4 rs, const u32 (&off)[N], f32x4 (&v)[N]) {
  bool need = true;
  u32 budget = SPIN_CAP;
#pragma unroll
  for (int k = 0; k < N; ++k) asm volatile("" : "=v"(v[k]));
#pragma unroll 1
  for (;;) {
    if (need) {
#pragma unroll
      for (int k = 0; k < N; ++k)
        asm volatile("buffer_load_dwordx4 %0, %1, %2, 0 offen sc1" : "+v"(v[k]) : "v"(off[k]), "s"(rs) : "memory");
    }
    asm volatile("s_waitcnt vmcnt(0)" ::: "memory");
    u32 a = 1u;
#pragma unroll
    for (int k = 0; k < N; ++k) {
      asm volatile("" : "+v"(v[k]));
      a &= __float_as_uint(v[k][0]) & __float_as_uint(v[k][1]) & __float_as_uint(v[k][2]) & __float_as_uint(v[k][3]);
    }
    need = a == 0u;
    if (__builtin_amdgcn_ballot_w64(need) == 0ull || budget == 0u) break;
    --budget;
    __builtin_amdgcn_s_sleep(1);
  }
}
__device__ __forceinline__ u32 now() { return (u32)__builtin_amdgcn_s_memtime(); }
__device__ __forceinline__ void wait_until(u32 t) {
#pragma unroll 1
  while ((int)(now() - t) < -160) __builtin_amdgcn_s_sleep(1);
#pragma unroll 1
  while ((int)(now() - t) < 0) {
  }
}
__device__ __forceinline__ u32 rtnow() { return (u32)__builtin_amdgcn_s_memrealtime(); }
__device__ __forceinline__ void wait_rt(u32 t) {
  u32 spins = 0;
#pragma unroll 1
  while ((int)(rtnow() - t) < -8 && ++spins < 400u) __builtin_amdgcn_s_sleep(1);
#pragma unroll 1
  while ((int)(rtnow() - t) < 0 && ++spins < 440u) {
  }
}
template <int BITS>
__device__ __forceinline__ u32 youngest(u32 t);
__device__ __forceinline__ void lds_wait(volatile LAS u32* p, u32 want) {
  u32 spins = 0;
#pragma unroll 1
  while (__builtin_amdgcn_readfirstlane(*p) != want) {
    if (++spins > (SPIN_CAP << 6)) break;
    __builtin_amdgcn_s_sleep(1);
  }
  asm volatile("" ::: "memory");
}
__device__ __forceinline__ u32 lds_wait_nz(volatile LAS u32* p) {
  u32 spins = 0, v;
#pragma unroll 1
  while ((v = __builtin_amdgcn_readfirstlane(*p)) == 0u) {
    if (++spins > (SPIN_CAP << 6)) break;
    __builtin_amdgcn_s_sleep(1);
  }
  asm volatile("" ::: "memory");
  return v;
}
template <int BITS>
__device__ __forceinline__ u32 youngest(u32 t) {
  const u32 nw = rtnow();
  const float age = (float)((nw - t) & ((1u << BITS) - 1u));
  return nw - (u32)__builtin_amdgcn_readfirstlane((int)-xrow16_max(max_ror8(max8(-age))));
}
__device__ __forceinline__ void stamp_poll(unsigned char* smem, u32x4 rq, u32 so) {
  u32 sv, budget = SPIN_CAP;
#pragma unroll 1
  for (;;) {
    asm volatile("buffer_load_dword %0, %1, %2, 0 offen sc1\n\ts_waitcnt vmcnt(0)" : "=&v"(sv) : "v"(so), "s"(rq) : "memory");
    if (__builtin_amdgcn_ballot_w64((sv & 1u) == 0u) == 0ull || budget == 0u) break;
    --budget;
    __builtin_amdgcn_s_sleep(1);
  }
  *(volatile LAS u32*)(smem + L_ANC) = youngest<24>(sv >> 1) | 1u;
}
__device__ __forceinline__ void lds_add(unsigned char* smem, int off, u32 v) {
  __hip_atomic_fetch_add((LAS u32*)(smem + off), v, __ATOMIC_RELAXED, __HIP_MEMORY_SCOPE_WORKGROUP);
}

__device__ __forceinline__ int fresh(int t) {
  asm volatile("" : "+v"(t));
  return t;
}
__device__ __forceinline__ f32x4 zero4() {
  f32x2 a, b;
  asm volatile("v_mov_b64 %0, 0\n\tv_mov_b64 %1, 0" : "=v"(a), "=v"(b));
  return f32x4{a[0], a[1], b[0], b[1]};
}
__device__ __forceinline__ void bar() { asm volatile("s_waitcnt lgkmcnt(0)\n\ts_barrier" ::: "memory"); }

__device__ __forceinline__ u32x2 trread(unsigned char* smem, int off) {
  return __builtin_bit_cast(u32x2, __builtin_amdgcn_ds_read_tr16_b64_v4i16((LAS s16x4*)(smem + off)));
}
__device__ __forceinline__ u32x4 wfrag(unsigned char* smem, int lb, int off) {
  const u32x2 a = trread(smem, lb + off), b = trread(smem, lb + off + 256);
  return u32x4{a[0], a[1], b[0], b[1]};
}
__device__ __forceinline__ int wlane(int lane, int cb) {
  const int i = lane & 15, G = lane >> 4;
  return (8 * G + (i >> 2)) * 64 + (i & 3) * 8 + ((cb ^ (G & 1)) * 32);
}
__device__ __forceinline__ u32x4 afrag(unsigned char* smem, int a) { return *reinterpret_cast<const u32x4*>(smem + a); }
__device__ __forceinline__ f32x4 mfma32(u32x4 w, u32x4 a, f32x4 c) {
  return __builtin_amdgcn_mfma_f32_16x16x32_bf16(__builtin_bit_cast(bf16x8, w), __builtin_bit_cast(bf16x8, a), c, 0, 0, 0);
}

__device__ __forceinline__ u32 dma_off(int ld, int p, int lane) {
  const int row = 16 * p + (lane >> 2), part = lane & 3;
  const int cb = (part >> 1) ^ (lane >> 5);
  return (u32)(row * ld + cb * 16 + (part & 1) * 8) * 2u;
}
__device__ __forceinline__ void dma_go(const u16* src, u32 voff, unsigned char* smem, int ldsoff) {
  const u32 dst = (u32)(uintptr_t)(LAS unsigned char*)smem + (u32)ldsoff;
  u32 keep;
  asm volatile("s_mov_b32 %0, m0\n\ts_mov_b32 m0, %2\n\ts_nop 2\n\tglobal_load_lds_dwordx4 %1, %3 nt\n\ts_mov_b32 m0, %0"
               : "=&s"(keep)
               : "v"(voff), "s"(dst), "s"(src)
               : "memory");
}
__device__ __forceinline__ void dma_piece(const u16* src, int ld, unsigned char* smem, int ldsoff, int p, int lane) {
  dma_go(src, dma_off(ld, p, lane), smem, ldsoff + p * 1024);
}

template <int CTRL>
__device__ __forceinline__ u32 dppu(u32 x) {
  return (u32)__builtin_amdgcn_mov_dpp((int)x, CTRL, 0xf, 0xf, true);
}
__device__ __forceinline__ float sum4(f32x4 d) { return (d[0] + d[1]) + (d[2] + d[3]); }
__device__ __forceinline__ u32 and4(u32x4 v) { return v[0] & v[1] & v[2] & v[3]; }
template <int AUX = 16>
__device__ __forceinline__ void store16(__amdgpu_buffer_rsrc_t rs, u32 off, u32x4 v) {
  __builtin_amdgcn_raw_buffer_store_b128(v, rs, off, 0, AUX);
}
#define POLL16(dst, voff, imm) \
  asm volatile("buffer_load_dwordx4 %0, %1, %2, 0 offen offset:%3 sc1" : "+v"(dst) : "v"(voff), "s"(rq), "n"(imm) : "memory")

struct LnS {
  float mb, ri;
};
__device__ __forceinline__ LnS ln_stats(float s1, float s2) {
#define S2(CTL)                                                     \
  "v_add_f32_dpp %0, %0, %0 " CTL " row_mask:0xf bank_mask:0xf\n\t" \
  "v_add_f32_dpp %1, %1, %1 " CTL " row_mask:0xf bank_mask:0xf\n\ts_nop 0\n\t"
  asm("s_nop 1\n\t" S2("quad_perm:[1,0,3,2]") S2("quad_perm:[2,3,0,1]") S2("row_half_mirror") : "+v"(s1), "+v"(s2));
#undef S2
  const float dm = s1 * (1.0f / D);
  const u32 mv = cvt2(dm, __builtin_fmaxf(fmaf(-dm, dm, s2 * (1.0f / D)), 0.f));
  LnS r;
  r.mb = bflo(mv);
  r.ri = __builtin_amdgcn_rcpf(bf16r(__builtin_amdgcn_sqrtf(bf16r(bfhi(mv) + 1e-5f))));
  return r;
}

struct G80 {
  u32x4 w[6];
  int ra, rb, rc;
};
__device__ __forceinline__ void g80_load(G80& a, unsigned char* smem, int lane, int wb, int xs, int land) {
  const int m = lane & 15, G = lane >> 4;
  const int l0 = wb + wlane(lane, 0), l1 = wb + wlane(lane, 1), bk = 4096 - (G >> 1) * 1024;
  a.w[0] = wfrag(smem, l0, 0);
  a.w[1] = wfrag(smem, l1, 0);
  a.w[2] = wfrag(smem, l0, 2048);
  a.w[3] = wfrag(smem, l1, 2048);
  a.w[4] = wfrag(smem, l0 + bk, 0);
  a.w[5] = wfrag(smem, l1 + bk, 0);
  const bool vec = m == 8 || (m < 8 && land >= 0);
  const int st = vec ? 64 : 0;
  const int sa = vec ? (m == 8 ? xs : land + 256 * m) + 16 * G : (m == 15 ? L_ONE : L_ZERO);
  const int sw = (vec && m < 8) ? (m & 3) * 64 : 0;
  a.ra = fresh(sa ^ sw);
  a.rb = fresh((sa + st) ^ sw);
  a.rc = fresh(G < 2 ? (sa + 2 * st) ^ sw : L_TAG);
}
template <bool WAIT = true, bool NT = false>
__device__ __forceinline__ void dma_poll(u32x4 rq, u32 voff, u32 land, u64 ea, u64 eb) {
  u32 keep;
  if (NT)
  asm volatile("s_mov_b32 %0, m0\n\ts_mov_b32 m0, %3\n\ts_mov_b64 exec, %4\n\t"
               "buffer_load_dwordx4 %1, %2, 0 offen sc1 nt lds\n\t"
               "s_mov_b64 exec, %5\n\t"
               "buffer_load_dwordx4 %1, %2, 0 offen offset:1024 sc1 nt lds\n\t"
               "s_mov_b64 exec, -1\n\ts_mov_b32 m0, %0"
               : "=&s"(keep)
               : "v"(voff), "s"(rq), "s"(land), "s"(ea), "s"(eb)
               : "memory");
  else
  asm volatile("s_mov_b32 %0, m0\n\ts_mov_b32 m0, %3\n\ts_mov_b64 exec, %4\n\t"
               "buffer_load_dwordx4 %1, %2, 0 offen sc1 lds\n\t"
               "s_mov_b64 exec, %5\n\t"
               "buffer_load_dwordx4 %1, %2, 0 offen offset:1024 sc1 lds\n\t"
               "s_mov_b64 exec, -1\n\ts_mov_b32 m0, %0"
               : "=&s"(keep)
               : "v"(voff), "s"(rq), "s"(land), "s"(ea), "s"(eb)
               : "memory");
  if (WAIT) asm volatile("s_waitcnt vmcnt(0)" ::: "memory");
}
template <bool X4>
__device__ __forceinline__ void ctl_load(u32x4 rq, u32 voff, u32 lds) {
  u32 keep;
  if (X4)
    asm volatile("s_mov_b32 %0, m0\n\ts_mov_b32 m0, %3\n\ts_nop 2\n\tbuffer_load_dwordx4 %1, %2, 0 offen sc1 lds\n\ts_mov_b32 m0, %0"
                 : "=&s"(keep) : "v"(voff), "s"(rq), "s"(lds) : "memory");
  else
    asm volatile("s_mov_b32 %0, m0\n\ts_mov_b32 m0, %3\n\ts_nop 2\n\tbuffer_load_dword %1, %2, 0 offen sc1 lds\n\ts_mov_b32 m0, %0"
                 : "=&s"(keep) : "v"(voff), "s"(rq), "s"(lds) : "memory");
}
template <int UN>
__device__ __forceinline__ void retry_masks(u64 need, int lane, u64& ea, u64& eb) {
  u32 m = (u32)need | (u32)(need >> 32);
  m |= m >> 16;
  m = (m | (m >> 8)) & 0xffu;
  const int ln = fresh(lane);
  ea = __builtin_amdgcn_ballot_w64(((m >> (ln / UN)) & 1u) != 0u);
  eb = __builtin_amdgcn_ballot_w64(((m >> ((ln + 64) / UN)) & 1u) != 0u);
}
#define MF "v_mfma_f32_16x16x32_bf16 "
#define F0 "v[52:55]"
#define F1 "v[56:59]"
#define F2 "v[60:63]"
#define FCLOB "v52", "v53", "v54", "v55", "v56", "v57", "v58", "v59", "v60", "v61", "v62", "v63"
#define AND3(a, b, c) "v_bitop3_b32 %[t], " a ", " b ", " c " bitop3:0x80\n\t"
template <bool POLL>
__device__ __forceinline__ void g80_run(G80& a, unsigned char* smem, u32x4 rq, int lane, u32 voff, u32 land, float& r0,
                                        float& r1, float& z) {
  f32x4 d0, d1;
  if (POLL) {
    f32x4 d2;
    constexpr u64 USED = 0xff30ff0330ff03ffull;
    u64 ea = USED, eb = USED;
    u32 budget = SPIN_CAP;
#pragma unroll 1
    for (;;) {
      dma_poll(rq, voff, land, ea, eb);
      u64 need;
      u32 t;
      asm volatile("ds_read_b128 " F0 ", %[ra]\n\tds_read_b128 " F1 ", %[rb]\n\tds_read_b128 " F2 ", %[rc]\n\t"
                   "s_waitcnt lgkmcnt(2)\n\t"
                   MF "%[d2], " F0 ", " F0 ", 0\n\t" AND3("v52", "v53", "v54")
                   MF "%[d0], " F0 ", %[w0], 0\n\t" MF "%[d1], " F0 ", %[w1], 0\n\t"
                   "s_waitcnt lgkmcnt(1)\n\t"
                   MF "%[d2], " F1 ", " F1 ", %[d2]\n\t" AND3("%[t]", "v55", "v56")
                   MF "%[d0], " F1 ", %[w2], %[d0]\n\t" AND3("%[t]", "v57", "v58")
                   MF "%[d1], " F1 ", %[w3], %[d1]\n\t"
                   "s_waitcnt lgkmcnt(0)\n\t"
                   MF "%[d2], " F2 ", " F2 ", %[d2]\n\t" AND3("%[t]", "v59", "v60") AND3("%[t]", "v61", "v62")
                   MF "%[d0], " F2 ", %[w4], %[d0]\n\t" AND3("%[t]", "1", "v63")
                   "v_cmp_eq_u32 %[need], 0, %[t]\n\t"
                   MF "%[d1], " F2 ", %[w5], %[d1]"
                   : [d0] "=&v"(d0), [d1] "=&v"(d1), [d2] "=&v"(d2), [t] "=&v"(t), [need] "=&s"(need)
                   : [ra] "v"(a.ra), [rb] "v"(a.rb), [rc] "v"(a.rc), [w0] "v"(a.w[0]), [w1] "v"(a.w[1]), [w2] "v"(a.w[2]),
                     [w3] "v"(a.w[3]), [w4] "v"(a.w[4]), [w5] "v"(a.w[5])
                   : "memory", FCLOB);
      need &= 0x00ff00ff00ff00ffull;
      if (need == 0ull || budget == 0u) break;
      --budget;
      retry_masks<16>(need, lane, ea, eb);
      ea &= USED;
      eb &= USED;
    }
    asm volatile("s_nop 1" : "+v"(d2));
    z = sum4(d2);
    asm volatile("s_nop 1" : "+v"(z), "+v"(d0), "+v"(d1));
    z = (lane & 15) == 15 ? 0.f : z;
    z = sum8(z);
    z += dpp<ROR8>(z);
  } else {
    const u32x4 f0 = afrag(smem, a.ra), f1 = afrag(smem, a.rb), f2 = afrag(smem, a.rc);
    d0 = d1 = f32x4{0.f, 0.f, 0.f, 0.f};
    d0 = mfma32(f0, a.w[0], d0);
    d1 = mfma32(f0, a.w[1], d1);
    d0 = mfma32(f1, a.w[2], d0);
    d1 = mfma32(f1, a.w[3], d1);
    d0 = mfma32(f2, a.w[4], d0);
    d1 = mfma32(f2, a.w[5], d1);
  }
  r0 = sum4(d0);
  r1 = sum4(d1);
}
__device__ __forceinline__ f32x4 quad4(float r) { return f32x4{r, dpp<0x55>(r), dpp<0xAA>(r), dpp<0xFF>(r)}; }

__device__ __forceinline__ int q_addr(int d) {
  const int w = d >> 1;
  return L_Q + ((w & 1) * 48 + (w >> 1)) * 4 + (d & 1) * 2;
}

__device__ __forceinline__ void q1_finish(unsigned char* smem, int lane, int wave) {
  const int e = wave * 32 + (lane & 7) * 4 + ((lane >> 4) & 1) * 2 + (lane >> 5);
  asm volatile("" ::: "memory");
  const u32x4 v = afrag(smem, L_LAND + wave * 2048 + lane * 16);
  const u32x4 w = afrag(smem, L_LAND + wave * 2048 + 1024 + lane * 16);
  const float t = tsum8(__builtin_bit_cast(f32x4, v));
  *reinterpret_cast<u16*>(smem + q_addr(e)) = (u16)cvt2(t, t);
  *reinterpret_cast<float*>(smem + L_CS + e * 4) = tsum8(__builtin_bit_cast(f32x4, w)) * (-1.0f / D);
}
template <bool LDS>
__device__ __forceinline__ void x_update(unsigned char* smem, u32x4 rq, int lane, u32 src, int i) {
  const int l = lane < 40 ? lane : 0;
  const u32 off = src + 4 * l;
  u32 v[8];
  if (LDS) {
#pragma unroll
    for (int k = 0; k < 8; ++k) v[k] = *reinterpret_cast<const u32*>(smem + (off ^ ((k & 3) * 64)) + 256 * k);
  } else {
    u32 budget = SPIN_CAP;
#pragma unroll 1
    for (;;) {
#pragma unroll
      for (int k = 0; k < 8; ++k)
        asm volatile("buffer_load_dword %0, %1, %2, 0 offen offset:%3 sc1" : "=v"(v[k]) : "v"(off), "s"(rq), "n"(256 * k) : "memory");
      asm volatile("s_waitcnt vmcnt(0)"
                   : "+v"(v[0]), "+v"(v[1]), "+v"(v[2]), "+v"(v[3]), "+v"(v[4]), "+v"(v[5]), "+v"(v[6]), "+v"(v[7])::"memory");
      const u32 a = v[0] & v[1] & v[2] & v[3] & v[4] & v[5] & v[6] & v[7] & 1u;
      if (__builtin_amdgcn_ballot_w64(a == 0u) == 0ull || budget == 0u) break;
      --budget;
      __builtin_amdgcn_s_sleep(2);
    }
  }
  float lo = 0.f, hi = 0.f;
#pragma unroll
  for (int k = 0; k < 8; ++k) {
    lo += bflo(v[k]);
    hi += bfhi(v[k]);
  }
  u32* px = reinterpret_cast<u32*>(smem + L_HB + 160 * i + 4 * l);
  const u32 xo = *px, y = cvt2(lo, hi);
  if (lane < 40) *px = cvt2(bflo(xo) + bflo(y), bfhi(xo) + bfhi(y));
}
template <int N>
__device__ __forceinline__ void col_poll(u32x4 rq, const u32 (&off)[N], float (&y)[N]) {
  u32x2 v[N];
  u32 budget = SPIN_CAP;
#pragma unroll 1
  for (;;) {
    u32 a = 1u;
#pragma unroll
    for (int k = 0; k < N; ++k)
      asm volatile("buffer_load_dwordx2 %0, %1, %2, 0 offen sc1" : "=v"(v[k]) : "v"(off[k]), "s"(rq) : "memory");
    asm volatile("s_waitcnt vmcnt(0)" ::: "memory");
#pragma unroll
    for (int k = 0; k < N; ++k) {
      asm volatile("" : "+v"(v[k]));
      a &= v[k][0] & v[k][1];
    }
    if (__builtin_amdgcn_ballot_w64(a == 0u) == 0ull || budget == 0u) break;
    --budget;
  }
#pragma unroll
  for (int k = 0; k < N; ++k) y[k] = bf16r(tsum8(f32x4{bflo(v[k][0]), bfhi(v[k][0]), bflo(v[k][1]), bfhi(v[k][1])}));
}

#define AUXQ 18
#define AUXC 16
#define LCL 24
#define QKP 1
#define CKQ 23
#define BEAT0 10
#define CKL 15
#define CKG 16
#define WBIG 10
#define TSTEP 240
#define QI 5
#define NINO 4
#define NINF 1
#define NPRE 2
#define NPF 2
#define NQF 0
#define NPOST (6 - NINO - NPRE)
static_assert(NPOST >= 0 && NPOST <= 1 && NINF + NPF + NQF <= 5 && NPF <= NPRE && NQF <= 1, "counts");
#define TPF 220
#define TFD 0
#define T_FW 30
#define T_FD 12
#define WP0 17
#define WPD 2
#define WPX 0
static_assert(WP0 + (NINO - 1) * WPD < 24 + 13, "pieces before the scores");
#define TPV 220
#define XA 13
#define XB 21
#define TSV 228
#define KSW 12
#define TS_O (TSV * (20 * (48 - KSW + NINO) + 12 * (48 - KSW + NINF)) / (32 * (48 - KSW + NINO)))
#define TS_F (TSV * (20 * (48 - KSW + NINO) + 12 * (48 - KSW + NINF)) / (32 * (48 - KSW + NINF)))
#define TSQ 256
#define ROW0 ((wave * 8 + c) * 8)
#define GST 512
#define ROT h
static_assert(XB - WBIG <= WBIG + 2 && WBIG + 2 <= XA, "window");
#define PACE_ASM                                                                                                     \
  "1:\n\ts_memrealtime s[98:99]\n\ts_waitcnt lgkmcnt(0)\n\ts_lshl_b32 s98, s98, 3\n\ts_sub_i32 s98, s98, %[tn]\n\t"  \
  "s_cmp_lt_i32 s98, 0\n\ts_cbranch_scc0 2f\n\ts_sleep 1\n\ts_branch 1b\n2:\n\ts_add_i32 %[tn], %[tn], %[ts]\n\t"
__device__ __forceinline__ void pace(u32& tnext, u32 tstep) {
  asm volatile(PACE_ASM : [tn] "+s"(tnext) : [ts] "s"(tstep) : "scc", "s98", "s99");
}
constexpr int kreg(int i) { return i % 3 < 2 ? 64 + 8 * (i / 3) + 4 * (i % 3) : 128 + 4 * (i / 3); }
constexpr int vreg(int i) { return 160 + 24 * (i / 6) + 8 * (i % 3) + 4 * ((i / 3) & 1); }
#define TBACK 0
#define LOADW(reg, voff, sbase, imm)                                                                                \
  asm volatile(PACE_ASM "global_load_dwordx4 v[%[r0]:%[r1]], %[p], %[sb] offset:%[im] nt\n\ts_waitcnt vmcnt(%[w])"  \
               : [tn] "+s"(tnext)                                                                                   \
               : [r0] "n"(reg), [r1] "n"((reg) + 3), [p] "v"(voff), [sb] "s"(sbase), [im] "n"(imm), [ts] "s"(tstep), \
                 [w] "n"(WBIG - 1)                                                                                  \
               : "scc", "s98", "s99")

#define HS_WAIT(COND)                                                                                \
  _Pragma("unroll 1") for (;;) {                                                                     \
    gd = CTL[512 + lane0] - CTL[576 + lane0];                                                        \
    const u64 ch = __builtin_amdgcn_ballot_w64(gd != 0u) & 0x1ffull;                                 \
    if (ch > 0xffull || (COND) || tmo == 0u) break;                                                  \
    if ((tmo & 3u) == 0u && lane0 == 0)                                                              \
      __builtin_amdgcn_raw_buffer_store_b32(hb, rs, WS_HB + h * HBS + c * 4, 0, 16);                 \
    hb += 4u;                                                                                        \
    --tmo;                                                                                           \
    __builtin_amdgcn_s_sleep(3);                                                                     \
    ctl_load<false>(rq, hso, (u32)(smb + L_CTL + 2048));                                             \
    asm volatile("s_waitcnt vmcnt(0)" ::: "memory");                                                 \
  }

struct QKAddr {
  int qad[6], idxq, wa;
  f32x4 D0, D1;
};
struct PVAddr {
  int ra, idx, oa, sa, la, ma;
  u32 dst;
  f32x4 O0, O1, O2;
  u32x4 A0, A1, A2, A3;
};
template <int K>
__device__ __forceinline__ void att_table(unsigned char* smem, int lane, int wave, u32 go) {
  const int n = lane & 15, G = lane >> 4;
  const int seg = n & 1, pr = (n >> 1) & 1, tq = n >> 2, xy = G >> 1;
  const int qa = L_Q + (pr * 48 + seg * 8 + (G & 1) * 4) * 4;
  u32 t[16];
#pragma unroll
  for (int k = 0; k < 4; ++k) t[k] = tq == k ? qa + xy * 64 : L_ZERO;
#pragma unroll
  for (int k = 0; k < 2; ++k) t[4 + k] = tq == 2 * k + xy ? qa + 128 : L_ZERO;
  t[6] = 0x4444 + pr * 0xAAAA;
  t[7] = L_P + wave * P_W + ((n >> 1) & 1) * P_PAR + ((((G & 1) * 2 + (n >> 3)) * 4 + (n & 1) * 2 + (G >> 1)) * 16) + ((n >> 2) & 1) * 8;
  t[8] = L_P + wave * P_W + ((n >> 2) & 1) * P_U + (n >> 3) * P_PAR + G * 64;
  t[9] = (n & 3) * 0x5555;
  t[10] = L_OM + (wave * OM_S + (n >> 3) * OM_P + (n & 7) * 4 + (G & 1) * 32) * 4;
  t[11] = L_OM + (wave * OM_S + DH + G) * 4;
  t[12] = L_OM + ((lane & 3) * OM_S + (wave & 3) * 64 + (lane >> 3) * 4 + (lane & 4) * 8) * 4;
  t[13] = L_OM + ((lane & 3) * OM_S + DH + 4) * 4;
  const int d0 = wave * 64 + (lane >> 3) * 8;
  t[14] = (go + (wave < 3 ? (d0 >= 96 ? 2048 - 192 : 0) + d0 * 2 : lane * 2048 + 192)) & 0xffffu;
  t[15] = q_addr(wave * 32 + (lane & 7) * 4 + ((lane >> 4) & 1) * 2 + (lane >> 5));
  *reinterpret_cast<u32x4*>(smem + L_TAB + wave * 2048 + K * 1024 + lane * 16) =
      u32x4{t[8 * K] | (t[8 * K + 4] << 16), t[8 * K + 1] | (t[8 * K + 5] << 16), t[8 * K + 2] | (t[8 * K + 6] << 16),
            t[8 * K + 3] | (t[8 * K + 7] << 16)};
}
__device__ __forceinline__ void att_setup(QKAddr& r, PVAddr& v, unsigned char* smem, int lane, int wave, int& qw) {
  const u32x4 a = afrag(smem, L_TAB + wave * 2048 + lane * 16), b = afrag(smem, L_TAB + wave * 2048 + 1024 + lane * 16);
  r.D0 = zero4();
  r.D1 = zero4();
  qw = fresh(b[3] >> 16);
#pragma unroll
  for (int k = 0; k < 4; ++k) r.qad[k] = fresh(a[k] & 0xffffu);
  r.qad[4] = fresh(a[0] >> 16);
  r.qad[5] = fresh(a[1] >> 16);
  r.idxq = fresh(a[2] >> 16);
  r.wa = fresh(a[3] >> 16);
  v.ra = fresh(b[0] & 0xffffu);
  v.idx = fresh(b[1] & 0xffffu);
  v.oa = fresh(b[2] & 0xffffu);
  v.la = fresh(b[3] & 0xffffu);
  v.sa = fresh(b[0] >> 16);
  v.ma = fresh(b[1] >> 16);
  v.dst = fresh(b[2] >> 16);
  asm volatile("" : "+v"(r.D0), "+v"(r.D1));
}
#define SM "v_smfmac_f32_16x16x64_bf16 "
#define P_WRITE_READ(QA)                                                                                              \
  asm volatile("ds_write2_b32 %[wa], %[p], %[p] offset1:%[o1]\n\t"                                                    \
               "ds_read_b128 %[a0], %[ra]\n\tds_read_b128 %[a1], %[ra] offset:16\n\t"                                 \
               "ds_read_b128 %[a2], %[ra] offset:32\n\tds_read_b128 %[a3], %[ra] offset:48"                           \
               : [a0] "=&v"(pv.A0), [a1] "=&v"(pv.A1), [a2] "=&v"(pv.A2), [a3] "=&v"(pv.A3), "+v"(sk)                 \
               : [wa] "v"(QA.wa + smb), [p] "v"(cvt2(pend, 0.f)), [ra] "v"(pv.ra + smb), [o1] "n"(P_U / 4 + 1)        \
               : "memory");
#define QK_SOFTMAX(QA)                                                                                                \
  {                                                                                                                   \
    u32x4 A0, A1, A2, A3, A4, A5;                                                                                     \
    float scl;                                                                                                        \
    asm volatile("ds_read_b128 %[a0], %[q0]\n\tds_read_b128 %[a1], %[q1]\n\tds_read_b128 %[a2], %[q2]\n\t"            \
                 "ds_read_b128 %[a3], %[q3]\n\tds_read_b128 %[a4], %[q4]\n\tds_read_b128 %[a5], %[q5]"                \
                 : [a0] "=&v"(A0), [a1] "=&v"(A1), [a2] "=&v"(A2), [a3] "=&v"(A3), [a4] "=&v"(A4), [a5] "=&v"(A5)     \
                 : [q0] "v"(QA.qad[0] + smb), [q1] "v"(QA.qad[1] + smb), [q2] "v"(QA.qad[2] + smb),                   \
                   [q3] "v"(QA.qad[3] + smb), [q4] "v"(QA.qad[4] + smb), [q5] "v"(QA.qad[5] + smb)                    \
                 : "memory");                                                                                         \
    asm volatile("ds_read_b32 %[s], %[z] offset:%[ri]\n\t"                                                            \
                 "s_waitcnt lgkmcnt(6)\n\t" SM "%[d0], %[a0], v[64:71], %[ix]\n\t" SM "%[d1], %[a0], v[96:103], %[ix]\n\t"   \
                 "s_waitcnt lgkmcnt(5)\n\t" SM "%[d0], %[a1], v[72:79], %[ix]\n\t" SM "%[d1], %[a1], v[104:111], %[ix]\n\t"   \
                 "s_waitcnt lgkmcnt(4)\n\t" SM "%[d0], %[a2], v[80:87], %[ix]\n\t" SM "%[d1], %[a2], v[112:119], %[ix]\n\t"   \
                 "s_waitcnt lgkmcnt(3)\n\t" SM "%[d0], %[a3], v[88:95], %[ix]\n\t" SM "%[d1], %[a3], v[120:127], %[ix]\n\t"   \
                 "s_waitcnt lgkmcnt(2)\n\t" SM "%[d0], %[a4], v[128:135], %[ix]\n\t" SM "%[d1], %[a4], v[144:151], %[ix]\n\t"   \
                 "s_waitcnt lgkmcnt(0)\n\t" SM "%[d0], %[a5], v[136:143], %[ix]\n\t" SM "%[d1], %[a5], v[152:159], %[ix]\n\t"   \
                                      \
                 "ds_read_b128 %[a0], %[z]\n\tds_read_b128 %[a1], %[z]\n\tds_read_b128 %[a2], %[z]\n\ts_nop 4"        \
                 : [d0] "+v"(QA.D0), [d1] "+v"(QA.D1), [s] "=&v"(scl), [a0] "+v"(A0), [a1] "+v"(A1), [a2] "+v"(A2),   \
                   [a3] "+v"(A3), [a4] "+v"(A4), [a5] "+v"(A5)                                                        \
                 : [ix] "v"(QA.idxq), [z] "v"(smb), [ri] "n"(L_RI), "{v[64:95]}"(kv0), "{v[96:127]}"(kv1),           \
                   "{v[128:159]}"(kv2)                                                                                \
                 : "memory");                                                                                         \
    pv.O0 = __builtin_bit_cast(f32x4, A0);                                                                            \
    pv.O1 = __builtin_bit_cast(f32x4, A1);                                                                            \
    pv.O2 = __builtin_bit_cast(f32x4, A2);                                                                            \
    const f32x2 e0 = f32x2{QA.D0[0], QA.D0[1]} + f32x2{QA.D0[2], QA.D0[3]};                                           \
    const f32x2 e1 = f32x2{QA.D1[0], QA.D1[1]} + f32x2{QA.D1[2], QA.D1[3]};                                           \
    const float s0 = e0[0] + dpp<XOR1>(e0[1]), s1 = e1[1] + dpp<XOR1>(e1[0]);                                         \
    const float sc = ((lane0 & 1) ? s1 : s0) * scl;                                                                   \
    pend = __builtin_amdgcn_exp2f(sc);                                                                                \
    pmax = 0.f;                                                                                                       \
    float sk = sc;                                                                                                    \
    P_WRITE_READ(QA)                                                                                                  \
    const bool unsafe =                                                                                               \
        (__builtin_amdgcn_ballot_w64(!(sk <= 24.f)) != 0ull) | (__builtin_amdgcn_ballot_w64(sk >= -24.f) == 0ull);    \
    if (__builtin_expect(unsafe, 0)) {                                                                                \
      pmax = xrow16_max(max_ror8(max8(sk)));                                                                          \
      pend = __builtin_amdgcn_exp2f(sk - pmax);                                                                       \
      asm volatile("s_waitcnt lgkmcnt(0)" : "+v"(pv.A0), "+v"(pv.A1), "+v"(pv.A2), "+v"(pv.A3));                      \
      P_WRITE_READ(QA)                                                                                                \
      asm volatile("s_waitcnt lgkmcnt(0)" : "+v"(pv.A0), "+v"(pv.A1), "+v"(pv.A2), "+v"(pv.A3));                      \
    }                                                                                                                 \
  }

using u32x16 = __attribute__((ext_vector_type(16))) unsigned;
using u32x32 = __attribute__((ext_vector_type(32))) unsigned;
template <int OFF>
__device__ __forceinline__ u16* karg() {
  u64 p;
  asm volatile("s_load_dwordx2 %0, %1, %2\n\ts_waitcnt lgkmcnt(0)" : "=s"(p) : "s"(__builtin_amdgcn_kernarg_segment_ptr()), "n"(OFF));
  return (u16*)p;
}
template <class T>
__device__ __forceinline__ T* mkptr(u32 lo, u32 hi) {
  return (T*)(((u64)hi << 32) | lo);
}

__global__ __launch_bounds__(NT, 2) void mega_kernel(Params) {
  __shared__ __attribute__((aligned(16))) unsigned char smem[L_TOTAL];
  u32x16 ka;
  u32x4 kb4;
  {
    u64 pc;
    u32 keep;
    asm volatile("s_load_dwordx16 %[ka], %[kp], 0x0\n\ts_load_dwordx4 %[kb], %[kp], 0x40\n\t"
                 "s_getpc_b64 %[pc]\n.Lmk_pc:\n\t"
                 "s_mov_b32 %[keep], m0\n\ts_mov_b32 m0, %[dst]\n\t"
                 "v_lshlrev_b32 v1, 7, %[tid]\n\tv_min_u32 v1, .Lmk_code_end-.Lmk_pc-4, v1\n\t"
                 "v_cmp_gt_u32 vcc, 0x140, %[tid]\n\ts_mov_b64 exec, vcc\n\t"
                 "global_load_lds_dword v1, %[pc]\n\ts_mov_b64 exec, -1\n\ts_mov_b32 m0, %[keep]\n\t"
                 "v_lshrrev_b32 v1, 2, %[tid]\n\tv_cmp_eq_u32 vcc, 3, v1\n\tv_mov_b32 v3, 0x3f803f80\n\ts_nop 0\n\t"
                 "v_cndmask_b32 v2, 0, 1, vcc\n\tv_cmp_eq_u32 vcc, 2, v1\n\tv_lshlrev_b32 v1, 2, %[tid]\n\ts_nop 0\n\t"
                 "v_cndmask_b32 v2, v2, v3, vcc\n\tv_cmp_eq_u32 vcc, 7, %[tid]\n\tv_mov_b32 v3, %[scl]\n\t"
                 "v_min_u32 v1, 80, v1\n\tv_cndmask_b32 v2, v2, v3, vcc\n\t"
                 "ds_write_b32 v1, v2 offset:%[sm]\n\t"
                 "s_waitcnt lgkmcnt(0)\n\ts_barrier"
                 : [ka] "=&s"(ka), [kb] "=&s"(kb4), [pc] "=&s"(pc), [keep] "=&s"(keep)
                 : [kp] "s"(__builtin_amdgcn_kernarg_segment_ptr()), [tid] "v"(threadIdx.x), [dst] "n"(L_PF),
                   [scl] "n"(__builtin_bit_cast(u32, SCL)), [sm] "n"(0)
                 : "memory", "vcc", "v1", "v2", "v3");
  }
  const u32 tstart = (u32)__builtin_amdgcn_s_memrealtime();
  const int tid = threadIdx.x, lane0 = tid & 63;
  const int wave = __builtin_amdgcn_readfirstlane(tid >> 6);
  const int b = blockIdx.x;
  const int g = b & 7, j = b >> 3, hsub = j & 3, c = j >> 2, h = g * 4 + hsub;
  const bool isO = j < 20;
  const bool leader = b == 0 && wave == 6;
  const u32 fslot = (u32)((j & 1) * 16384 + ((j - 20) >> 1) * 128);
  u32 tmo = SPIN_CAP;
  volatile LAS u32* cnt = (volatile LAS u32*)(smem + L_CNT);
  Params P;
  P.x0 = mkptr<const u16>(ka[0], ka[1]);
  P.kc = mkptr<const u16>(ka[2], ka[3]);
  P.vc = mkptr<const u16>(ka[4], ka[5]);
  P.wq = mkptr<const u16>(ka[6], ka[7]);
  P.wo = mkptr<const u16>(ka[8], ka[9]);
  P.w1 = mkptr<const u16>(ka[10], ka[11]);
  P.w3 = mkptr<const u16>(ka[12], ka[13]);
  P.ws = mkptr<u32>(kb4[2], kb4[3]);
  u32* const wsb = (u32*)(((uintptr_t)P.ws + 0x1fffffu) & ~(uintptr_t)0x1fffffu);
  const __amdgpu_buffer_rsrc_t rs = __builtin_amdgcn_make_buffer_rsrc(wsb, 0, (int)WS_END, 0x00020000);
  const u32x4 rq = {(u32)(uintptr_t)wsb, (u32)((uintptr_t)wsb >> 32) & 0xffffu, WS_END, 0x00020000u};

  u32 anc = 0u;
  u32 tref = 0u, ar = 0u;
  u32x32 kv0, kv1, kv2, kv3, kv4, kv5;
  asm volatile(""
               : "={v[64:95]}"(kv0), "={v[96:127]}"(kv1), "={v[128:159]}"(kv2), "={v[160:191]}"(kv3), "={v[192:223]}"(kv4),
                 "={v[224:255]}"(kv5));
  const int smb = (int)(uintptr_t)(LAS unsigned char*)smem;
  const u32 gop = A_GO + go_slot(hsub * 2) + c * 256;
  u32 tstep = TSTEP;
  u32 tnext = tstart * 8u + (u32)wave * (TSTEP / 8) + 5 * TSQ;
  const size_t rowbase = (size_t)h * SK + (SK - WIN) + ROW0;
  const u16* vb = P.vc + rowbase * DH;
  const int rot = ROT;
  const u16* ws0 = isO ? P.wo + (size_t)(768 * g) * D + 32 * j : ((j & 1) ? P.w3 : P.w1) + 192 * g + 32 * ((j - 20) >> 1);
  {
    constexpr int L_CTL = L_LAND + 6 * 2048;
    volatile LAS u32* const CTL = (volatile LAS u32*)(smem + L_CTL);
    u32 gd, hb = 0u;
    const u32 lho = WS_HB + (lane0 >> 1) * HBS + (lane0 & 1) * 16;
    const u32 hso = lane0 < 8 ? WS_HB + (u32)h * HBS + lane0 * 4 : WS_GOF + g * HBS;
    if (wave < 6) {
      const u16* s0 = P.wq + (size_t)(80 * c) * (H * DH) + h * DH + wave * 32;
      u32 tq = tnext - 5 * TSQ;
#pragma unroll 1
      for (int p = 0; p < 5; ++p) {
        pace(tq, TSQ);
        dma_piece(s0, H * DH, smem, L_WQ + wave * 5120, p, lane0);
      }
    } else {
      if (leader) {
        ctl_load<true>(rq, lho, (u32)(smb + L_CTL));
        ctl_load<false>(rq, WS_GOF + (lane0 & 7) * HBS, (u32)(smb + L_CTL + 2560));
      }
      u32 rtt;
      {
        u32 pa = 0u, pb = 0u;
        u64 t1, t2;
        const u32 pbo = WS_PB + (u32)(wave & 1) * (SIDE + 128u) + (u32)((b >> 3) & 7) * 16384u + (u32)((b & 7) * 4 + (b >> 6)) * 256u;
        asm volatile(".p2align 6\n\ts_mov_b64 exec, 1\n\t"
                     "buffer_atomic_or %0, off, %4, %5 sc0 sc1\n\tbuffer_atomic_or %1, off, %4, %6 sc0 sc1\n\t"
                     "s_mov_b64 exec, -1\n\t"
                     "s_waitcnt vmcnt(1)\n\ts_memtime %2\n\ts_waitcnt vmcnt(0)\n\ts_memtime %3\n\ts_waitcnt lgkmcnt(0)"
                     : "+v"(pa), "+v"(pb), "=&s"(t1), "=&s"(t2)
                     : "s"(rq), "s"(pbo), "s"(pbo ^ SIDE)
                     : "memory");
        rtt = __builtin_amdgcn_readfirstlane((u32)t2 - (u32)t1) + 1u;
      }
      if (wave == 6) {
        *(volatile LAS u32*)(smem + L_VOTE) = rtt;
        u32 keep;
        asm volatile("s_mov_b32 %0, m0\n\ts_mov_b32 m0, %4\n\ts_nop 2\n\tglobal_load_lds_dwordx4 %1, %3\n\t"
                     "global_load_lds_dword %2, %3 offset:1024\n\ts_mov_b32 m0, %0"
                     : "=&s"(keep)
                     : "v"(lane0 * 16), "v"(lane0 * 4), "s"(P.x0), "s"((u32)(smb + L_HB))
                     : "memory");
      } else {
        ctl_load<false>(rq, hso, (u32)(smb + L_CTL + 2304));
        store16<AUXC>(rs, (u32)(b & 3) * AREA + (u32)(b >> 7) * A_GY + (u32)((b >> 2) & 31) * 1024 + lane0 * 16, u32x4{0u, 0u, 0u, 0u});
        const int df = (int)(lds_wait_nz((volatile LAS u32*)(smem + L_VOTE)) - rtt);
        hb = df > PTHR ? 1u << (g >> 2) : (df < -PTHR ? 2u >> (g >> 2) : 0u);
      }
    }
    if (tid == 0) *(volatile LAS u32*)(smem + L_ANC) = 0u;
    *reinterpret_cast<u32x4*>(smem + L_P + wave * P_W + lane0 * 16) = u32x4{0u, 0u, 0u, 0u};
    *reinterpret_cast<u32x4*>(smem + (lane0 < 16 ? L_P + wave * P_W + 1024 + lane0 * 16 : L_OM + (wave * OM_S + OM_P + DH) * 4)) =
        u32x4{0u, 0u, 0u, 0u};
    const u16* kb = P.kc + rowbase * DH;
    const u32 vo = (lane0 >> 3) * 384 + 16 * (lane0 & 7);
    const u32 kw = (u32)(smb + L_W2 + wave * 1024 + lane0 * 16), kr = (u32)(smb + L_W2 + wave * 1024 + (lane0 & 15) * 64 + (lane0 >> 4) * 16);
#define KPERM(i)                                                                          \
  asm volatile("ds_write_b128 %[w], v[%[r0]:%[r1]]\n\tds_read_b128 v[%[r0]:%[r1]], %[r]" \
               :                                                                          \
               : [w] "v"(kw), [r] "v"(kr), [r0] "n"(kreg(i)), [r1] "n"(kreg(i) + 3)       \
               : "memory")
#define WPIECE(t)                                                                                                    \
  if ((t) >= WP0 && ((t) - WP0) % WPD == 0 && ((t) - WP0) / WPD < NINO && (isO || ((t) - WP0) / WPD < NINF)) {      \
    pace(tnext, tstep + WPX);                                                                                        \
    dma_piece(ws0, isO ? D : F, smem, L_WB, wave * (isO ? 6 : 5) + ((t) - WP0) / WPD, fresh(lane0));                 \
  }
#define KLOADS(A, B)                                                          \
  _Pragma("unroll") for (int i = A; i < B; ++i) {                             \
    if (i == KSW - 1) tstep = isO ? TS_O : TS_F;                              \
    LOADW(kreg(i), vo, kb + ((i / 3 + rot) & 7) * GST * DH, 128 * (i % 3));    \
    if (i >= WBIG - 1) KPERM(i >= WBIG - 1 ? i - WBIG + 1 : 0);               \
    WPIECE(i)                                                                 \
  }
    static_assert(QI > 2 && BEAT0 >= QI + 3 && CKL >= BEAT0 + 3 && CKG >= CKL && CKQ > CKG && CKQ <= 23, "order");
    KLOADS(0, 2)
    if (wave == 6) {
      asm volatile("s_waitcnt vmcnt(2)" ::: "memory");
      const u32x4 lv = afrag(smem, L_HB + lane0 * 16);
      const u32 xb = *reinterpret_cast<const u32*>(smem + L_HB + 1024 + lane0 * 4);
      float v[10];
#pragma unroll
      for (int k = 0; k < 4; ++k) {
        v[2 * k] = bflo(lv[k]);
        v[2 * k + 1] = bfhi(lv[k]);
      }
      v[8] = bflo(xb);
      v[9] = bfhi(xb);
      float sm = 0.f;
#pragma unroll
      for (int k = 0; k < 10; ++k) sm += v[k];
      const float mean = wave_sum(sm) * (1.0f / D);
      float q = 0.f;
#pragma unroll
      for (int k = 0; k < 10; ++k) q = fmaf(v[k] - mean, v[k] - mean, q);
      const float var = wave_sum(q) * (1.0f / D);
      const float mb = bf16r(mean);
      const float ri = __builtin_amdgcn_rcpf(bf16r(__builtin_amdgcn_sqrtf(bf16r(bf16r(var) + 1e-5f))));
      u32 hp[5];
#pragma unroll
      for (int k = 0; k < 5; ++k) {
        const u32 cc = cvt2(v[2 * k] - mb, v[2 * k + 1] - mb);
        hp[k] = cvt2(bflo(cc) * ri, bfhi(cc) * ri);
      }
      *reinterpret_cast<u32x4*>(smem + L_LN + lane0 * 16) = u32x4{hp[0], hp[1], hp[2], hp[3]};
      *reinterpret_cast<u32*>(smem + L_LN + 1024 + lane0 * 4) = hp[4];
      asm volatile("s_waitcnt lgkmcnt(0)" ::: "memory");
      *(volatile LAS u32*)(smem + L_VOTE + 4) = 1u;
    }
    KLOADS(2, QI)
    int q_l0, q_l1, q_bk, q_ra, q_rb, q_rc;
    u32 q_dst;
    {
      const int lane = fresh(lane0), m = lane & 15, G = lane >> 4;
      q_l0 = fresh(L_WQ + wave * 5120 + wlane(lane, 0));
      q_l1 = fresh(L_WQ + wave * 5120 + wlane(lane, 1));
      q_bk = fresh(4096 - (G >> 1) * 1024);
      const int st = m == 8 ? 64 : 0;
      const int sa = m == 8 ? L_LN + 160 * c + 16 * G : (m == 15 ? L_ONE : L_ZERO);
      q_ra = fresh(sa);
      q_rb = fresh(sa + st);
      q_rc = fresh(G < 2 ? sa + 2 * st : L_TAG);
      q_dst = fresh(WS_GQ1 + (u32)(h * 8 + c) * 2048 + (G & 1) * 1024 + wave * 128 + (lane >> 5) * 64 + m * 4);
    }
    KLOADS(QI, QI + 1)
    f32x4 q_d0, q_d1;
    asm volatile("" : "=v"(q_d0), "=v"(q_d1));
    if (wave < 6) {
      asm volatile("s_waitcnt vmcnt(%0)" ::"n"(QI + 1) : "memory");
      lds_wait((volatile LAS u32*)(smem + L_VOTE + 4), 1u);
      const u32x4 f0 = afrag(smem, q_ra), f1 = afrag(smem, q_rb), f2 = afrag(smem, q_rc);
      q_d0 = q_d1 = f32x4{0.f, 0.f, 0.f, 0.f};
      q_d0 = mfma32(f0, wfrag(smem, q_l0, 0), q_d0);
      q_d1 = mfma32(f0, wfrag(smem, q_l1, 0), q_d1);
      q_d0 = mfma32(f1, wfrag(smem, q_l0, 2048), q_d0);
      q_d1 = mfma32(f1, wfrag(smem, q_l1, 2048), q_d1);
      q_d0 = mfma32(f2, wfrag(smem, q_l0 + q_bk, 0), q_d0);
      q_d1 = mfma32(f2, wfrag(smem, q_l1 + q_bk, 0), q_d1);
    }
    KLOADS(QI + 1, QI + 2)
    if (wave < 6) {
      auto sw = __builtin_amdgcn_permlane32_swap(__float_as_uint(sum4(q_d0)), __float_as_uint(sum4(q_d1)), false, false);
      const f32x4 qd = quad4(__uint_as_float(sw[1]));
      if ((lane0 & 3) == 0) store16<AUXQ>(rs, q_dst, __builtin_bit_cast(u32x4, qd));
    }
    KLOADS(QI + 2, QI + 3)
    att_table<0>(smem, fresh(lane0), wave, gop);
    KLOADS(QI + 3, QI + 4)
    att_table<1>(smem, fresh(lane0), wave, gop);
    KLOADS(QI + 4, BEAT0 + 1)
    asm volatile("s_waitcnt vmcnt(%0)" ::"n"(BEAT0 - QI - 1) : "memory");
    if (wave < 6 && lane0 == 0) lds_add(smem, L_CNT + 4, 1u);
    if (wave == 7) {
      lds_wait(cnt + 1, 6u);
      hb += (__builtin_amdgcn_readfirstlane(CTL[576 + c]) & ~3u) + 4u;
      if (lane0 == 0) __builtin_amdgcn_raw_buffer_store_b32(hb, rs, WS_HB + h * HBS + c * 4, 0, 16);
      hb += 4u;
    }
    KLOADS(BEAT0 + 1, CKL + 1)
    if (wave == 7) ctl_load<false>(rq, hso, (u32)(smb + L_CTL + 2048));
    KLOADS(CKL + 1, CKG + 1)
    if (leader) ctl_load<true>(rq, lho, (u32)(smb + L_CTL + 1024));
    KLOADS(CKG + 1, CKQ + 1)
    if (wave == 7) {
      asm volatile("s_waitcnt vmcnt(%0)" ::"n"(CKQ - CKL) : "memory");
      HS_WAIT((u32)ch == 0xffu)
      if (lane0 == 0) cnt[1] = 7u;
    }
    KLOADS(CKQ + 1, 24)
#define VLOADS(A, B)                                                                           \
  _Pragma("unroll") for (int i = A; i < B; ++i) {                                              \
    LOADW(vreg(i), vo, vb + ((i / 3 + rot) & 7) * GST * DH, 128 * (i % 3));                     \
    KPV(i)                                                                                     \
    WPIECE(24 + i)                                                                             \
  }
#define KPV(i) if (i < WBIG - 1) KPERM(i < WBIG - 1 ? 25 + i - WBIG : 0);
    VLOADS(0, 2)
    if (leader) {
      asm volatile("s_waitcnt vmcnt(%0)" ::"n"(25 - CKG) : "memory");
      const u32x4 xa = afrag(smem, L_CTL + lane0 * 16);
      u32x4 lv;
#pragma unroll 1
      for (;;) {
        asm volatile("" ::: "memory");
        lv = afrag(smem, L_CTL + 1024 + lane0 * 16);
        const bool same = lv[0] == xa[0] || lv[1] == xa[1] || lv[2] == xa[2] || lv[3] == xa[3];
        if (__builtin_amdgcn_ballot_w64(same) == 0ull || tmo == 0u) break;
        --tmo;
        __builtin_amdgcn_s_sleep(4);
        ctl_load<true>(rq, lho, (u32)(smb + L_CTL + 1024));
        asm volatile("s_waitcnt vmcnt(0)" ::: "memory");
      }
      u32 nv = 256u;
#pragma unroll
      for (int e = 0; e < 4; ++e)
        nv += (u32)__builtin_popcountll(__builtin_amdgcn_ballot_w64((lv[e] & 2u) != 0u)) -
              (u32)__builtin_popcountll(__builtin_amdgcn_ballot_w64((lv[e] & 1u) != 0u));
      if (lane0 < 8) __builtin_amdgcn_raw_buffer_store_b32(CTL[640 + lane0] + (nv > 256u ? 2u : 1u), rs, WS_GOF + lane0 * HBS, 0, 16);
    }
    const u32 qo = WS_GQ1 + (u32)(h * 8 + (lane0 >> 3)) * 2048 + wave * 128 + (lane0 & 7) * 16;
    if (wave < 6) {
      lds_wait(cnt + 1, 7u);
      dma_poll<false, AUXQ == 18>(rq, qo, (u32)(smb + L_LAND + wave * 2048), ~0ull, ~0ull);
    }
    VLOADS(2, XB - WBIG)
    if (wave == 7) ctl_load<false>(rq, hso, (u32)(smb + L_CTL + 2048));
    VLOADS(XB - WBIG, WBIG + 2)
    if (wave < 6) {
      q1_finish(smem, fresh(lane0), wave);
      asm volatile("s_waitcnt lgkmcnt(0)" ::: "memory");
      if (lane0 == 0) lds_add(smem, L_CNT, 1u);
    }
#undef KPV
#define KPV(i)
    VLOADS(WBIG + 2, XA)
    if (QKP) tstep >>= 1;
    if (QKP && wave < 4) tnext += (XB - XA) * tstep;
    if (wave >= 4) {
      VLOADS(XA, XB)
      if (QKP) tnext += (XB - XA) * tstep;
      if (wave == 7) {
        HS_WAIT(false)
        if (lane0 == 8) *(volatile LAS u32*)(smem + L_VOTE) = gd >> 1;
      }
    }
  }

#pragma unroll 1
  for (int half = 0;; ++half) {
    {
      float pend, pmax;
      PVAddr pv;
      QKAddr qa;
      const int lane = fresh(lane0);
      const u32 off[1] = {ar + A_GQ + hsub * 16384 + (wave < 6 ? (lane >> 3) * 896 + wave * 128 + (lane & 7) * 16 : (lane & 7) * 896 + 768)};
      f32x4 qv[1];
      asm volatile("" : "=v"(qv[0]));
      if (half == 1) anc = lds_wait_nz((volatile LAS u32*)(smem + L_ANC));
      if (half == 1 && wave < 7) {
        wait_rt(anc + A_Q);
        POLL16(qv[0], off[0], 0);
      }
      int qw;
      att_setup(qa, pv, smem, lane, wave, qw);
      if (half == 0) {
        lds_wait(cnt, 6u);
      } else {
        if (wave < 7) {
          asm volatile("s_waitcnt vmcnt(0)" : "+v"(qv[0])::"memory");
          if (__builtin_expect(__builtin_amdgcn_ballot_w64((and4(__builtin_bit_cast(u32x4, qv[0])) & 1u) == 0u) != 0ull, 0))
            gpoll4<1>(rq, off, qv);
          if (wave < 6) {
            const float t = tsum8(qv[0]);
            *reinterpret_cast<u16*>(smem + qw) = (u16)cvt2(t, t);
          } else {
            const LnS ls = ln_stats(qv[0][3], qv[0][0] + qv[0][1] + qv[0][2]);
            *reinterpret_cast<float*>(smem + L_RI) = ls.ri * SCL;
          }
        }
        if (wave < 4) __builtin_amdgcn_s_setprio(2);
        bar();
      }
      QK_SOFTMAX(qa)
      const int wp = isO ? wave * 6 + NINO : wave * 5 + NINF, wq_ = wp + (isO ? NPRE : NPF);
      u32 wv;
      asm volatile("" : "=v"(wv));
      if (half == 0) {
        const int lv_ = fresh(lane0);
        const u32 vo = (lv_ >> 3) * 384 + 16 * (lv_ & 7);
        if (wave < 4) {
          VLOADS(XA, XB)
        }
        if (QKP) tstep <<= 1;
        VLOADS(XB, LCL)
#define NOLC 0
        if (wave == 7 && !NOLC) {
          const u32 a7 = (u32)(g & 3) * AREA + (((u32)(g >> 2) ^ __builtin_amdgcn_readfirstlane(*(volatile LAS u32*)(smem + L_VOTE))) & 1u) * SIDE +
                         (u32)(j >> 3) * 16384u + (u32)(j & 7) * 1024u;
          const u32x4 zz = __builtin_bit_cast(u32x4, zero4());
          store16(rs, lv_ * 16 + a7 + 2 * 16384, zz);
          store16(rs, lv_ * 16 + a7 + (j < 16 ? 6 : 8) * 16384, zz);
          store16(rs, lv_ * 16 + a7 + 12 * 16384, zz);
        }
        VLOADS(LCL, 24)
        tnext += isO ? -TBACK : TFD;
#pragma unroll 1
        for (int p = 0; p < (isO ? NPRE : NPF); ++p) {
          pace(tnext, isO ? TPV : TPF);
          dma_piece(ws0, isO ? D : F, smem, L_WB, wp + p, fresh(lane0));
        }
        if (NPOST || NQF) wv = fresh(dma_off(isO ? D : F, wq_, fresh(lane0)));
      }
#define DA(CTL) "v_add_f32_dpp %[l], %[l], %[l] " CTL " row_mask:0xf bank_mask:0xf\n\t"
#define PW(W) "s_waitcnt vmcnt(6*" #W "+%[no]) lgkmcnt(" #W ")\n\ts_cbranch_scc0 1f\n\ts_waitcnt vmcnt(6*" #W "+%[nf])\n1:\n\t"
#define PX(o, v, a) SM "%[o" #o "], %[a" #a "], v[160+24*" #a "+8*" #o ":167+24*" #a "+8*" #o "], %[ix]\n\t"
      asm volatile("s_cmp_gt_i32 %[y], 19\n\t"
                   PW(3) PX(0, x, 0) DA("quad_perm:[1,0,3,2]") PX(1, y, 0) PX(2, z, 0)
                   PW(2) PX(0, x, 1) DA("quad_perm:[2,3,0,1]") PX(1, y, 1) PX(2, z, 1)
                   PW(1) PX(0, x, 2) DA("row_half_mirror") PX(1, y, 2) PX(2, z, 2)
                   PW(0) PX(0, x, 3) DA("row_ror:8") PX(1, y, 3) PX(2, z, 3) "s_setprio 0"
                   : [o0] "+v"(pv.O0), [o1] "+v"(pv.O1), [o2] "+v"(pv.O2), [a0] "+v"(pv.A0), [a1] "+v"(pv.A1),
                     [a2] "+v"(pv.A2), [a3] "+v"(pv.A3), [l] "+v"(pend)
                   : [ix] "v"(pv.idx), [no] "n"(NPRE), [nf] "n"(NPF), [y] "s"(j - 32 * half), "{v[160:191]}"(kv3),
                     "{v[192:223]}"(kv4), "{v[224:255]}"(kv5)
                   : "memory", "scc");
      if ((isO ? NPOST : NQF) && half == 0) dma_go(ws0, wv, smem, L_WB + wq_ * 1024);
      asm volatile("ds_write2_b32 %[la], %[l], %[mx] offset1:4\n\ts_mov_b64 exec, %[m]\n\ts_nop 2\n\t"
                   "ds_write_b128 %[a], %[o0]\n\tds_write_b128 %[a], %[o1] offset:256\n\t"
                   "ds_write_b128 %[a], %[o2] offset:512\n\t"
                   "s_mov_b64 exec, -1\n\ts_waitcnt lgkmcnt(0)\n\ts_barrier"
                   :
                   : [m] "s"(0xff00ff0000ff00ffull), [a] "v"(pv.oa + smb), [o0] "v"(pv.O0), [o1] "v"(pv.O1), [o2] "v"(pv.O2),
                     [la] "v"(pv.la + smb), [l] "v"(pend), [mx] "v"(pmax)
                   : "memory");
      tref = now();
      if (half == 0) ar = (u32)(g & 3) * AREA + (((u32)(g >> 2) ^ __builtin_amdgcn_readfirstlane(*(volatile LAS u32*)(smem + L_VOTE))) & 1u) * SIDE;
      if (wave < 4) {
        const float* mp = reinterpret_cast<const float*>(smem + pv.ma);
        float m0 = mp[0], m1 = mp[4 * OM_S];
        f32x4 v = *reinterpret_cast<const f32x4*>(smem + pv.sa), v1 = *reinterpret_cast<const f32x4*>(smem + pv.sa + OM_P * 4);
        f32x4 u = *reinterpret_cast<const f32x4*>(smem + pv.sa + OM_S * 16);
        f32x4 u1 = *reinterpret_cast<const f32x4*>(smem + pv.sa + OM_S * 16 + OM_P * 4);
        asm volatile("" : "+v"(m0), "+v"(m1), "+v"(v), "+v"(v1), "+v"(u), "+v"(u1));
        v += v1;
        u += u1;
        float M = 0.f;
        if (__builtin_expect(__builtin_amdgcn_ballot_w64((__float_as_uint(m0) | __float_as_uint(m1)) != 0u) != 0ull, 0)) {
          M = fmx(m0, m1);
          M = fmx(M, dpp<XOR1>(M));
          M = fmx(M, dpp<XOR2>(M));
          v *= __builtin_amdgcn_exp2f(m0 - M);
          u *= __builtin_amdgcn_exp2f(m1 - M);
        }
        v = sum4x4(v + u);
        u32 p0 = cvt2(v[0], v[1]), p1 = cvt2(v[2], v[3]);
        if (wave == 3) {
          p0 = __float_as_uint(M);
          p1 = __float_as_uint(sum4(v));
        }
        p0 |= 1u;
        p1 |= 1u;
        const u32x4 pk = {p0, p1, dppu<XOR7>(p0), dppu<XOR7>(p1)};
        asm volatile("s_mov_b64 exec, %2\n\tbuffer_store_dwordx4 %0, %1, %3, %4 offen sc1\n\ts_mov_b64 exec, -1"
                     :
                     : "v"(pk), "v"(pv.dst), "s"(wave < 3 ? 0x0101010101010101ull : 0x3ull), "s"(rq), "s"(ar + (u32)half * GO_SZ)
                     : "memory");
      }
    }
    const u32 go = ar + A_GO + (u32)half * GO_SZ;
    const u32 gyw = A_GY + (u32)half * GY_PAIR;
    const u32 gy = gyw + ar;
    const u32 gh = go + A_GH;
    const int land = L_LAND + wave * 2048;
    const u32 landa = (u32)(uintptr_t)(LAS unsigned char*)smem + (u32)land;
    if (isO) {
      {
        const int lane = fresh(lane0);
        const u32 voff = fresh(go + go_slot(wave) + ((lane * 16) ^ ((lane >> 4) * 64)));
        if (half)
          wait_rt(anc + A_O);
        else
          wait_until(tref + 24 * T_O);
        dma_poll<false>(rq, voff, landa, ~0ull, ~0ull);
        const int n = lane & 15, G = lane >> 4;
        const int r0_ = land + (n & 7) * 256 + (n & 3) * 64;
        const int ra = fresh(n < 8 ? r0_ + G * 16 : r0_ ^ 192), rb = fresh((r0_ + G * 16) ^ 64), rc = fresh((r0_ + G * 16) ^ 128);
        const int l0 = fresh(L_WB + wave * 6144 + wlane(lane, 0)), l1 = fresh(L_WB + wave * 6144 + wlane(lane, 1));
        asm volatile("s_waitcnt vmcnt(2)" ::: "memory");
        u32x4 wf[6];
#pragma unroll
        for (int k = 0; k < 3; ++k) {
          wf[2 * k] = wfrag(smem, l0, 2048 * k);
          wf[2 * k + 1] = wfrag(smem, l1, 2048 * k);
        }
        const int rw = fresh(L_RED + wave * 144 + lane * 4);
        const int rr = fresh(L_RED + (lane & 3) * 144 + ((lane >> 2) & 7) * 16);
        const u32 dst = fresh(gyw + gy_rep((lane & 3) + (lane >> 5) * 4) + gy_chunk(4 * j + ((lane >> 3) & 3)) + g * 256);
        f32x4 d0, d1;
        float mc, lc, t1_;
        u32 budget = SPIN_CAP;
#pragma unroll 1
        for (;;) {
          u64 need;
          u32 t;
          asm volatile("s_waitcnt vmcnt(0)\n\t"
                       "ds_read_b128 " F0 ", %[ra]\n\tds_read_b128 " F1 ", %[rb]\n\tds_read_b128 " F2 ", %[rc]\n\t"
                       "s_waitcnt lgkmcnt(2)\n\t"
                       MF "%[d0], " F0 ", %[w0], 0\n\t"
                       "v_add_f32_dpp %[l], v53, v53 quad_perm:[1,0,3,2] row_mask:0xf bank_mask:0xf\n\t"
                       MF "%[d1], " F0 ", %[w1], 0\n\t" AND3("v52", "v53", "v54")
                       "s_waitcnt lgkmcnt(1)\n\t"
                       MF "%[d0], " F1 ", %[w2], %[d0]\n\t"
                       "v_add_f32_dpp %[l], %[l], %[l] quad_perm:[2,3,0,1] row_mask:0xf bank_mask:0xf\n\t" AND3("%[t]", "v55", "v56")
                       MF "%[d1], " F1 ", %[w3], %[d1]\n\t" AND3("%[t]", "v57", "v58")
                       "v_add_f32_dpp %[l], %[l], %[l] row_half_mirror row_mask:0xf bank_mask:0xf\n\t"
                       "s_waitcnt lgkmcnt(0)\n\t"
                       MF "%[d0], " F2 ", %[w4], %[d0]\n\t" AND3("%[t]", "v59", "v60") AND3("%[t]", "v61", "v62")
                       MF "%[d1], " F2 ", %[w5], %[d1]\n\t" AND3("%[t]", "1", "v63")
                       "v_rcp_f32 %[l], %[l]\n\tv_cmp_eq_u32 %[need], 0, %[t]"
                       : [d0] "=&v"(d0), [d1] "=&v"(d1), [t] "=&v"(t), [need] "=&s"(need), [l] "=&v"(lc), "=&{v52}"(mc), "=&{v53}"(t1_)
                       : [ra] "v"(ra), [rb] "v"(rb), [rc] "v"(rc), [w0] "v"(wf[0]), [w1] "v"(wf[1]), [w2] "v"(wf[2]), [w3] "v"(wf[3]),
                         [w4] "v"(wf[4]), [w5] "v"(wf[5])
                       : "memory", "v54", "v55", "v56", "v57", "v58", "v59", "v60", "v61", "v62", "v63");
          if (__builtin_expect(need == 0ull || budget == 0u, 1)) break;
          --budget;
          u64 ea, eb;
          retry_masks<16>(need, lane, ea, eb);
          dma_poll<false>(rq, voff, landa, ea, eb);
        }
        float rl = __uint_as_float(__builtin_amdgcn_readlane(__float_as_uint(lc), 8));
        asm volatile("s_nop 1" : "+v"(d0), "+v"(d1));
        if (__builtin_expect((__builtin_amdgcn_ballot_w64(__float_as_uint(mc) != 1u) & 0xff00ull) != 0ull, 0)) {
          const float fc = __builtin_amdgcn_exp2f(mc - max8(mc));
          const u32 sc = __float_as_uint(fc * __builtin_amdgcn_rcpf(sum8(fc * t1_)));
#pragma unroll
          for (int e = 0; e < 4; ++e) {
            const float se = __uint_as_float(G == 0 ? __builtin_amdgcn_readlane(sc, 8 + e) : __builtin_amdgcn_readlane(sc, 12 + e));
            d0[e] *= se;
            d1[e] *= se;
          }
          rl = 1.0f;
        }
        {
          auto sw = __builtin_amdgcn_permlane16_swap(__float_as_uint(sum4(d0)), __float_as_uint(sum4(d1)), false, false);
          const float r = (__uint_as_float(sw[0]) + __uint_as_float(sw[1])) * rl;
          if (lane < 32) *reinterpret_cast<float*>(smem + rw) = r;
        }
        bar();
        if (wave == 0) {
          const f32x4 y = sum4x4(*reinterpret_cast<const f32x4*>(smem + rr) + *reinterpret_cast<const f32x4*>(smem + rr + 576));
          const u32 p0 = cvt2(y[0], y[1]) | 1u, p1 = cvt2(y[2], y[3]) | 1u;
          const u32x4 pk = {p0, p1, dppu<XOR7>(p0), dppu<XOR7>(p1)};
          asm volatile("s_mov_b64 exec, %2\n\tbuffer_store_dwordx4 %0, %1, %3, 0 offen sc1\n\ts_mov_b64 exec, -1"
                       :
                       : "v"(pk), "v"(dst), "s"(0x0f0f0f0f0f0f0f0full), "s"(rq)
                       : "memory");
        }
      }
      tref = now();
      if (half == 0) {
        if (wave >= 1 && wave < 7) {
          const u16* s0 = karg<56>() + (size_t)(192 * g) * D + 32 * j;
#pragma unroll
          for (int p = 0; p < 2; ++p) dma_piece(s0, D, smem, L_W2, (wave - 1) * 2 + p, fresh(lane0));
          asm volatile("s_waitcnt vmcnt(0)" ::: "memory");
        }
        bar();
        if (wave == 4) {
          wait_until(tref + 24 * T_H);
          stamp_poll(smem, rq, ar + st_line(fresh(lane0) & 7));
        }
        if (wave == 5) {
          wait_until(tref + 24 * T_LAZY);
          x_update<false>(smem, rq, fresh(lane0), gy + gy_row(c), c);
        }
      } else if (g == 0 && wave == 2) {
        const int lane = fresh(lane0);
        const int col = j * 32 + (lane & 7) * 4 + ((lane >> 4) & 1) * 2 + (lane >> 5);
        u32 x0v, dummy;
        u16* const po = karg<64>() + col;
        asm volatile("global_load_ushort %0, %2, off\n\tglobal_load_ushort %1, %3, off\n\ts_waitcnt vmcnt(0)"
                     : "=&v"(x0v), "=&v"(dummy)
                     : "v"(karg<0>() + col), "v"(po)
                     : "memory");
        wait_until(tref + 24 * T_LAZY);
        const u32 o = gy + gy_chunk(4 * j + ((lane >> 1) & 3)) + (lane >> 3) * 256 + (lane & 1) * 8;
        const u32 off[3] = {o - GY_PAIR, o - GY_PAIR + GY_INST, o};
        float y[3];
        col_poll<3>(rq, off, y);
        const float x3 = bf16r(bf16r(bf16r(bflo(x0v) + y[0]) + y[1]) + y[2]);
        const u32 off2[1] = {o + GY_INST};
        wait_rt(anc + A_F);
        float y4[1];
        col_poll<1>(rq, off2, y4);
        const float x4 = x3 + y4[0];
        if ((lane & 8) == 0) asm volatile("global_store_short %0, %1, off" ::"v"(po), "v"(cvt2(x4, x4)) : "memory");
      }
      if (wave >= 6) {
        const int lane = fresh(lane0), wv = wave - 6;
        const int n = lane & 15, G = lane >> 4;
        const int l0 = fresh(L_W2 + wlane(lane, wv));
        u32x4 wf[6];
#pragma unroll
        for (int k = 0; k < 6; ++k) wf[k] = wfrag(smem, l0, 2048 * k);
        const int l48 = lane < 48 ? lane : 0;
        const u32 off[2] = {gh + (u32)l48 * 16, gh + (u32)l48 * 16 + 16384};
        const int aw = fresh(L_ACT + wv * 384 + l48 * 8);
        const int ab = fresh(L_ACT + wv * 384 + 16 * G);
        const u32 dst = fresh(gyw + GY_INST + gy_rep(n & 7) + gy_chunk(4 * j + wv * 2 + (G >> 1)) + g * 256);
        f32x4 v[2];
        if (half)
          wait_rt(anc + A_H);
        else
          wait_until(tref + 24 * T_H);
        gpoll4<2>(rq, off, v);
        {
          f32x4 a, u;
#pragma unroll
          for (int e = 0; e < 4; ++e) {
            a[e] = bfhi(__float_as_uint(v[0][e]));
            u[e] = bfhi(__float_as_uint(v[1][e]));
          }
          const f32x4 hh = a * u;
          *reinterpret_cast<u32x2*>(smem + aw) = u32x2{cvt2(hh[0], hh[1]), cvt2(hh[2], hh[3])};
        }
        u32x4 af[4];
#pragma unroll
        for (int k = 0; k < 4; ++k) af[k] = afrag(smem, ab + 64 * k);
        asm volatile("" : "+v"(af[0]), "+v"(af[1]), "+v"(af[2]), "+v"(af[3]));
        f32x4 d = {0.f, 0.f, 0.f, 0.f};
        d = mfma32(wf[0], af[0], d);
        d = mfma32(wf[1], af[1], d);
        asm volatile("" : "+v"(d)::"memory");
        af[0] = afrag(smem, ab + 256);
        af[1] = afrag(smem, ab + 320);
        d = mfma32(wf[2], af[2], d);
        d = mfma32(wf[3], af[3], d);
        d = mfma32(wf[4], af[0], d);
        d = mfma32(wf[5], af[1], d);
        const u32 p0 = cvt2(d[0], d[1]) | 1u, p1 = cvt2(d[2], d[3]) | 1u;
        auto s0 = __builtin_amdgcn_permlane16_swap(p0, p0, false, false);
        auto s1 = __builtin_amdgcn_permlane16_swap(p1, p1, false, false);
        if ((lane & (half ? 27 : 24)) == 0) store16(rs, dst, u32x4{s0[0], s1[0], s0[1], s1[1]});
      }
    } else {
      const int lane = fresh(lane0);
      const int n = lane & 15, G = lane >> 4;
      if (half == 1) {
        x_update<false>(smem, rq, lane, gy - GY_PAIR + GY_INST + gy_row(wave), wave);
      } else {
#pragma unroll 1
        for (int p = NINF + NPF + NQF; p < 5; ++p) {
          wait_until(tref + 24 * (T_FW + (p - (NINF + NPF + NQF)) * T_FD));
          dma_piece(ws0, F, smem, L_WB, wave * 5 + p, lane);
        }
      }
      asm volatile("s_waitcnt vmcnt(0) lgkmcnt(0)" ::: "memory");
      G80 a;
      g80_load(a, smem, lane, L_WB + wave * 5120, L_HB + 160 * wave, land);
      const u32 voff = fresh(gy + gy_row(wave) + ((lane * 16) ^ ((lane >> 4) * 64)));
      const int rw = fresh(L_RED + n * 144 + wave * 16 + G * 4);
      const int sw_ = fresh(n == 0 ? L_ST + wave * 16 + G * 4 : L_ACT);
      float r0, r1, z;
      if (half)
        wait_rt(anc + A_Y);
      else
        wait_until(tref + 24 * T_YF);
      g80_run<true>(a, smem, rq, lane, voff, landa, r0, r1, z);
      *reinterpret_cast<float*>(smem + rw) = r0;
      *reinterpret_cast<float*>(smem + rw + 16 * 144) = r1;
      *reinterpret_cast<float*>(smem + sw_) = z;
      const int ln = fresh(lane0);
      const int rr = fresh(L_RED + (ln & 31) * 144 + (ln >> 5) * 64), rs_ = fresh(L_ST + 16 * (ln & 7));
      const u32 dst = fresh(gh + fslot + ln * 4);
      bar();
      tref = now();
      if (half == 0 && wave == 1 && j == 20 && ln < 8)
        __builtin_amdgcn_raw_buffer_store_b32((rtnow() << 1) | 1u, rs, gy_rep(ln) + st_line(g), 0, 16);
      if (wave == 0) {
        f32x4 st = *reinterpret_cast<const f32x4*>(smem + rs_);
        f32x4 q0 = *reinterpret_cast<const f32x4*>(smem + rr), q1 = *reinterpret_cast<const f32x4*>(smem + rr + 16);
        f32x4 q2 = *reinterpret_cast<const f32x4*>(smem + rr + 32), q3 = *reinterpret_cast<const f32x4*>(smem + rr + 48);
        asm volatile("" : "+v"(st), "+v"(q0), "+v"(q1), "+v"(q2), "+v"(q3));
        const LnS ls = ln_stats(st[3], st[0] + st[1] + st[2]);
        const f32x4 qs = (q0 + q1) + (q2 + q3);
        float r = fmaf(-ls.mb, qs[3], qs[0] + qs[1] + qs[2]);
        auto sw = __builtin_amdgcn_permlane32_swap(__float_as_uint(r), __float_as_uint(r), false, false);
        r = bf16r((__uint_as_float(sw[0]) + __uint_as_float(sw[1])) * ls.ri);
        if ((j & 1) == 0) r = bf16r(r * __builtin_amdgcn_rcpf(1.0f + __builtin_amdgcn_exp2f(r * -1.4426950408889634f)));
        const f32x4 qd = quad4(r);
        if ((ln & 35) == 0) store_quad(rs, dst, qd);
      }
      if (half == 0) {
        x_update<true>(smem, rq, fresh(lane0), land, wave);
        if (wave == 7) {
          wait_until(tref + 24 * T_AN);
          stamp_poll(smem, rq, ar + st_line(ln & 7));
        }
      }
    }
    if (half == 1) break;
    {
      if (wave < 6) {
        const int lane = fresh(lane0);
        const int n = lane & 15;
        G80 a;
        g80_load(a, smem, lane, L_WQ + wave * 5120, L_HB + 160 * c, land);
        const u32 voff = fresh(gy + GY_INST + gy_row(c) + ((lane * 16) ^ ((lane >> 4) * 64)));
        const float cc = *reinterpret_cast<const float*>(smem + L_CS + wave * 128 + (lane >> 5) * 64 + n * 4);
        const u32 gq = ar + A_GQ + hsub * 16384 + c * 896;
        const u32 dst = fresh(gq + wave * 128 + (lane >> 5) * 64 + n * 4);
        float r0, r1, z;
        bar();
        wait_rt(__builtin_amdgcn_readfirstlane(*(volatile LAS u32*)(smem + L_ANC)) + A_E);
        g80_run<true>(a, smem, rq, lane, voff, landa, r0, r1, z);
        auto sw = __builtin_amdgcn_permlane32_swap(__float_as_uint(r0), __float_as_uint(r1), false, false);
        const float sh = cc * __uint_as_float(__builtin_amdgcn_readlane(__float_as_uint(z), 48));
        float r = __uint_as_float(sw[0]) +
                  __uint_as_float((u32)__builtin_amdgcn_update_dpp((int)sw[1], (int)__float_as_uint(sh), 0xE4, 0xA, 0xf, false));
        auto sx = __builtin_amdgcn_permlane16_swap(__float_as_uint(r), __float_as_uint(r), false, false);
        r = __uint_as_float(sx[0]) + __uint_as_float(sx[1]);
        const f32x4 qd = quad4(r);
        if ((lane & 19) == 0) store_quad(rs, dst, qd);
        if (wave == 5) {
          const u32 zu = __float_as_uint(z);
          const f32x4 zq = {__uint_as_float(__builtin_amdgcn_readlane(zu, 0)), __uint_as_float(__builtin_amdgcn_readlane(zu, 16)),
                            __uint_as_float(__builtin_amdgcn_readlane(zu, 32)), __uint_as_float(__builtin_amdgcn_readlane(zu, 48))};
          if (lane == 0) store_quad(rs, gq + 768, zq);
        }
      } else {
        bar();
      }
    }
  }
  asm volatile(".Lmk_code_end:");
}

}

extern "C" void kernel_launch(void* const* d_in, const int* in_sizes, int n_in, void* d_out, int out_size, void* d_ws,
                              size_t ws_size, hipStream_t stream) {
  Params P;
  P.x0 = (const u16*)d_in[0];
  P.kc = (const u16*)d_in[1];
  P.vc = (const u16*)d_in[2];
  P.wq = (const u16*)d_in[3];
  P.wo = (const u16*)d_in[4];
  P.w1 = (const u16*)d_in[6];
  P.w3 = (const u16*)d_in[7];
  P.w2 = (const u16*)d_in[8];
  P.out = (u16*)d_out;
  P.ws = (u32*)d_ws;
  mega_kernel<<<NWG, NT, 0, stream>>>(P);
}
```

```cpp
#include <hip/hip_runtime.h>
#include <hip/hip_bf16.h>
#include <stdint.h>

namespace {

typedef unsigned int u32;
typedef unsigned short u16;
typedef unsigned long long u64;
using u32x2 = __attribute__((ext_vector_type(2))) unsigned;
using u32x4 = __attribute__((ext_vector_type(4))) unsigned;
using f32x2 = __attribute__((ext_vector_type(2))) float;
using f32x4 = __attribute__((ext_vector_type(4))) float;
using s16x4 = __attribute__((ext_vector_type(4))) short;
using bf16x2 = __attribute__((ext_vector_type(2))) __bf16;
using bf16x8 = __attribute__((ext_vector_type(8))) __bf16;
#define LAS __attribute__((address_space(3)))

constexpr int D = 640, H = 32, DH = 192, WIN = 4096, SK = 32768, F = 1536;
constexpr int NWG = 256, NT = 512;
constexpr u32 SPIN_CAP = 1u << 16;
constexpr float SCL = 0.07216878364870322f * 1.4426950408889634f;

#define T_O 40
#define T_YF 122
#define T_H 120
#define T_AN 35
#define PTHR 60
#define T_LAZY 60

#define A_E 122
#define A_Q (A_E + 84)
#define A_O (A_Q + 146)
#define A_Y (A_O + 96)
#define A_H (A_Y + 94)
#define A_F (A_H + 58)


constexpr u32 SIDE = 8192, AREA = 16 * 16384;
constexpr u32 A_GO = 0;
constexpr u32 A_GQ = 4 * 16384;
constexpr u32 A_GH = A_GQ + 7168;
constexpr u32 A_GY = 8 * 16384;
constexpr u32 GO_SZ = 32768, GY_PAIR = 65536, GY_INST = 32768;
constexpr u32 WS_CLR = 4 * AREA;
constexpr u32 WS_PB = WS_CLR;
#define HBS 4352
constexpr u32 WS_GOF = WS_PB + (128 << 10), WS_HB = WS_GOF + 8 * HBS;
#define Q1S 2048
constexpr u32 WS_GQ1 = WS_GOF + (192 << 10);
constexpr u32 WS_END = WS_GQ1 + 256 * 2048;
static_assert(WS_END <= (2u << 20) && WS_CLR == 256 * 4096, "layout");
__device__ __forceinline__ u32 go_slot(int w) { return (u32)(w >> 2) * 16384u + (u32)(w & 3) * 2048u; }
__device__ __forceinline__ u32 gy_row(int i) { return go_slot(i); }
__device__ __forceinline__ u32 gy_chunk(int u) {
  const int i = (u * 205) >> 11;
  return gy_row(i) + (u32)(u - 10 * i) * 16u;
}
__device__ __forceinline__ u32 st_line(int g) { return A_GH + 768u + (u32)(g >> 1) * 16384u + (u32)(g & 1) * 128u; }
__device__ __forceinline__ u32 gy_rep(int r) { return (u32)(r & 3) * AREA + (u32)(r >> 2) * SIDE; }

constexpr int L_ZERO = 0;
constexpr int L_CNT = 16;
constexpr int L_RI = 28;
constexpr int L_ONE = 32;
constexpr int L_TAG = 48;
constexpr int L_ST = 64;
constexpr int L_VOTE = L_ST;
constexpr int L_ANC = 192;
constexpr int L_Q = 256;
constexpr int L_CS = 640;
constexpr int L_HB = L_CS + 768;
constexpr int L_ACT = L_HB + D * 2;
constexpr int L_RED = L_ACT + 2 * 384;
constexpr int L_P = L_RED + 32 * 144;
constexpr int P_W = 1280, P_U = 592, P_PAR = 288;
constexpr int OM_S = 400, OM_P = 200;
constexpr int L_OM = L_P + 8 * P_W;
constexpr int L_LN = L_OM;
constexpr int L_LAND = (L_OM + 8 * OM_S * 4 + 255) / 256 * 256;
constexpr int L_WQ = L_LAND + 8 * 2048;
constexpr int L_WB = L_WQ + 6 * 5120;
constexpr int L_W2 = L_WB + 768 * 64;
constexpr int L_PF = L_W2 + 192 * 64;
constexpr int L_TAB = L_PF + 256;
constexpr int L_TOTAL = L_TAB + 8 * 2048;
static_assert(L_LAND % 256 == 0 && L_P % 16 == 0 && L_WQ % 16 == 0 && L_HB % 16 == 0 && L_RED % 16 == 0, "alignment");


struct Params {
  const u16* x0;
  const u16* kc;
  const u16* vc;
  const u16* wq;
  const u16* wo;
  const u16* w1;
  const u16* w3;
  const u16* w2;
  u16* out;
  u32* ws;
};

__device__ __forceinline__ u32 cvt2(float lo, float hi) {
  return __builtin_bit_cast(u32, __builtin_convertvector(f32x2{lo, hi}, bf16x2));
}
__device__ __forceinline__ float bflo(u32 w) { return __uint_as_float(w << 16); }
__device__ __forceinline__ float bfhi(u32 w) { return __uint_as_float(w & 0xffff0000u); }
__device__ __forceinline__ float bf16r(float x) { return bflo(cvt2(x, x)); }

template <int CTRL>
__device__ __forceinline__ float dpp(float x) {
  return __builtin_bit_cast(float, __builtin_amdgcn_mov_dpp(__builtin_bit_cast(int, x), CTRL, 0xf, 0xf, true));
}
constexpr int XOR1 = 0xB1, XOR2 = 0x4E, XOR7 = 0x141, ROR8 = 0x128;
__device__ __forceinline__ float fmx(float a, float b) {
  float r;
  asm("v_max_f32 %0, %1, %2" : "=v"(r) : "v"(a), "v"(b));
  return r;
}
__device__ __forceinline__ float xrow16_max(float x) {
  auto s = __builtin_amdgcn_permlane16_swap(__float_as_uint(x), __float_as_uint(x), false, false);
  x = fmx(__uint_as_float(s[0]), __uint_as_float(s[1]));
  auto t = __builtin_amdgcn_permlane32_swap(__float_as_uint(x), __float_as_uint(x), false, false);
  return fmx(__uint_as_float(t[0]), __uint_as_float(t[1]));
}
__device__ __forceinline__ float xrow16_sum(float x) {
  auto s = __builtin_amdgcn_permlane16_swap(__float_as_uint(x), __float_as_uint(x), false, false);
  x = __uint_as_float(s[0]) + __uint_as_float(s[1]);
  auto t = __builtin_amdgcn_permlane32_swap(__float_as_uint(x), __float_as_uint(x), false, false);
  return __uint_as_float(t[0]) + __uint_as_float(t[1]);
}
__device__ __forceinline__ float sum8(float v) {
  v += dpp<XOR1>(v);
  v += dpp<XOR2>(v);
  v += dpp<XOR7>(v);
  return v;
}
__device__ __forceinline__ float max8(float v) {
  asm("s_nop 1\n\tv_max_f32_dpp %0, %0, %0 quad_perm:[1,0,3,2] row_mask:0xf bank_mask:0xf\n\t"
      "s_nop 1\n\tv_max_f32_dpp %0, %0, %0 quad_perm:[2,3,0,1] row_mask:0xf bank_mask:0xf\n\t"
      "s_nop 1\n\tv_max_f32_dpp %0, %0, %0 row_half_mirror row_mask:0xf bank_mask:0xf"
      : "+v"(v));
  return v;
}
__device__ __forceinline__ float max_ror8(float v) {
  asm("s_nop 1\n\tv_max_f32_dpp %0, %0, %0 row_ror:8 row_mask:0xf bank_mask:0xf" : "+v"(v));
  return v;
}
__device__ __forceinline__ f32x4 sum4x4(f32x4 v) {
  float a = v[0], b = v[1], c = v[2], d = v[3];
#define S4(CTL)                                               \
  "v_add_f32_dpp %0, %0, %0 " CTL " row_mask:0xf bank_mask:0xf\n\t" \
  "v_add_f32_dpp %1, %1, %1 " CTL " row_mask:0xf bank_mask:0xf\n\t" \
  "v_add_f32_dpp %2, %2, %2 " CTL " row_mask:0xf bank_mask:0xf\n\t" \
  "v_add_f32_dpp %3, %3, %3 " CTL " row_mask:0xf bank_mask:0xf\n\t"
  asm("s_nop 1\n\t" S4("quad_perm:[1,0,3,2]") S4("quad_perm:[2,3,0,1]") : "+v"(a), "+v"(b), "+v"(c), "+v"(d));
#undef S4
  return f32x4{a, b, c, d};
}
__device__ __forceinline__ float wave_sum(float v) {
  v = sum8(v);
  v += dpp<ROR8>(v);
  return xrow16_sum(v);
}
__device__ __forceinline__ float tsum8(f32x4 a) {
  auto s0 = __builtin_amdgcn_permlane32_swap(__float_as_uint(a[0]), __float_as_uint(a[1]), false, false);
  auto s1 = __builtin_amdgcn_permlane32_swap(__float_as_uint(a[2]), __float_as_uint(a[3]), false, false);
  const float r0 = __uint_as_float(s0[0]) + __uint_as_float(s0[1]);
  const float r1 = __uint_as_float(s1[0]) + __uint_as_float(s1[1]);
  auto t = __builtin_amdgcn_permlane16_swap(__float_as_uint(r0), __float_as_uint(r1), false, false);
  float r = __uint_as_float(t[0]) + __uint_as_float(t[1]);
  r += dpp<ROR8>(r);
  return r;
}

__device__ __forceinline__ u32 tagv(float v) { return __float_as_uint(v) | 1u; }
template <int AUX = 16>
__device__ __forceinline__ void store_quad(__amdgpu_buffer_rsrc_t rs, u32 off, f32x4 v) {
  const u32x4 s = {tagv(v[0]), tagv(v[1]), tagv(v[2]), tagv(v[3])};
  __builtin_amdgcn_raw_buffer_store_b128(s, rs, off, 0, AUX);
}
template <int N>
__device__ __forceinline__ void gpoll4(u32x4 rs, const u32 (&off)[N], f32x4 (&v)[N]) {
  bool need = true;
  u32 budget = SPIN_CAP;
#pragma unroll
  for (int k = 0; k < N; ++k) asm volatile("" : "=v"(v[k]));
#pragma unroll 1
  for (;;) {
    if (need) {
#pragma unroll
      for (int k = 0; k < N; ++k)
        asm volatile("buffer_load_dwordx4 %0, %1, %2, 0 offen sc1" : "+v"(v[k]) : "v"(off[k]), "s"(rs) : "memory");
    }
    asm volatile("s_waitcnt vmcnt(0)" ::: "memory");
    u32 a = 1u;
#pragma unroll
    for (int k = 0; k < N; ++k) {
      asm volatile("" : "+v"(v[k]));
      a &= __float_as_uint(v[k][0]) & __float_as_uint(v[k][1]) & __float_as_uint(v[k][2]) & __float_as_uint(v[k][3]);
    }
    need = a == 0u;
    if (__builtin_amdgcn_ballot_w64(need) == 0ull || budget == 0u) break;
    --budget;
    __builtin_amdgcn_s_sleep(1);
  }
}
__device__ __forceinline__ u32 now() { return (u32)__builtin_amdgcn_s_memtime(); }
__device__ __forceinline__ void wait_until(u32 t) {
#pragma unroll 1
  while ((int)(now() - t) < -160) __builtin_amdgcn_s_sleep(1);
#pragma unroll 1
  while ((int)(now() - t) < 0) {
  }
}
__device__ __forceinline__ u32 rtnow() { return (u32)__builtin_amdgcn_s_memrealtime(); }
__device__ __forceinline__ void wait_rt(u32 t) {
  u32 spins = 0;
#pragma unroll 1
  while ((int)(rtnow() - t) < -8 && ++spins < 400u) __builtin_amdgcn_s_sleep(1);
#pragma unroll 1
  while ((int)(rtnow() - t) < 0 && ++spins < 440u) {
  }
}
template <int BITS>
__device__ __forceinline__ u32 youngest(u32 t);
__device__ __forceinline__ void lds_wait(volatile LAS u32* p, u32 want) {
  u32 spins = 0;
#pragma unroll 1
  while (__builtin_amdgcn_readfirstlane(*p) != want) {
    if (++spins > (SPIN_CAP << 6)) break;
    __builtin_amdgcn_s_sleep(1);
  }
  asm volatile("" ::: "memory");
}
__device__ __forceinline__ u32 lds_wait_nz(volatile LAS u32* p) {
  u32 spins = 0, v;
#pragma unroll 1
  while ((v = __builtin_amdgcn_readfirstlane(*p)) == 0u) {
    if (++spins > (SPIN_CAP << 6)) break;
    __builtin_amdgcn_s_sleep(1);
  }
  asm volatile("" ::: "memory");
  return v;
}
template <int BITS>
__device__ __forceinline__ u32 youngest(u32 t) {
  const u32 nw = rtnow();
  const float age = (float)((nw - t) & ((1u << BITS) - 1u));
  return nw - (u32)__builtin_amdgcn_readfirstlane((int)-xrow16_max(max_ror8(max8(-age))));
}
__device__ __forceinline__ void stamp_poll(unsigned char* smem, u32x4 rq, u32 so) {
  u32 sv, budget = SPIN_CAP;
#pragma unroll 1
  for (;;) {
    asm volatile("buffer_load_dword %0, %1, %2, 0 offen sc1\n\ts_waitcnt vmcnt(0)" : "=&v"(sv) : "v"(so), "s"(rq) : "memory");
    if (__builtin_amdgcn_ballot_w64((sv & 1u) == 0u) == 0ull || budget == 0u) break;
    --budget;
    __builtin_amdgcn_s_sleep(1);
  }
  *(volatile LAS u32*)(smem + L_ANC) = youngest<24>(sv >> 1) | 1u;
}
__device__ __forceinline__ void lds_add(unsigned char* smem, int off, u32 v) {
  __hip_atomic_fetch_add((LAS u32*)(smem + off), v, __ATOMIC_RELAXED, __HIP_MEMORY_SCOPE_WORKGROUP);
}

__device__ __forceinline__ int fresh(int t) {
  asm volatile("" : "+v"(t));
  return t;
}
__device__ __forceinline__ f32x4 zero4() {
  f32x2 a, b;
  asm volatile("v_mov_b64 %0, 0\n\tv_mov_b64 %1, 0" : "=v"(a), "=v"(b));
  return f32x4{a[0], a[1], b[0], b[1]};
}
__device__ __forceinline__ void bar() { asm volatile("s_waitcnt lgkmcnt(0)\n\ts_barrier" ::: "memory"); }

__device__ __forceinline__ u32x2 trread(unsigned char* smem, int off) {
  return __builtin_bit_cast(u32x2, __builtin_amdgcn_ds_read_tr16_b64_v4i16((LAS s16x4*)(smem + off)));
}
__device__ __forceinline__ u32x4 wfrag(unsigned char* smem, int lb, int off) {
  const u32x2 a = trread(smem, lb + off), b = trread(smem, lb + off + 256);
  return u32x4{a[0], a[1], b[0], b[1]};
}
__device__ __forceinline__ int wlane(int lane, int cb) {
  const int i = lane & 15, G = lane >> 4;
  return (8 * G + (i >> 2)) * 64 + (i & 3) * 8 + ((cb ^ (G & 1)) * 32);
}
__device__ __forceinline__ u32x4 afrag(unsigned char* smem, int a) { return *reinterpret_cast<const u32x4*>(smem + a); }
__device__ __forceinline__ f32x4 mfma32(u32x4 w, u32x4 a, f32x4 c) {
  return __builtin_amdgcn_mfma_f32_16x16x32_bf16(__builtin_bit_cast(bf16x8, w), __builtin_bit_cast(bf16x8, a), c, 0, 0, 0);
}

__device__ __forceinline__ void dma_piece(const u16* src, int ld, unsigned char* smem, int ldsoff, int p, int lane) {
  const int row = 16 * p + (lane >> 2), part = lane & 3;
  const int cb = (part >> 1) ^ (lane >> 5);
  const u32 voff = (u32)(row * ld + cb * 16 + (part & 1) * 8) * 2u;
  const u32 dst = (u32)(uintptr_t)(LAS unsigned char*)smem + (u32)(ldsoff + p * 1024);
  u32 keep;
  asm volatile("s_mov_b32 %0, m0\n\ts_mov_b32 m0, %2\n\ts_nop 2\n\tglobal_load_lds_dwordx4 %1, %3 nt\n\ts_mov_b32 m0, %0"
               : "=&s"(keep)
               : "v"(voff), "s"(dst), "s"(src)
               : "memory");
}

template <int CTRL>
__device__ __forceinline__ u32 dppu(u32 x) {
  return (u32)__builtin_amdgcn_mov_dpp((int)x, CTRL, 0xf, 0xf, true);
}
__device__ __forceinline__ float sum4(f32x4 d) { return (d[0] + d[1]) + (d[2] + d[3]); }
__device__ __forceinline__ u32 and4(u32x4 v) { return v[0] & v[1] & v[2] & v[3]; }
template <int AUX = 16>
__device__ __forceinline__ void store16(__amdgpu_buffer_rsrc_t rs, u32 off, u32x4 v) {
  __builtin_amdgcn_raw_buffer_store_b128(v, rs, off, 0, AUX);
}
#define POLL16(dst, voff, imm) \
  asm volatile("buffer_load_dwordx4 %0, %1, %2, 0 offen offset:%3 sc1" : "+v"(dst) : "v"(voff), "s"(rq), "n"(imm) : "memory")

struct LnS {
  float mb, ri;
};
__device__ __forceinline__ LnS ln_stats(float s1, float s2) {
#define S2(CTL)                                                     \
  "v_add_f32_dpp %0, %0, %0 " CTL " row_mask:0xf bank_mask:0xf\n\t" \
  "v_add_f32_dpp %1, %1, %1 " CTL " row_mask:0xf bank_mask:0xf\n\ts_nop 0\n\t"
  asm("s_nop 1\n\t" S2("quad_perm:[1,0,3,2]") S2("quad_perm:[2,3,0,1]") S2("row_half_mirror") : "+v"(s1), "+v"(s2));
#undef S2
  const float dm = s1 * (1.0f / D);
  const u32 mv = cvt2(dm, __builtin_fmaxf(fmaf(-dm, dm, s2 * (1.0f / D)), 0.f));
  LnS r;
  r.mb = bflo(mv);
  r.ri = __builtin_amdgcn_rcpf(bf16r(__builtin_amdgcn_sqrtf(bf16r(bfhi(mv) + 1e-5f))));
  return r;
}

struct G80 {
  u32x4 w[6];
  int ra, rb, rc;
};
__device__ __forceinline__ void g80_load(G80& a, unsigned char* smem, int lane, int wb, int xs, int land) {
  const int m = lane & 15, G = lane >> 4;
  const int l0 = wb + wlane(lane, 0), l1 = wb + wlane(lane, 1), bk = 4096 - (G >> 1) * 1024;
  a.w[0] = wfrag(smem, l0, 0);
  a.w[1] = wfrag(smem, l1, 0);
  a.w[2] = wfrag(smem, l0, 2048);
  a.w[3] = wfrag(smem, l1, 2048);
  a.w[4] = wfrag(smem, l0 + bk, 0);
  a.w[5] = wfrag(smem, l1 + bk, 0);
  const bool vec = m == 8 || (m < 8 && land >= 0);
  const int st = vec ? 64 : 0;
  const int sa = vec ? (m == 8 ? xs : land + 256 * m) + 16 * G : (m == 15 ? L_ONE : L_ZERO);
  const int sw = (vec && m < 8) ? (m & 3) * 64 : 0;
  a.ra = fresh(sa ^ sw);
  a.rb = fresh((sa + st) ^ sw);
  a.rc = fresh(G < 2 ? (sa + 2 * st) ^ sw : L_TAG);
}
template <bool WAIT = true>
__device__ __forceinline__ void dma_poll(u32x4 rq, u32 voff, u32 land, u64 ea, u64 eb) {
  u32 keep;
  asm volatile("s_mov_b32 %0, m0\n\ts_mov_b32 m0, %3\n\ts_mov_b64 exec, %4\n\t"
               "buffer_load_dwordx4 %1, %2, 0 offen sc1 lds\n\t"
               "s_mov_b64 exec, %5\n\t"
               "buffer_load_dwordx4 %1, %2, 0 offen offset:1024 sc1 lds\n\t"
               "s_mov_b64 exec, -1\n\ts_mov_b32 m0, %0"
               : "=&s"(keep)
               : "v"(voff), "s"(rq), "s"(land), "s"(ea), "s"(eb)
               : "memory");
  if (WAIT) asm volatile("s_waitcnt vmcnt(0)" ::: "memory");
}
template <bool X4, bool NT = false>
__device__ __forceinline__ void ctl_load(u32x4 rq, u32 voff, u32 lds) {
  u32 keep;
  if (NT)
    asm volatile("s_mov_b32 %0, m0\n\ts_mov_b32 m0, %3\n\ts_nop 2\n\tbuffer_load_dwordx4 %1, %2, 0 offen sc1 nt lds\n\ts_mov_b32 m0, %0"
                 : "=&s"(keep) : "v"(voff), "s"(rq), "s"(lds) : "memory");
  else if (X4)
    asm volatile("s_mov_b32 %0, m0\n\ts_mov_b32 m0, %3\n\ts_nop 2\n\tbuffer_load_dwordx4 %1, %2, 0 offen sc1 lds\n\ts_mov_b32 m0, %0"
                 : "=&s"(keep) : "v"(voff), "s"(rq), "s"(lds) : "memory");
  else
    asm volatile("s_mov_b32 %0, m0\n\ts_mov_b32 m0, %3\n\ts_nop 2\n\tbuffer_load_dword %1, %2, 0 offen sc1 lds\n\ts_mov_b32 m0, %0"
                 : "=&s"(keep) : "v"(voff), "s"(rq), "s"(lds) : "memory");
}
template <int UN>
__device__ __forceinline__ void retry_masks(u64 need, int lane, u64& ea, u64& eb) {
  u32 m = (u32)need | (u32)(need >> 32);
  m |= m >> 16;
  m = (m | (m >> 8)) & 0xffu;
  const int ln = fresh(lane);
  ea = __builtin_amdgcn_ballot_w64(((m >> (ln / UN)) & 1u) != 0u);
  eb = __builtin_amdgcn_ballot_w64(((m >> ((ln + 64) / UN)) & 1u) != 0u);
}
#define MF "v_mfma_f32_16x16x32_bf16 "
#define F0 "v[52:55]"
#define F1 "v[56:59]"
#define F2 "v[60:63]"
#define FCLOB "v52", "v53", "v54", "v55", "v56", "v57", "v58", "v59", "v60", "v61", "v62", "v63"
#define AND3(a, b, c) "v_bitop3_b32 %[t], " a ", " b ", " c " bitop3:0x80\n\t"
template <bool POLL>
__device__ __forceinline__ void g80_run(G80& a, unsigned char* smem, u32x4 rq, int lane, u32 voff, u32 land, float& r0,
                                        float& r1, float& z) {
  f32x4 d0, d1;
  if (POLL) {
    f32x4 d2;
    constexpr u64 USED = 0xff30ff0330ff03ffull;
    u64 ea = USED, eb = USED;
    u32 budget = SPIN_CAP;
#pragma unroll 1
    for (;;) {
      dma_poll(rq, voff, land, ea, eb);
      u64 need;
      u32 t;
      asm volatile("ds_read_b128 " F0 ", %[ra]\n\tds_read_b128 " F1 ", %[rb]\n\tds_read_b128 " F2 ", %[rc]\n\t"
                   "s_waitcnt lgkmcnt(2)\n\t"
                   MF "%[d2], " F0 ", " F0 ", 0\n\t" AND3("v52", "v53", "v54")
                   MF "%[d0], " F0 ", %[w0], 0\n\t" MF "%[d1], " F0 ", %[w1], 0\n\t"
                   "s_waitcnt lgkmcnt(1)\n\t"
                   MF "%[d2], " F1 ", " F1 ", %[d2]\n\t" AND3("%[t]", "v55", "v56")
                   MF "%[d0], " F1 ", %[w2], %[d0]\n\t" AND3("%[t]", "v57", "v58")
                   MF "%[d1], " F1 ", %[w3], %[d1]\n\t"
                   "s_waitcnt lgkmcnt(0)\n\t"
                   MF "%[d2], " F2 ", " F2 ", %[d2]\n\t" AND3("%[t]", "v59", "v60") AND3("%[t]", "v61", "v62")
                   MF "%[d0], " F2 ", %[w4], %[d0]\n\t" AND3("%[t]", "1", "v63")
                   "v_cmp_eq_u32 %[need], 0, %[t]\n\t"
                   MF "%[d1], " F2 ", %[w5], %[d1]"
                   : [d0] "=&v"(d0), [d1] "=&v"(d1), [d2] "=&v"(d2), [t] "=&v"(t), [need] "=&s"(need)
                   : [ra] "v"(a.ra), [rb] "v"(a.rb), [rc] "v"(a.rc), [w0] "v"(a.w[0]), [w1] "v"(a.w[1]), [w2] "v"(a.w[2]),
                     [w3] "v"(a.w[3]), [w4] "v"(a.w[4]), [w5] "v"(a.w[5])
                   : "memory", FCLOB);
      need &= 0x00ff00ff00ff00ffull;
      if (need == 0ull || budget == 0u) break;
      --budget;
      retry_masks<16>(need, lane, ea, eb);
      ea &= USED;
      eb &= USED;
    }
    asm volatile("s_nop 1" : "+v"(d2));
    z = sum4(d2);
    asm volatile("s_nop 1" : "+v"(z), "+v"(d0), "+v"(d1));
    z = (lane & 15) == 15 ? 0.f : z;
    z = sum8(z);
    z += dpp<ROR8>(z);
  } else {
    const u32x4 f0 = afrag(smem, a.ra), f1 = afrag(smem, a.rb), f2 = afrag(smem, a.rc);
    d0 = d1 = f32x4{0.f, 0.f, 0.f, 0.f};
    d0 = mfma32(f0, a.w[0], d0);
    d1 = mfma32(f0, a.w[1], d1);
    d0 = mfma32(f1, a.w[2], d0);
    d1 = mfma32(f1, a.w[3], d1);
    d0 = mfma32(f2, a.w[4], d0);
    d1 = mfma32(f2, a.w[5], d1);
  }
  r0 = sum4(d0);
  r1 = sum4(d1);
}
__device__ __forceinline__ f32x4 quad4(float r) { return f32x4{r, dpp<0x55>(r), dpp<0xAA>(r), dpp<0xFF>(r)}; }

__device__ __forceinline__ int q_addr(int d) {
  const int w = d >> 1;
  return L_Q + ((w & 1) * 48 + (w >> 1)) * 4 + (d & 1) * 2;
}

__device__ __forceinline__ void q1_finish(unsigned char* smem, int lane, int wave) {
  const int e = wave * 32 + (lane & 7) * 4 + ((lane >> 4) & 1) * 2 + (lane >> 5);
  asm volatile("" ::: "memory");
  const u32x4 v = afrag(smem, L_LAND + wave * 2048 + lane * 16);
  const float t = tsum8(f32x4{bflo(v[0]), bflo(v[1]), bflo(v[2]), bflo(v[3])});
  *reinterpret_cast<u16*>(smem + q_addr(e)) = (u16)cvt2(t, t);
  *reinterpret_cast<float*>(smem + L_CS + e * 4) = tsum8(f32x4{bfhi(v[0]), bfhi(v[1]), bfhi(v[2]), bfhi(v[3])}) * (-1.0f / D);
}
template <bool LDS>
__device__ __forceinline__ void x_update(unsigned char* smem, u32x4 rq, int lane, u32 src, int i) {
  const int l = lane < 40 ? lane : 0;
  const u32 off = src + 4 * l;
  u32 v[8];
  if (LDS) {
#pragma unroll
    for (int k = 0; k < 8; ++k) v[k] = *reinterpret_cast<const u32*>(smem + (off ^ ((k & 3) * 64)) + 256 * k);
  } else {
    u32 budget = SPIN_CAP;
#pragma unroll 1
    for (;;) {
#pragma unroll
      for (int k = 0; k < 8; ++k)
        asm volatile("buffer_load_dword %0, %1, %2, 0 offen offset:%3 sc1" : "=v"(v[k]) : "v"(off), "s"(rq), "n"(256 * k) : "memory");
      asm volatile("s_waitcnt vmcnt(0)"
                   : "+v"(v[0]), "+v"(v[1]), "+v"(v[2]), "+v"(v[3]), "+v"(v[4]), "+v"(v[5]), "+v"(v[6]), "+v"(v[7])::"memory");
      const u32 a = v[0] & v[1] & v[2] & v[3] & v[4] & v[5] & v[6] & v[7] & 1u;
      if (__builtin_amdgcn_ballot_w64(a == 0u) == 0ull || budget == 0u) break;
      --budget;
      __builtin_amdgcn_s_sleep(2);
    }
  }
  float lo = 0.f, hi = 0.f;
#pragma unroll
  for (int k = 0; k < 8; ++k) {
    lo += bflo(v[k]);
    hi += bfhi(v[k]);
  }
  u32* px = reinterpret_cast<u32*>(smem + L_HB + 160 * i + 4 * l);
  const u32 xo = *px, y = cvt2(lo, hi);
  if (lane < 40) *px = cvt2(bflo(xo) + bflo(y), bfhi(xo) + bfhi(y));
}
template <int N>
__device__ __forceinline__ void col_poll(u32x4 rq, const u32 (&off)[N], float (&y)[N]) {
  u32x2 v[N];
  u32 budget = SPIN_CAP;
#pragma unroll 1
  for (;;) {
    u32 a = 1u;
#pragma unroll
    for (int k = 0; k < N; ++k)
      asm volatile("buffer_load_dwordx2 %0, %1, %2, 0 offen sc1" : "=v"(v[k]) : "v"(off[k]), "s"(rq) : "memory");
    asm volatile("s_waitcnt vmcnt(0)" ::: "memory");
#pragma unroll
    for (int k = 0; k < N; ++k) {
      asm volatile("" : "+v"(v[k]));
      a &= v[k][0] & v[k][1];
    }
    if (__builtin_amdgcn_ballot_w64(a == 0u) == 0ull || budget == 0u) break;
    --budget;
  }
#pragma unroll
  for (int k = 0; k < N; ++k) y[k] = bf16r(tsum8(f32x4{bflo(v[k][0]), bfhi(v[k][0]), bflo(v[k][1]), bfhi(v[k][1])}));
}

#define PFB 16
#define AUXQ 18
#define LCL 24
#define QKP 2
#define CKQ 23
#define BEAT0 10
#define CKL 15
#define CKG 16
#define WBIG 10
#define TSTEP 236
#define QI 5
#define NINO 4
#define NINF 1
#define NPRE 2
#define NPF 2
static_assert(NINO + NPRE == 6 && NINF + NPF <= 5 && NPF <= NPRE, "counts");
#define SKW 0
#define KS2 5
#define TDEC 0
#define ROTW 0
#define TPF 220
#define TFD 0
#define T_FW 30
#define T_FD 12
#define WP0 17
#define WPD 2
#define WSP 1
#define WPX 0
static_assert(WP0 + (NINO - 1) * WPD < 24 + 13, "pieces before the scores");
#define TPV 220
#define XA 13
#define XB 19
#define TSV 230
#define KSW 12
#define TS_O (TSV * (20 * (48 - KSW + NINO) + 12 * (48 - KSW + NINF)) / (32 * (48 - KSW + NINO)))
#define TS_F (TSV * (20 * (48 - KSW + NINO) + 12 * (48 - KSW + NINF)) / (32 * (48 - KSW + NINF)))
#define TSQ 216
#define ROW0 ((wave * 8 + c) * 8)
#define GST 512
#define ROT h
static_assert(XB - WBIG <= WBIG + 2 && WBIG + 2 <= XA, "window");
#define PACE_ASM                                                                                                     \
  "1:\n\ts_memrealtime s[98:99]\n\ts_waitcnt lgkmcnt(0)\n\ts_lshl_b32 s98, s98, 3\n\ts_sub_i32 s98, s98, %[tn]\n\t"  \
  "s_cmp_lt_i32 s98, 0\n\ts_cbranch_scc0 2f\n\ts_sleep 1\n\ts_branch 1b\n2:\n\ts_add_i32 %[tn], %[tn], %[ts]\n\t"
__device__ __forceinline__ void pace(u32& tnext, u32 tstep) {
  asm volatile(PACE_ASM : [tn] "+s"(tnext) : [ts] "s"(tstep) : "scc", "s98", "s99");
}
constexpr int kreg(int i) { return i % 3 < 2 ? 64 + 8 * (i / 3) + 4 * (i % 3) : 128 + 4 * (i / 3); }
constexpr int vreg(int i) { return 160 + 24 * (i / 6) + 8 * (i % 3) + 4 * ((i / 3) & 1); }
#define TBACK 0
#define LOADW(reg, voff, sbase, imm)                                                                                \
  asm volatile(PACE_ASM "global_load_dwordx4 v[%[r0]:%[r1]], %[p], %[sb] offset:%[im] nt\n\ts_waitcnt vmcnt(%[w])"  \
               : [tn] "+s"(tnext)                                                                                   \
               : [r0] "n"(reg), [r1] "n"((reg) + 3), [p] "v"(voff), [sb] "s"(sbase), [im] "n"(imm), [ts] "s"(tstep), \
                 [w] "n"(WBIG - 1)                                                                                  \
               : "scc", "s98", "s99")

#define HS_WAIT(COND)                                                                                \
  _Pragma("unroll 1") for (;;) {                                                                     \
    gd = CTL[512 + lane0] - CTL[576 + lane0];                                                        \
    const u64 ch = __builtin_amdgcn_ballot_w64(gd != 0u) & 0x1ffull;                                 \
    if (ch > 0xffull || (COND) || tmo == 0u) break;                                                  \
    if ((tmo & 3u) == 0u && lane0 == 0)                                                              \
      __builtin_amdgcn_raw_buffer_store_b32(hb, rs, WS_HB + h * HBS + c * 4, 0, 16);                 \
    hb += 4u;                                                                                        \
    --tmo;                                                                                           \
    __builtin_amdgcn_s_sleep(3);                                                                     \
    ctl_load<false>(rq, hso, (u32)(smb + L_CTL + 2048));                                             \
    asm volatile("s_waitcnt vmcnt(0)" ::: "memory");                                                 \
  }

struct QKAddr {
  int qad[6], idxq, wa;
  f32x4 D0, D1;
};
struct PVAddr {
  int ra, idx, oa, sa, la, ma;
  u32 dst;
  f32x4 O0, O1, O2;
  u32x4 A0, A1, A2, A3;
};
template <int K>
__device__ __forceinline__ void att_table(unsigned char* smem, int lane, int wave, u32 go) {
  const int n = lane & 15, G = lane >> 4;
  const int seg = n & 1, pr = (n >> 1) & 1, tq = n >> 2, xy = G >> 1;
  const int qa = L_Q + (pr * 48 + seg * 8 + (G & 1) * 4) * 4;
  u32 t[16];
#pragma unroll
  for (int k = 0; k < 4; ++k) t[k] = tq == k ? qa + xy * 64 : L_ZERO;
#pragma unroll
  for (int k = 0; k < 2; ++k) t[4 + k] = tq == 2 * k + xy ? qa + 128 : L_ZERO;
  t[6] = 0x4444 + pr * 0xAAAA;
  t[7] = L_P + wave * P_W + ((n >> 1) & 1) * P_PAR + ((((G & 1) * 2 + (n >> 3)) * 4 + (n & 1) * 2 + (G >> 1)) * 16) + ((n >> 2) & 1) * 8;
  t[8] = L_P + wave * P_W + ((n >> 2) & 1) * P_U + (n >> 3) * P_PAR + G * 64;
  t[9] = (n & 3) * 0x5555;
  t[10] = L_OM + (wave * OM_S + (n >> 3) * OM_P + (n & 7) * 4 + (G & 1) * 32) * 4;
  t[11] = L_OM + (wave * OM_S + DH + G) * 4;
  t[12] = L_OM + ((lane & 3) * OM_S + (wave & 3) * 64 + (lane >> 3) * 4 + (lane & 4) * 8) * 4;
  t[13] = L_OM + ((lane & 3) * OM_S + DH + 4) * 4;
  const int d0 = wave * 64 + (lane >> 3) * 8;
  t[14] = (go + (wave < 3 ? (d0 >= 96 ? 2048 - 192 : 0) + d0 * 2 : lane * 2048 + 192)) & 0xffffu;
  t[15] = q_addr(wave * 32 + (lane & 7) * 4 + ((lane >> 4) & 1) * 2 + (lane >> 5));
  *reinterpret_cast<u32x4*>(smem + L_TAB + wave * 2048 + K * 1024 + lane * 16) =
      u32x4{t[8 * K] | (t[8 * K + 4] << 16), t[8 * K + 1] | (t[8 * K + 5] << 16), t[8 * K + 2] | (t[8 * K + 6] << 16),
            t[8 * K + 3] | (t[8 * K + 7] << 16)};
}
__device__ __forceinline__ void att_setup(QKAddr& r, PVAddr& v, unsigned char* smem, int lane, int wave, int& qw) {
  const u32x4 a = afrag(smem, L_TAB + wave * 2048 + lane * 16), b = afrag(smem, L_TAB + wave * 2048 + 1024 + lane * 16);
  r.D0 = zero4();
  r.D1 = zero4();
  qw = fresh(b[3] >> 16);
#pragma unroll
  for (int k = 0; k < 4; ++k) r.qad[k] = fresh(a[k] & 0xffffu);
  r.qad[4] = fresh(a[0] >> 16);
  r.qad[5] = fresh(a[1] >> 16);
  r.idxq = fresh(a[2] >> 16);
  r.wa = fresh(a[3] >> 16);
  v.ra = fresh(b[0] & 0xffffu);
  v.idx = fresh(b[1] & 0xffffu);
  v.oa = fresh(b[2] & 0xffffu);
  v.la = fresh(b[3] & 0xffffu);
  v.sa = fresh(b[0] >> 16);
  v.ma = fresh(b[1] >> 16);
  v.dst = fresh(b[2] >> 16);
  asm volatile("" : "+v"(r.D0), "+v"(r.D1));
}
#define SM "v_smfmac_f32_16x16x64_bf16 "
#define P_WRITE_READ(QA)                                                                                              \
  asm volatile("ds_write2_b32 %[wa], %[p], %[p] offset1:%[o1]\n\t"                                                    \
               "ds_read_b128 %[a0], %[ra]\n\tds_read_b128 %[a1], %[ra] offset:16\n\t"                                 \
               "ds_read_b128 %[a2], %[ra] offset:32\n\tds_read_b128 %[a3], %[ra] offset:48"                           \
               : [a0] "=&v"(pv.A0), [a1] "=&v"(pv.A1), [a2] "=&v"(pv.A2), [a3] "=&v"(pv.A3), "+v"(sk)                 \
               : [wa] "v"(QA.wa + smb), [p] "v"(cvt2(pend, 0.f)), [ra] "v"(pv.ra + smb), [o1] "n"(P_U / 4 + 1)        \
               : "memory");
#define QK_SOFTMAX(QA)                                                                                                \
  {                                                                                                                   \
    u32x4 A0, A1, A2, A3, A4, A5;                                                                                     \
    float scl;                                                                                                        \
    asm volatile("ds_read_b128 %[a0], %[q0]\n\tds_read_b128 %[a1], %[q1]\n\tds_read_b128 %[a2], %[q2]\n\t"            \
                 "ds_read_b128 %[a3], %[q3]\n\tds_read_b128 %[a4], %[q4]\n\tds_read_b128 %[a5], %[q5]"                \
                 : [a0] "=&v"(A0), [a1] "=&v"(A1), [a2] "=&v"(A2), [a3] "=&v"(A3), [a4] "=&v"(A4), [a5] "=&v"(A5)     \
                 : [q0] "v"(QA.qad[0] + smb), [q1] "v"(QA.qad[1] + smb), [q2] "v"(QA.qad[2] + smb),                   \
                   [q3] "v"(QA.qad[3] + smb), [q4] "v"(QA.qad[4] + smb), [q5] "v"(QA.qad[5] + smb)                    \
                 : "memory");                                                                                         \
    asm volatile("ds_read_b32 %[s], %[z] offset:%[ri]\n\t"                                                            \
                 "s_waitcnt lgkmcnt(6)\n\t" SM "%[d0], %[a0], v[64:71], %[ix]\n\t" SM "%[d1], %[a0], v[96:103], %[ix]\n\t"   \
                 "s_waitcnt lgkmcnt(5)\n\t" SM "%[d0], %[a1], v[72:79], %[ix]\n\t" SM "%[d1], %[a1], v[104:111], %[ix]\n\t"   \
                 "s_waitcnt lgkmcnt(4)\n\t" SM "%[d0], %[a2], v[80:87], %[ix]\n\t" SM "%[d1], %[a2], v[112:119], %[ix]\n\t"   \
                 "s_waitcnt lgkmcnt(3)\n\t" SM "%[d0], %[a3], v[88:95], %[ix]\n\t" SM "%[d1], %[a3], v[120:127], %[ix]\n\t"   \
                 "s_waitcnt lgkmcnt(2)\n\t" SM "%[d0], %[a4], v[128:135], %[ix]\n\t" SM "%[d1], %[a4], v[144:151], %[ix]\n\t"   \
                 "s_waitcnt lgkmcnt(0)\n\t" SM "%[d0], %[a5], v[136:143], %[ix]\n\t" SM "%[d1], %[a5], v[152:159], %[ix]\n\t"   \
                                      \
                 "ds_read_b128 %[a0], %[z]\n\tds_read_b128 %[a1], %[z]\n\tds_read_b128 %[a2], %[z]\n\ts_nop 4"        \
                 : [d0] "+v"(QA.D0), [d1] "+v"(QA.D1), [s] "=&v"(scl), [a0] "+v"(A0), [a1] "+v"(A1), [a2] "+v"(A2),   \
                   [a3] "+v"(A3), [a4] "+v"(A4), [a5] "+v"(A5)                                                        \
                 : [ix] "v"(QA.idxq), [z] "v"(smb), [ri] "n"(L_RI), "{v[64:95]}"(kv0), "{v[96:127]}"(kv1),           \
                   "{v[128:159]}"(kv2)                                                                                \
                 : "memory");                                                                                         \
    pv.O0 = __builtin_bit_cast(f32x4, A0);                                                                            \
    pv.O1 = __builtin_bit_cast(f32x4, A1);                                                                            \
    pv.O2 = __builtin_bit_cast(f32x4, A2);                                                                            \
    const f32x2 e0 = f32x2{QA.D0[0], QA.D0[1]} + f32x2{QA.D0[2], QA.D0[3]};                                           \
    const f32x2 e1 = f32x2{QA.D1[0], QA.D1[1]} + f32x2{QA.D1[2], QA.D1[3]};                                           \
    const float s0 = e0[0] + dpp<XOR1>(e0[1]), s1 = e1[1] + dpp<XOR1>(e1[0]);                                         \
    const float sc = ((lane0 & 1) ? s1 : s0) * scl;                                                                   \
    pend = __builtin_amdgcn_exp2f(sc);                                                                                \
    pmax = 0.f;                                                                                                       \
    float sk = sc;                                                                                                    \
    P_WRITE_READ(QA)                                                                                                  \
    const bool unsafe =                                                                                               \
        (__builtin_amdgcn_ballot_w64(!(sk <= 24.f)) != 0ull) | (__builtin_amdgcn_ballot_w64(sk >= -24.f) == 0ull);    \
    if (__builtin_expect(unsafe, 0)) {                                                                                \
      pmax = xrow16_max(max_ror8(max8(sk)));                                                                          \
      pend = __builtin_amdgcn_exp2f(sk - pmax);                                                                       \
      asm volatile("s_waitcnt lgkmcnt(0)" : "+v"(pv.A0), "+v"(pv.A1), "+v"(pv.A2), "+v"(pv.A3));                      \
      P_WRITE_READ(QA)                                                                                                \
      asm volatile("s_waitcnt lgkmcnt(0)" : "+v"(pv.A0), "+v"(pv.A1), "+v"(pv.A2), "+v"(pv.A3));                      \
    }                                                                                                                 \
  }

using u32x16 = __attribute__((ext_vector_type(16))) unsigned;
using u32x32 = __attribute__((ext_vector_type(32))) unsigned;
template <int OFF>
__device__ __forceinline__ u16* karg() {
  u64 p;
  asm volatile("s_load_dwordx2 %0, %1, %2\n\ts_waitcnt lgkmcnt(0)" : "=s"(p) : "s"(__builtin_amdgcn_kernarg_segment_ptr()), "n"(OFF));
  return (u16*)p;
}
template <class T>
__device__ __forceinline__ T* mkptr(u32 lo, u32 hi) {
  return (T*)(((u64)hi << 32) | lo);
}

__global__ __launch_bounds__(NT, 2) void mega_kernel(Params) {
  __shared__ __attribute__((aligned(16))) unsigned char smem[L_TOTAL];
  u32x16 ka;
  u32x4 kb4;
  u32 rtt;
  u64 ts64;
  {
    static_assert(AREA == 1 << 18 && A_GY == 1 << 17 && SIDE == 1 << 13, "clear addresses");
    asm volatile("s_load_dwordx16 s[36:51], %[kp], 0x0\n\ts_load_dwordx4 s[52:55], %[kp], 0x40\n\t"
                 "s_getpc_b64 s[58:59]\n.Lmk_pc:\n\t"
                 "s_mov_b32 s62, m0\n\ts_mov_b32 m0, %[dst]\n\t"
                 "s_cmpk_lt_u32 %[b], %[pfb]\n\ts_cselect_b32 s57, 0x140, 0\n\t"
                 "v_lshlrev_b32 v1, 7, %[tid]\n\tv_min_u32 v1, .Lmk_code_end-.Lmk_pc-4, v1\n\t"
                 "v_cmp_gt_u32 vcc, s57, %[tid]\n\ts_mov_b64 exec, vcc\n\t"
                 "global_load_lds_dword v1, s[58:59]\n\ts_mov_b64 exec, -1\n\t"
                 "v_readfirstlane_b32 s56, %[tid]\n\t"
                 "v_lshrrev_b32 v1, 2, %[tid]\n\tv_cmp_eq_u32 vcc, 3, v1\n\tv_mov_b32 v3, 0x3f803f80\n\ts_lshr_b32 s57, %[b], 5\n\t"
                 "v_cndmask_b32 v2, 0, 1, vcc\n\tv_cmp_eq_u32 vcc, 2, v1\n\tv_lshlrev_b32 v1, 2, %[tid]\n\ts_bfe_u32 s60, %[b], 0x20003\n\t"
                 "v_cndmask_b32 v2, v2, v3, vcc\n\tv_cmp_eq_u32 vcc, 7, %[tid]\n\tv_mov_b32 v3, %[scl]\n\t"
                 "v_min_u32 v1, 80, v1\n\tv_cndmask_b32 v2, v2, v3, vcc\n\t"
                 "ds_write_b32 v1, v2 offset:%[sm]\n\t"
                 "v_lshlrev_b32 v2, 4, %[tid]\n\tv_and_b32 v1, 0x3ff, v2\n\t"
                 "s_cmpk_ge_u32 s56, 448\n\ts_cbranch_scc1 1f\n\t"
                 "s_mul_i32 s57, s57, 0xf0000\n\ts_and_b32 s61, %[b], 7\n\ts_lshl2_add_u32 s60, s61, s60\n\t"
                 "v_bfe_u32 v3, %[tid], 2, 4\n\ts_mul_i32 s60, s60, 0x180\n\tv_mul_u32_u24 v3, 0x3000, v3\n\t"
                 "s_add_u32 s57, s57, s56\n\tv_bitop3_b32 v2, v2, %[tid], 32 bitop3:0x78\n\ts_add_u32 s57, s57, s60\n\t"
                 "v_and_or_b32 v3, v2, 48, v3\n\ts_mul_i32 s60, s56, 80\n\tv_add_u32 v3, s57, v3\n\t"
                 "s_add_u32 s60, s60, %[lwq]\n\ts_cmpk_eq_u32 s56, 384\n\ts_cselect_b64 vcc, -1, 0\n\ts_cselect_b32 m0, %[lhb], s60\n\t"
                 "v_cndmask_b32 v3, v3, v1, vcc\n\ts_mov_b32 %[rtt], 0\n\t"
                 "s_waitcnt lgkmcnt(0)\n\ts_cselect_b64 s[58:59], s[36:37], s[42:43]\n\ts_barrier\n\t"
                 "global_load_lds_dwordx4 v3, s[58:59] nt\n\t"
                 "s_memrealtime %[ts]\n\ts_branch 2f\n"
                 "1:\n\ts_lshl_b32 s57, s57, 17\n\ts_lshl_b32 s60, s60, 13\n\ts_and_b32 s61, %[b], 15\n\ts_or_b32 s57, s57, s60\n\t"
                 "s_lshl_b32 s61, s61, 9\n\t"
                 "v_mov_b64 v[4:5], 0\n\ts_or_b32 s57, s57, s61\n\tv_mov_b64 v[6:7], 0\n\t"
                 "v_or_b32 v1, s57, v1\n\tv_xor_b32 v2, 0x2000, v1\n\ts_mov_b32 exec_hi, 0\n\t"
                 "s_waitcnt lgkmcnt(0)\n\t"
                 "s_add_u32 s58, s54, 0x1fffff\n\ts_addc_u32 s59, s55, 0\n\ts_and_b32 s58, s58, 0xffe00000\n\t"
                 "global_store_dwordx4 v1, v[4:7], s[58:59] sc1\n\tglobal_store_dwordx4 v2, v[4:7], s[58:59] sc1\n\t"
                 "s_barrier\n\ts_mov_b64 exec, -1\n\ts_memrealtime %[ts]\n\t"
                 "s_waitcnt vmcnt(1)\n\ts_memtime s[58:59]\n\ts_waitcnt vmcnt(0)\n\ts_memtime s[60:61]\n\ts_waitcnt lgkmcnt(0)\n\t"
                 "s_sub_u32 %[rtt], s60, s58\n"
                 "2:\n\ts_mov_b32 m0, s62\n\ts_waitcnt lgkmcnt(0)"
                 : "=&{s[36:51]}"(ka), "=&{s[52:55]}"(kb4), [rtt] "=&s"(rtt), [ts] "=&s"(ts64)
                 : [kp] "s"(__builtin_amdgcn_kernarg_segment_ptr()), [tid] "v"(threadIdx.x), [b] "s"(blockIdx.x), [dst] "n"(L_PF),
                   [scl] "n"(__builtin_bit_cast(u32, SCL)), [sm] "n"(0), [lwq] "n"(L_WQ), [lhb] "n"(L_HB), [pfb] "n"(PFB)
                 : "memory", "vcc", "scc", "v1", "v2", "v3", "v4", "v5", "v6", "v7", "s56", "s57", "s58", "s59", "s60", "s61", "s62");
  }
  const u32 tstart = (u32)ts64;
  const int tid = threadIdx.x, lane0 = tid & 63;
  const int wave = __builtin_amdgcn_readfirstlane(tid >> 6);
  const int b = blockIdx.x;
  const int g = b & 7, j = b >> 3, hsub = j & 3, c = j >> 2, h = g * 4 + hsub;
  const bool isO = j < 20;
  const bool leader = b == 0 && wave == 6;
  const u32 fslot = (u32)((j & 1) * 16384 + ((j - 20) >> 1) * 128);
  u32 tmo = SPIN_CAP;
  volatile LAS u32* cnt = (volatile LAS u32*)(smem + L_CNT);
  Params P;
  P.x0 = mkptr<const u16>(ka[0], ka[1]);
  P.kc = mkptr<const u16>(ka[2], ka[3]);
  P.vc = mkptr<const u16>(ka[4], ka[5]);
  P.wq = mkptr<const u16>(ka[6], ka[7]);
  P.wo = mkptr<const u16>(ka[8], ka[9]);
  P.w1 = mkptr<const u16>(ka[10], ka[11]);
  P.w3 = mkptr<const u16>(ka[12], ka[13]);
  P.ws = mkptr<u32>(kb4[2], kb4[3]);
  u32* const wsb = (u32*)(((uintptr_t)P.ws + 0x1fffffu) & ~(uintptr_t)0x1fffffu);
  const __amdgpu_buffer_rsrc_t rs = __builtin_amdgcn_make_buffer_rsrc(wsb, 0, (int)WS_END, 0x00020000);
  const u32x4 rq = {(u32)(uintptr_t)wsb, (u32)((uintptr_t)wsb >> 32) & 0xffffu, WS_END, 0x00020000u};

  u32 anc = 0u;
  u32 tref = 0u, ar = 0u;
  u32x32 kv0, kv1, kv2, kv3, kv4, kv5;
  asm volatile(""
               : "={v[64:95]}"(kv0), "={v[96:127]}"(kv1), "={v[128:159]}"(kv2), "={v[160:191]}"(kv3), "={v[192:223]}"(kv4),
                 "={v[224:255]}"(kv5));
  const int smb = (int)(uintptr_t)(LAS unsigned char*)smem;
  const u32 gop = A_GO + go_slot(hsub * 2) + c * 256;
  u32 tstep = TSTEP + (g < 4 ? SKW : -SKW);
  u32 tnext = tstart * 8u + (u32)wave * (TSTEP / 8) + 5 * TSQ;
  const size_t rowbase = (size_t)h * SK + (SK - WIN) + ROW0;
  const u16* vb = P.vc + rowbase * DH;
  const int rot = ROT;
  const u16* ws0 = isO ? P.wo + (size_t)(768 * g) * D + 32 * j : ((j & 1) ? P.w3 : P.w1) + 192 * g + 32 * ((j - 20) >> 1);
  int wn = ROTW == 0 ? 0 : (ROTW == 1 ? (isO ? (j >> 1) % 6 : (((j - 20) >> 2) * 2 + (j & 1)) % 5) : (isO ? (j >> 2) : (j - 20) >> 2));
  {
    constexpr int L_CTL = L_LAND + 6 * 2048;
    volatile LAS u32* const CTL = (volatile LAS u32*)(smem + L_CTL);
    u32 gd, hb = 0u;
    const u32 lho = WS_HB + (lane0 >> 1) * HBS + (lane0 & 1) * 16;
    const u32 hso = lane0 < 8 ? WS_HB + (u32)h * HBS + lane0 * 4 : WS_GOF + g * HBS;
    if (wave < 6) {
      const u16* s0 = P.wq + (size_t)(80 * c) * (H * DH) + h * DH + wave * 32;
      u32 tq = tnext - 4 * TSQ;
#pragma unroll 1
      for (int p = 1; p < 5; ++p) {
        pace(tq, TSQ);
        dma_piece(s0, H * DH, smem, L_WQ + wave * 5120, p, lane0);
      }
    } else if (wave == 6) {
      u32 keep;
      asm volatile("s_mov_b32 %0, m0\n\ts_mov_b32 m0, %3\n\ts_nop 2\n\tglobal_load_lds_dword %1, %2 offset:1024\n\ts_mov_b32 m0, %0"
                   : "=&s"(keep)
                   : "v"(lane0 * 4), "s"(P.x0), "s"((u32)(smb + L_HB))
                   : "memory");
      if (leader) {
        ctl_load<true>(rq, lho, (u32)(smb + L_CTL));
        ctl_load<false>(rq, WS_GOF + (lane0 & 7) * HBS, (u32)(smb + L_CTL + 2560));
      }
      asm volatile("s_waitcnt vmcnt(0)" ::: "memory");
      const u32x4 lv = afrag(smem, L_HB + lane0 * 16);
      const u32 xb = *reinterpret_cast<const u32*>(smem + L_HB + 1024 + lane0 * 4);
      float v[10];
#pragma unroll
      for (int k = 0; k < 4; ++k) {
        v[2 * k] = bflo(lv[k]);
        v[2 * k + 1] = bfhi(lv[k]);
      }
      v[8] = bflo(xb);
      v[9] = bfhi(xb);
      float sm = 0.f;
#pragma unroll
      for (int k = 0; k < 10; ++k) sm += v[k];
      const float mean = wave_sum(sm) * (1.0f / D);
      float q = 0.f;
#pragma unroll
      for (int k = 0; k < 10; ++k) q = fmaf(v[k] - mean, v[k] - mean, q);
      const float var = wave_sum(q) * (1.0f / D);
      const float mb = bf16r(mean);
      const float ri = __builtin_amdgcn_rcpf(bf16r(__builtin_amdgcn_sqrtf(bf16r(bf16r(var) + 1e-5f))));
      u32 hp[5];
#pragma unroll
      for (int k = 0; k < 5; ++k) {
        const u32 cc = cvt2(v[2 * k] - mb, v[2 * k + 1] - mb);
        hp[k] = cvt2(bflo(cc) * ri, bfhi(cc) * ri);
      }
      *reinterpret_cast<u32x4*>(smem + L_LN + lane0 * 16) = u32x4{hp[0], hp[1], hp[2], hp[3]};
      *reinterpret_cast<u32*>(smem + L_LN + 1024 + lane0 * 4) = hp[4];
      asm volatile("s_waitcnt lgkmcnt(0)" ::: "memory");
      *(volatile LAS u32*)(smem + L_VOTE + 4) = 1u;
    } else {
      ctl_load<false>(rq, hso, (u32)(smb + L_CTL + 2304));
      hb = rtt > PTHR ? 1u << ((g >> 2) ^ (j & 1)) : 0u;
    }
    if (tid == 0) *(volatile LAS u32*)(smem + L_ANC) = 0u;
    *reinterpret_cast<u32x4*>(smem + L_P + wave * P_W + lane0 * 16) = u32x4{0u, 0u, 0u, 0u};
    *reinterpret_cast<u32x4*>(smem + (lane0 < 16 ? L_P + wave * P_W + 1024 + lane0 * 16 : L_OM + (wave * OM_S + OM_P + DH) * 4)) =
        u32x4{0u, 0u, 0u, 0u};
    const u16* kb = P.kc + rowbase * DH;
    const u32 vo = (lane0 >> 3) * 384 + 16 * (lane0 & 7);
    const u32 kw = (u32)(smb + L_W2 + wave * 1024 + lane0 * 16), kr = (u32)(smb + L_W2 + wave * 1024 + (lane0 & 15) * 64 + (lane0 >> 4) * 16);
#define KPERM(i)                                                                          \
  asm volatile("ds_write_b128 %[w], v[%[r0]:%[r1]]\n\tds_read_b128 v[%[r0]:%[r1]], %[r]" \
               :                                                                          \
               : [w] "v"(kw), [r] "v"(kr), [r0] "n"(kreg(i)), [r1] "n"(kreg(i) + 3)       \
               : "memory")
#define WNEXT                                                                                  \
  dma_piece(ws0, isO ? D : F, smem, L_WB, wave * (isO ? 6 : 5) + wn, fresh(lane0));            \
  wn = wn == (isO ? 5 : 4) ? 0 : wn + 1;
#define WPIECE(t)                                                                                                    \
  if ((t) >= WP0 && ((t) - WP0) % WPD < WSP && ((t) - WP0) / WPD < NINO && (isO || ((t) - WP0) / WPD < NINF) &&      \
      (WSP == 1 || (wave & (WSP - 1)) == ((t) - WP0) % WPD)) {                                                       \
    pace(tnext, tstep + WPX);                                                                                        \
    WNEXT                                                                                                            \
  }
#define KLOADS(A, B)                                                          \
  _Pragma("unroll") for (int i = A; i < B; ++i) {                             \
    if (i == KSW - 1) tstep += isO ? TS_O - TSTEP : TS_F - TSTEP;             \
    LOADW(kreg(i), vo, kb + ((i / 3 + rot) & 7) * GST * DH, 128 * (i % 3));    \
    if (i >= WBIG - 1) KPERM(i >= WBIG - 1 ? i - WBIG + 1 : 0);               \
    WPIECE(i)                                                                 \
  }
    static_assert(QI > 2 && BEAT0 >= QI + 3 && CKL >= BEAT0 + 3 && CKG >= CKL && CKQ > CKG && CKQ <= 23, "order");
    KLOADS(0, QI)
    int q_l0, q_l1, q_bk, q_ra, q_rb, q_rc;
    u32 q_dst;
    {
      const int lane = fresh(lane0), m = lane & 15, G = lane >> 4;
      q_l0 = fresh(L_WQ + wave * 5120 + wlane(lane, 0));
      q_l1 = fresh(L_WQ + wave * 5120 + wlane(lane, 1));
      q_bk = fresh(4096 - (G >> 1) * 1024);
      const int st = m == 8 ? 64 : 0;
      const int sa = m == 8 ? L_LN + 160 * c + 16 * G : (m == 15 ? L_ONE : L_ZERO);
      q_ra = fresh(sa);
      q_rb = fresh(sa + st);
      q_rc = fresh(G < 2 ? sa + 2 * st : L_TAG);
      q_dst = fresh(WS_GQ1 + (u32)(h * 8 + c) * Q1S + wave * 128 + (lane >> 5) * 64 + m * 4);
    }
    KLOADS(QI, QI + 1)
    f32x4 q_d0, q_d1;
    asm volatile("" : "=v"(q_d0), "=v"(q_d1));
    if (wave < 6) {
      asm volatile("s_waitcnt vmcnt(%0)" ::"n"(QI + 1) : "memory");
      lds_wait((volatile LAS u32*)(smem + L_VOTE + 4), 1u);
      const u32x4 f0 = afrag(smem, q_ra), f1 = afrag(smem, q_rb), f2 = afrag(smem, q_rc);
      q_d0 = q_d1 = f32x4{0.f, 0.f, 0.f, 0.f};
      q_d0 = mfma32(f0, wfrag(smem, q_l0, 0), q_d0);
      q_d1 = mfma32(f0, wfrag(smem, q_l1, 0), q_d1);
      q_d0 = mfma32(f1, wfrag(smem, q_l0, 2048), q_d0);
      q_d1 = mfma32(f1, wfrag(smem, q_l1, 2048), q_d1);
      q_d0 = mfma32(f2, wfrag(smem, q_l0 + q_bk, 0), q_d0);
      q_d1 = mfma32(f2, wfrag(smem, q_l1 + q_bk, 0), q_d1);
    }
    KLOADS(QI + 1, QI + 2)
    if (wave < 6) {
      auto sw = __builtin_amdgcn_permlane32_swap(__float_as_uint(sum4(q_d0)), __float_as_uint(sum4(q_d1)), false, false);
      auto sx = __builtin_amdgcn_permlane16_swap(sw[1], sw[1], false, false);
      const f32x4 qd = quad4(__uint_as_float(cvt2(__uint_as_float(sx[0]), __uint_as_float(sx[1]))));
      if ((lane0 & 19) == 0) store16<AUXQ>(rs, q_dst, __builtin_bit_cast(u32x4, qd));
    }
    KLOADS(QI + 2, QI + 3)
    att_table<0>(smem, fresh(lane0), wave, gop);
    KLOADS(QI + 3, QI + 4)
    att_table<1>(smem, fresh(lane0), wave, gop);
    KLOADS(QI + 4, BEAT0 + 1)
    asm volatile("s_waitcnt vmcnt(%0)" ::"n"(BEAT0 - QI - 1) : "memory");
    if (wave < 6 && lane0 == 0) lds_add(smem, L_CNT + 4, 1u);
    if (wave == 7) {
      lds_wait(cnt + 1, 6u);
      hb += (__builtin_amdgcn_readfirstlane(CTL[576 + c]) & ~3u) + 4u;
      if (lane0 == 0) __builtin_amdgcn_raw_buffer_store_b32(hb, rs, WS_HB + h * HBS + c * 4, 0, 16);
      hb += 4u;
    }
    KLOADS(BEAT0 + 1, CKL + 1)
    if (wave == 7) ctl_load<false>(rq, hso, (u32)(smb + L_CTL + 2048));
    KLOADS(CKL + 1, CKG + 1)
    if (leader) ctl_load<true>(rq, lho, (u32)(smb + L_CTL + 1024));
    KLOADS(CKG + 1, CKQ + 1)
    if (wave == 7) {
      asm volatile("s_waitcnt vmcnt(%0)" ::"n"(CKQ - CKL) : "memory");
      HS_WAIT((u32)ch == 0xffu)
      if (lane0 == 0) cnt[1] = 7u;
    }
    KLOADS(CKQ + 1, 24)
#define VLOADS(A, B)                                                                           \
  _Pragma("unroll") for (int i = A; i < B; ++i) {                                              \
    if (i == KS2) tstep -= TDEC;                                                               \
    LOADW(vreg(i), vo, vb + ((i / 3 + rot) & 7) * GST * DH, 128 * (i % 3));                     \
    KPV(i)                                                                                     \
    WPIECE(24 + i)                                                                             \
  }
#define KPV(i) if (i < WBIG - 1) KPERM(i < WBIG - 1 ? 25 + i - WBIG : 0);
    VLOADS(0, 2)
    if (leader) {
      asm volatile("s_waitcnt vmcnt(%0)" ::"n"(25 - CKG) : "memory");
      const u32x4 xa = afrag(smem, L_CTL + lane0 * 16);
      u32x4 lv;
#pragma unroll 1
      for (;;) {
        asm volatile("" ::: "memory");
        lv = afrag(smem, L_CTL + 1024 + lane0 * 16);
        const bool same = lv[0] == xa[0] || lv[1] == xa[1] || lv[2] == xa[2] || lv[3] == xa[3];
        if (__builtin_amdgcn_ballot_w64(same) == 0ull || tmo == 0u) break;
        --tmo;
        __builtin_amdgcn_s_sleep(4);
        ctl_load<true>(rq, lho, (u32)(smb + L_CTL + 1024));
        asm volatile("s_waitcnt vmcnt(0)" ::: "memory");
      }
      u32 nv = 256u;
#pragma unroll
      for (int e = 0; e < 4; ++e)
        nv += (u32)__builtin_popcountll(__builtin_amdgcn_ballot_w64((lv[e] & 2u) != 0u)) -
              (u32)__builtin_popcountll(__builtin_amdgcn_ballot_w64((lv[e] & 1u) != 0u));
      if (lane0 < 8) __builtin_amdgcn_raw_buffer_store_b32(CTL[640 + lane0] + (nv > 256u ? 2u : 1u), rs, WS_GOF + lane0 * HBS, 0, 16);
    }
    const u32 qo = WS_GQ1 + (u32)(h * 8 + (lane0 >> 3)) * Q1S + wave * 128 + (lane0 & 7) * 16;
    if (wave < 6) {
      lds_wait(cnt + 1, 7u);
      ctl_load<true, true>(rq, qo, (u32)(smb + L_LAND + wave * 2048));
    }
    VLOADS(2, XB - WBIG)
    if (wave == 7) ctl_load<false>(rq, hso, (u32)(smb + L_CTL + 2048));
    VLOADS(XB - WBIG, WBIG + 2)
    if (wave < 6) {
      q1_finish(smem, fresh(lane0), wave);
      asm volatile("s_waitcnt lgkmcnt(0)" ::: "memory");
      if (lane0 == 0) lds_add(smem, L_CNT, 1u);
    }
#undef KPV
#define KPV(i)
    VLOADS(WBIG + 2, XA)
    if (QKP) tstep >>= 1;
    if (QKP) tnext += wave < 4 ? (XB - XA) * tstep : (QKP == 2 ? -tstep : 0);
    if (wave >= 4) {
      VLOADS(XA, XB)
      if (QKP) tnext += (XB - XA + (QKP == 2)) * tstep;
      if (wave == 7) {
        HS_WAIT(false)
        if (lane0 == 8) *(volatile LAS u32*)(smem + L_VOTE) = gd >> 1;
      }
    }
  }

#pragma unroll 1
  for (int half = 0;; ++half) {
    {
      float pend, pmax;
      PVAddr pv;
      QKAddr qa;
      const int lane = fresh(lane0);
      const u32 off[1] = {ar + A_GQ + hsub * 16384 + (wave < 6 ? (lane >> 3) * 896 + wave * 128 + (lane & 7) * 16 : (lane & 7) * 896 + 768)};
      f32x4 qv[1];
      asm volatile("" : "=v"(qv[0]));
      if (half == 1) anc = lds_wait_nz((volatile LAS u32*)(smem + L_ANC));
      if (half == 1 && wave < 7) {
        wait_rt(anc + A_Q);
        POLL16(qv[0], off[0], 0);
      }
      int qw;
      att_setup(qa, pv, smem, lane, wave, qw);
      if (half == 0) {
        lds_wait(cnt, 6u);
      } else {
        if (wave < 7) {
          asm volatile("s_waitcnt vmcnt(0)" : "+v"(qv[0])::"memory");
          if (__builtin_expect(__builtin_amdgcn_ballot_w64((and4(__builtin_bit_cast(u32x4, qv[0])) & 1u) == 0u) != 0ull, 0))
            gpoll4<1>(rq, off, qv);
          if (wave < 6) {
            const float t = tsum8(qv[0]);
            *reinterpret_cast<u16*>(smem + qw) = (u16)cvt2(t, t);
          } else {
            const LnS ls = ln_stats(qv[0][3], qv[0][0] + qv[0][1] + qv[0][2]);
            *reinterpret_cast<float*>(smem + L_RI) = ls.ri * SCL;
          }
        }
        if (wave < 4) __builtin_amdgcn_s_setprio(2);
        bar();
      }
      QK_SOFTMAX(qa)
      if (half == 0) {
        const int lv_ = fresh(lane0);
        const u32 vo = (lv_ >> 3) * 384 + 16 * (lv_ & 7);
        if (wave < 4) {
          VLOADS(XA, XB)
        }
        if (QKP) tstep <<= 1;
        VLOADS(XB, LCL)
#define NOLC 0
        if (wave == 7 && !NOLC) {
          const u32 a7 = (u32)(g & 3) * AREA + (((u32)(g >> 2) ^ __builtin_amdgcn_readfirstlane(*(volatile LAS u32*)(smem + L_VOTE))) & 1u) * SIDE +
                         (u32)(j >> 3) * 16384u + (u32)(j & 7) * 1024u;
          const u32x4 zz = __builtin_bit_cast(u32x4, zero4());
          store16(rs, lv_ * 16 + a7 + 2 * 16384, zz);
          store16(rs, lv_ * 16 + a7 + (j < 16 ? 6 : 8) * 16384, zz);
          store16(rs, lv_ * 16 + a7 + 12 * 16384, zz);
        }
        VLOADS(LCL, 24)
        tnext += isO ? -TBACK : TFD;
#pragma unroll 1
        for (int p = 0; p < (isO ? NPRE : NPF); ++p) {
          pace(tnext, isO ? TPV : TPF);
          WNEXT
        }
      }
#define DA(CTL) "v_add_f32_dpp %[l], %[l], %[l] " CTL " row_mask:0xf bank_mask:0xf\n\t"
#define PW(W) "s_waitcnt vmcnt(6*" #W "+%[no]) lgkmcnt(" #W ")\n\ts_cbranch_scc0 1f\n\ts_waitcnt vmcnt(6*" #W "+%[nf])\n1:\n\t"
#define PX(o, v, a) SM "%[o" #o "], %[a" #a "], v[160+24*" #a "+8*" #o ":167+24*" #a "+8*" #o "], %[ix]\n\t"
      asm volatile("s_cmp_gt_i32 %[y], 19\n\t"
                   PW(3) PX(0, x, 0) DA("quad_perm:[1,0,3,2]") PX(1, y, 0) PX(2, z, 0)
                   PW(2) PX(0, x, 1) DA("quad_perm:[2,3,0,1]") PX(1, y, 1) PX(2, z, 1)
                   PW(1) PX(0, x, 2) DA("row_half_mirror") PX(1, y, 2) PX(2, z, 2)
                   PW(0) PX(0, x, 3) DA("row_ror:8") PX(1, y, 3) PX(2, z, 3) "s_setprio 0"
                   : [o0] "+v"(pv.O0), [o1] "+v"(pv.O1), [o2] "+v"(pv.O2), [a0] "+v"(pv.A0), [a1] "+v"(pv.A1),
                     [a2] "+v"(pv.A2), [a3] "+v"(pv.A3), [l] "+v"(pend)
                   : [ix] "v"(pv.idx), [no] "n"(NPRE), [nf] "n"(NPF), [y] "s"(j - 32 * half), "{v[160:191]}"(kv3),
                     "{v[192:223]}"(kv4), "{v[224:255]}"(kv5)
                   : "memory", "scc");
      asm volatile("ds_write2_b32 %[la], %[l], %[mx] offset1:4\n\ts_mov_b64 exec, %[m]\n\ts_nop 2\n\t"
                   "ds_write_b128 %[a], %[o0]\n\tds_write_b128 %[a], %[o1] offset:256\n\t"
                   "ds_write_b128 %[a], %[o2] offset:512\n\t"
                   "s_mov_b64 exec, -1\n\ts_waitcnt lgkmcnt(0)\n\ts_barrier"
                   :
                   : [m] "s"(0xff00ff0000ff00ffull), [a] "v"(pv.oa + smb), [o0] "v"(pv.O0), [o1] "v"(pv.O1), [o2] "v"(pv.O2),
                     [la] "v"(pv.la + smb), [l] "v"(pend), [mx] "v"(pmax)
                   : "memory");
      tref = now();
      if (half == 0) ar = (u32)(g & 3) * AREA + (((u32)(g >> 2) ^ __builtin_amdgcn_readfirstlane(*(volatile LAS u32*)(smem + L_VOTE))) & 1u) * SIDE;
      if (wave < 4) {
        const float* mp = reinterpret_cast<const float*>(smem + pv.ma);
        float m0 = mp[0], m1 = mp[4 * OM_S];
        f32x4 v = *reinterpret_cast<const f32x4*>(smem + pv.sa), v1 = *reinterpret_cast<const f32x4*>(smem + pv.sa + OM_P * 4);
        f32x4 u = *reinterpret_cast<const f32x4*>(smem + pv.sa + OM_S * 16);
        f32x4 u1 = *reinterpret_cast<const f32x4*>(smem + pv.sa + OM_S * 16 + OM_P * 4);
        asm volatile("" : "+v"(m0), "+v"(m1), "+v"(v), "+v"(v1), "+v"(u), "+v"(u1));
        v += v1;
        u += u1;
        float M = 0.f;
        if (__builtin_expect(__builtin_amdgcn_ballot_w64((__float_as_uint(m0) | __float_as_uint(m1)) != 0u) != 0ull, 0)) {
          M = fmx(m0, m1);
          M = fmx(M, dpp<XOR1>(M));
          M = fmx(M, dpp<XOR2>(M));
          v *= __builtin_amdgcn_exp2f(m0 - M);
          u *= __builtin_amdgcn_exp2f(m1 - M);
        }
        v = sum4x4(v + u);
        u32 p0 = cvt2(v[0], v[1]), p1 = cvt2(v[2], v[3]);
        if (wave == 3) {
          p0 = __float_as_uint(M);
          p1 = __float_as_uint(sum4(v));
        }
        p0 |= 1u;
        p1 |= 1u;
        const u32x4 pk = {p0, p1, dppu<XOR7>(p0), dppu<XOR7>(p1)};
        asm volatile("s_mov_b64 exec, %2\n\tbuffer_store_dwordx4 %0, %1, %3, %4 offen sc1\n\ts_mov_b64 exec, -1"
                     :
                     : "v"(pk), "v"(pv.dst), "s"(wave < 3 ? 0x0101010101010101ull : 0x3ull), "s"(rq), "s"(ar + (u32)half * GO_SZ)
                     : "memory");
      }
    }
    const u32 go = ar + A_GO + (u32)half * GO_SZ;
    const u32 gyw = A_GY + (u32)half * GY_PAIR;
    const u32 gy = gyw + ar;
    const u32 gh = go + A_GH;
    const int land = L_LAND + wave * 2048;
    const u32 landa = (u32)(uintptr_t)(LAS unsigned char*)smem + (u32)land;
    if (isO) {
      {
        const int lane = fresh(lane0);
        const u32 voff = fresh(go + go_slot(wave) + ((lane * 16) ^ ((lane >> 4) * 64)));
        if (half)
          wait_rt(anc + A_O);
        else
          wait_until(tref + 24 * T_O);
        dma_poll<false>(rq, voff, landa, ~0ull, ~0ull);
        const int n = lane & 15, G = lane >> 4;
        const int r0_ = land + (n & 7) * 256 + (n & 3) * 64;
        const int ra = fresh(n < 8 ? r0_ + G * 16 : r0_ ^ 192), rb = fresh((r0_ + G * 16) ^ 64), rc = fresh((r0_ + G * 16) ^ 128);
        const int l0 = fresh(L_WB + wave * 6144 + wlane(lane, 0)), l1 = fresh(L_WB + wave * 6144 + wlane(lane, 1));
        asm volatile("s_waitcnt vmcnt(2)" ::: "memory");
        u32x4 wf[6];
#pragma unroll
        for (int k = 0; k < 3; ++k) {
          wf[2 * k] = wfrag(smem, l0, 2048 * k);
          wf[2 * k + 1] = wfrag(smem, l1, 2048 * k);
        }
        const int rw = fresh(L_RED + wave * 144 + lane * 4);
        const int rr = fresh(L_RED + (lane & 3) * 144 + ((lane >> 2) & 7) * 16);
        const u32 dst = fresh(gyw + gy_rep((lane & 3) + (lane >> 5) * 4) + gy_chunk(4 * j + ((lane >> 3) & 3)) + g * 256);
        f32x4 d0, d1;
        float mc, lc, t1_;
        u32 budget = SPIN_CAP;
#pragma unroll 1
        for (;;) {
          u64 need;
          u32 t;
          asm volatile("s_waitcnt vmcnt(0)\n\t"
                       "ds_read_b128 " F0 ", %[ra]\n\tds_read_b128 " F1 ", %[rb]\n\tds_read_b128 " F2 ", %[rc]\n\t"
                       "s_waitcnt lgkmcnt(2)\n\t"
                       MF "%[d0], " F0 ", %[w0], 0\n\t"
                       "v_add_f32_dpp %[l], v53, v53 quad_perm:[1,0,3,2] row_mask:0xf bank_mask:0xf\n\t"
                       MF "%[d1], " F0 ", %[w1], 0\n\t" AND3("v52", "v53", "v54")
                       "s_waitcnt lgkmcnt(1)\n\t"
                       MF "%[d0], " F1 ", %[w2], %[d0]\n\t"
                       "v_add_f32_dpp %[l], %[l], %[l] quad_perm:[2,3,0,1] row_mask:0xf bank_mask:0xf\n\t" AND3("%[t]", "v55", "v56")
                       MF "%[d1], " F1 ", %[w3], %[d1]\n\t" AND3("%[t]", "v57", "v58")
                       "v_add_f32_dpp %[l], %[l], %[l] row_half_mirror row_mask:0xf bank_mask:0xf\n\t"
                       "s_waitcnt lgkmcnt(0)\n\t"
                       MF "%[d0], " F2 ", %[w4], %[d0]\n\t" AND3("%[t]", "v59", "v60") AND3("%[t]", "v61", "v62")
                       MF "%[d1], " F2 ", %[w5], %[d1]\n\t" AND3("%[t]", "1", "v63")
                       "v_rcp_f32 %[l], %[l]\n\tv_cmp_eq_u32 %[need], 0, %[t]"
                       : [d0] "=&v"(d0), [d1] "=&v"(d1), [t] "=&v"(t), [need] "=&s"(need), [l] "=&v"(lc), "=&{v52}"(mc), "=&{v53}"(t1_)
                       : [ra] "v"(ra), [rb] "v"(rb), [rc] "v"(rc), [w0] "v"(wf[0]), [w1] "v"(wf[1]), [w2] "v"(wf[2]), [w3] "v"(wf[3]),
                         [w4] "v"(wf[4]), [w5] "v"(wf[5])
                       : "memory", "v54", "v55", "v56", "v57", "v58", "v59", "v60", "v61", "v62", "v63");
          if (__builtin_expect(need == 0ull || budget == 0u, 1)) break;
          --budget;
          u64 ea, eb;
          retry_masks<16>(need, lane, ea, eb);
          dma_poll<false>(rq, voff, landa, ea, eb);
        }
        float rl = __uint_as_float(__builtin_amdgcn_readlane(__float_as_uint(lc), 8));
        asm volatile("s_nop 1" : "+v"(d0), "+v"(d1));
        if (__builtin_expect((__builtin_amdgcn_ballot_w64(__float_as_uint(mc) != 1u) & 0xff00ull) != 0ull, 0)) {
          const float fc = __builtin_amdgcn_exp2f(mc - max8(mc));
          const u32 sc = __float_as_uint(fc * __builtin_amdgcn_rcpf(sum8(fc * t1_)));
#pragma unroll
          for (int e = 0; e < 4; ++e) {
            const float se = __uint_as_float(G == 0 ? __builtin_amdgcn_readlane(sc, 8 + e) : __builtin_amdgcn_readlane(sc, 12 + e));
            d0[e] *= se;
            d1[e] *= se;
          }
          rl = 1.0f;
        }
        {
          auto sw = __builtin_amdgcn_permlane16_swap(__float_as_uint(sum4(d0)), __float_as_uint(sum4(d1)), false, false);
          const float r = (__uint_as_float(sw[0]) + __uint_as_float(sw[1])) * rl;
          if (lane < 32) *reinterpret_cast<float*>(smem + rw) = r;
        }
        bar();
        if (wave == 0) {
          const f32x4 y = sum4x4(*reinterpret_cast<const f32x4*>(smem + rr) + *reinterpret_cast<const f32x4*>(smem + rr + 576));
          const u32 p0 = cvt2(y[0], y[1]) | 1u, p1 = cvt2(y[2], y[3]) | 1u;
          const u32x4 pk = {p0, p1, dppu<XOR7>(p0), dppu<XOR7>(p1)};
          asm volatile("s_mov_b64 exec, %2\n\tbuffer_store_dwordx4 %0, %1, %3, 0 offen sc1\n\ts_mov_b64 exec, -1"
                       :
                       : "v"(pk), "v"(dst), "s"(0x0f0f0f0f0f0f0f0full), "s"(rq)
                       : "memory");
        }
      }
      tref = now();
      if (half == 0) {
        if (wave >= 1 && wave < 7) {
          const u16* s0 = karg<56>() + (size_t)(192 * g) * D + 32 * j;
#pragma unroll
          for (int p = 0; p < 2; ++p) dma_piece(s0, D, smem, L_W2, (wave - 1) * 2 + p, fresh(lane0));
          asm volatile("s_waitcnt vmcnt(0)" ::: "memory");
        }
        bar();
        if (wave == 4) {
          wait_until(tref + 24 * T_H);
          stamp_poll(smem, rq, ar + st_line(fresh(lane0) & 7));
        }
        if (wave == 5) {
          wait_until(tref + 24 * T_LAZY);
          x_update<false>(smem, rq, fresh(lane0), gy + gy_row(c), c);
        }
      } else if (g == 0 && wave == 2) {
        const int lane = fresh(lane0);
        const int col = j * 32 + (lane & 7) * 4 + ((lane >> 4) & 1) * 2 + (lane >> 5);
        u32 x0v, dummy;
        u16* const po = karg<64>() + col;
        asm volatile("global_load_ushort %0, %2, off\n\tglobal_load_ushort %1, %3, off\n\ts_waitcnt vmcnt(0)"
                     : "=&v"(x0v), "=&v"(dummy)
                     : "v"(karg<0>() + col), "v"(po)
                     : "memory");
        wait_until(tref + 24 * T_LAZY);
        const u32 o = gy + gy_chunk(4 * j + ((lane >> 1) & 3)) + (lane >> 3) * 256 + (lane & 1) * 8;
        const u32 off[3] = {o - GY_PAIR, o - GY_PAIR + GY_INST, o};
        float y[3];
        col_poll<3>(rq, off, y);
        const float x3 = bf16r(bf16r(bf16r(bflo(x0v) + y[0]) + y[1]) + y[2]);
        const u32 off2[1] = {o + GY_INST};
        wait_rt(anc + A_F);
        float y4[1];
        col_poll<1>(rq, off2, y4);
        const float x4 = x3 + y4[0];
        if ((lane & 8) == 0) asm volatile("global_store_short %0, %1, off" ::"v"(po), "v"(cvt2(x4, x4)) : "memory");
      }
      if (wave >= 6) {
        const int lane = fresh(lane0), wv = wave - 6;
        const int n = lane & 15, G = lane >> 4;
        const int l0 = fresh(L_W2 + wlane(lane, wv));
        u32x4 wf[6];
#pragma unroll
        for (int k = 0; k < 6; ++k) wf[k] = wfrag(smem, l0, 2048 * k);
        const int l48 = lane < 48 ? lane : 0;
        const u32 off[2] = {gh + (u32)l48 * 16, gh + (u32)l48 * 16 + 16384};
        const int aw = fresh(L_ACT + wv * 384 + l48 * 8);
        const int ab = fresh(L_ACT + wv * 384 + 16 * G);
        const u32 dst = fresh(gyw + GY_INST + gy_rep(n & 7) + gy_chunk(4 * j + wv * 2 + (G >> 1)) + g * 256);
        f32x4 v[2];
        if (half)
          wait_rt(anc + A_H);
        else
          wait_until(tref + 24 * T_H);
        gpoll4<2>(rq, off, v);
        {
          f32x4 a, u;
#pragma unroll
          for (int e = 0; e < 4; ++e) {
            a[e] = bfhi(__float_as_uint(v[0][e]));
            u[e] = bfhi(__float_as_uint(v[1][e]));
          }
          const f32x4 hh = a * u;
          *reinterpret_cast<u32x2*>(smem + aw) = u32x2{cvt2(hh[0], hh[1]), cvt2(hh[2], hh[3])};
        }
        u32x4 af[4];
#pragma unroll
        for (int k = 0; k < 4; ++k) af[k] = afrag(smem, ab + 64 * k);
        asm volatile("" : "+v"(af[0]), "+v"(af[1]), "+v"(af[2]), "+v"(af[3]));
        f32x4 d = {0.f, 0.f, 0.f, 0.f};
        d = mfma32(wf[0], af[0], d);
        d = mfma32(wf[1], af[1], d);
        asm volatile("" : "+v"(d)::"memory");
        af[0] = afrag(smem, ab + 256);
        af[1] = afrag(smem, ab + 320);
        d = mfma32(wf[2], af[2], d);
        d = mfma32(wf[3], af[3], d);
        d = mfma32(wf[4], af[0], d);
        d = mfma32(wf[5], af[1], d);
        const u32 p0 = cvt2(d[0], d[1]) | 1u, p1 = cvt2(d[2], d[3]) | 1u;
        auto s0 = __builtin_amdgcn_permlane16_swap(p0, p0, false, false);
        auto s1 = __builtin_amdgcn_permlane16_swap(p1, p1, false, false);
        if ((lane & (half ? 27 : 24)) == 0) store16(rs, dst, u32x4{s0[0], s1[0], s0[1], s1[1]});
      }
    } else {
      const int lane = fresh(lane0);
      const int n = lane & 15, G = lane >> 4;
      if (half == 1) {
        x_update<false>(smem, rq, lane, gy - GY_PAIR + GY_INST + gy_row(wave), wave);
      } else {
#pragma unroll 1
        for (int p = NINF + NPF; p < 5; ++p) {
          wait_until(tref + 24 * (T_FW + (p - (NINF + NPF)) * T_FD));
          WNEXT
        }
      }
      asm volatile("s_waitcnt vmcnt(0) lgkmcnt(0)" ::: "memory");
      G80 a;
      g80_load(a, smem, lane, L_WB + wave * 5120, L_HB + 160 * wave, land);
      const u32 voff = fresh(gy + gy_row(wave) + ((lane * 16) ^ ((lane >> 4) * 64)));
      const int rw = fresh(L_RED + n * 144 + wave * 16 + G * 4);
      const int sw_ = fresh(n == 0 ? L_ST + wave * 16 + G * 4 : L_ACT);
      float r0, r1, z;
      if (half)
        wait_rt(anc + A_Y);
      else
        wait_until(tref + 24 * T_YF);
      g80_run<true>(a, smem, rq, lane, voff, landa, r0, r1, z);
      *reinterpret_cast<float*>(smem + rw) = r0;
      *reinterpret_cast<float*>(smem + rw + 16 * 144) = r1;
      *reinterpret_cast<float*>(smem + sw_) = z;
      const int ln = fresh(lane0);
      const int rr = fresh(L_RED + (ln & 31) * 144 + (ln >> 5) * 64), rs_ = fresh(L_ST + 16 * (ln & 7));
      const u32 dst = fresh(gh + fslot + ln * 4);
      bar();
      tref = now();
      if (half == 0 && wave == 1 && j == 20 && ln < 8)
        __builtin_amdgcn_raw_buffer_store_b32((rtnow() << 1) | 1u, rs, gy_rep(ln) + st_line(g), 0, 16);
      if (wave == 0) {
        f32x4 st = *reinterpret_cast<const f32x4*>(smem + rs_);
        f32x4 q0 = *reinterpret_cast<const f32x4*>(smem + rr), q1 = *reinterpret_cast<const f32x4*>(smem + rr + 16);
        f32x4 q2 = *reinterpret_cast<const f32x4*>(smem + rr + 32), q3 = *reinterpret_cast<const f32x4*>(smem + rr + 48);
        asm volatile("" : "+v"(st), "+v"(q0), "+v"(q1), "+v"(q2), "+v"(q3));
        const LnS ls = ln_stats(st[3], st[0] + st[1] + st[2]);
        const f32x4 qs = (q0 + q1) + (q2 + q3);
        float r = fmaf(-ls.mb, qs[3], qs[0] + qs[1] + qs[2]);
        auto sw = __builtin_amdgcn_permlane32_swap(__float_as_uint(r), __float_as_uint(r), false, false);
        r = bf16r((__uint_as_float(sw[0]) + __uint_as_float(sw[1])) * ls.ri);
        if ((j & 1) == 0) r = bf16r(r * __builtin_amdgcn_rcpf(1.0f + __builtin_amdgcn_exp2f(r * -1.4426950408889634f)));
        const f32x4 qd = quad4(r);
        if ((ln & 35) == 0) store_quad(rs, dst, qd);
      }
      if (half == 0) {
        x_update<true>(smem, rq, fresh(lane0), land, wave);
        if (wave == 7) {
          wait_until(tref + 24 * T_AN);
          stamp_poll(smem, rq, ar + st_line(ln & 7));
        }
      }
    }
    if (half == 1) break;
    {
      if (wave < 6) {
        const int lane = fresh(lane0);
        const int n = lane & 15;
        G80 a;
        g80_load(a, smem, lane, L_WQ + wave * 5120, L_HB + 160 * c, land);
        const u32 voff = fresh(gy + GY_INST + gy_row(c) + ((lane * 16) ^ ((lane >> 4) * 64)));
        const float cc = *reinterpret_cast<const float*>(smem + L_CS + wave * 128 + (lane >> 5) * 64 + n * 4);
        const u32 gq = ar + A_GQ + hsub * 16384 + c * 896;
        const u32 dst = fresh(gq + wave * 128 + (lane >> 5) * 64 + n * 4);
        float r0, r1, z;
        bar();
        wait_rt(__builtin_amdgcn_readfirstlane(*(volatile LAS u32*)(smem + L_ANC)) + A_E);
        g80_run<true>(a, smem, rq, lane, voff, landa, r0, r1, z);
        auto sw = __builtin_amdgcn_permlane32_swap(__float_as_uint(r0), __float_as_uint(r1), false, false);
        const float sh = cc * __uint_as_float(__builtin_amdgcn_readlane(__float_as_uint(z), 48));
        float r = __uint_as_float(sw[0]) +
                  __uint_as_float((u32)__builtin_amdgcn_update_dpp((int)sw[1], (int)__float_as_uint(sh), 0xE4, 0xA, 0xf, false));
        auto sx = __builtin_amdgcn_permlane16_swap(__float_as_uint(r), __float_as_uint(r), false, false);
        r = __uint_as_float(sx[0]) + __uint_as_float(sx[1]);
        const f32x4 qd = quad4(r);
        if ((lane & 19) == 0) store_quad(rs, dst, qd);
        if (wave == 5) {
          const u32 zu = __float_as_uint(z);
          const f32x4 zq = {__uint_as_float(__builtin_amdgcn_readlane(zu, 0)), __uint_as_float(__builtin_amdgcn_readlane(zu, 16)),
                            __uint_as_float(__builtin_amdgcn_readlane(zu, 32)), __uint_as_float(__builtin_amdgcn_readlane(zu, 48))};
          if (lane == 0) store_quad(rs, gq + 768, zq);
        }
      } else {
        bar();
      }
    }
  }
  asm volatile(".Lmk_code_end:");
}

}

extern "C" void kernel_launch(void* const* d_in, const int* in_sizes, int n_in, void* d_out, int out_size, void* d_ws,
                              size_t ws_size, hipStream_t stream) {
  Params P;
  P.x0 = (const u16*)d_in[0];
  P.kc = (const u16*)d_in[1];
  P.vc = (const u16*)d_in[2];
  P.wq = (const u16*)d_in[3];
  P.wo = (const u16*)d_in[4];
  P.w1 = (const u16*)d_in[6];
  P.w3 = (const u16*)d_in[7];
  P.w2 = (const u16*)d_in[8];
  P.out = (u16*)d_out;
  P.ws = (u32*)d_ws;
  mega_kernel<<<NWG, NT, 0, stream>>>(P);
}
```

```cpp
#include <hip/hip_runtime.h>
#include <hip/hip_bf16.h>
#include <stdint.h>

namespace {

typedef unsigned int u32;
typedef unsigned short u16;
typedef unsigned long long u64;
using u32x2 = __attribute__((ext_vector_type(2))) unsigned;
using u32x4 = __attribute__((ext_vector_type(4))) unsigned;
using f32x2 = __attribute__((ext_vector_type(2))) float;
using f32x4 = __attribute__((ext_vector_type(4))) float;
using s16x4 = __attribute__((ext_vector_type(4))) short;
using bf16x2 = __attribute__((ext_vector_type(2))) __bf16;
using bf16x8 = __attribute__((ext_vector_type(8))) __bf16;
#define LAS __attribute__((address_space(3)))

constexpr int D = 640, H = 32, DH = 192, WIN = 4096, SK = 32768, F = 1536;
constexpr int NWG = 256, NT = 512;
constexpr u32 SPIN_CAP = 1u << 16;
constexpr float SCL = 0.07216878364870322f * 1.4426950408889634f;

#define T_O 40
#define T_YF 126
#define T_H 128
#define T_AN 35
#define PTHR 60
#define T_LAZY 60

#define A_E 116
#define A_Q (A_E + 80)
#define A_O (A_Q + 139)
#define A_Y (A_O + 91)
#define A_H (A_Y + 89)
#define A_F (A_H + 55)
#define A_K 75
#define SCHED(a) (anc + (a) + (((u32)((a) * A_K / 25) * asc) >> 11))


constexpr u32 SIDE = 8192, AREA = 16 * 16384;
constexpr u32 A_GO = 0;
constexpr u32 A_GQ = 4 * 16384;
constexpr u32 A_GH = A_GQ + 7168;
constexpr u32 A_GY = 8 * 16384;
constexpr u32 GO_SZ = 32768, GY_PAIR = 65536, GY_INST = 32768;
constexpr u32 WS_CLR = 4 * AREA;
constexpr u32 WS_PB = WS_CLR;
#define HBS 4352
constexpr u32 WS_GOF = WS_PB + (128 << 10), WS_HB = WS_GOF + 8 * HBS;
__device__ __forceinline__ u32 rp_line(int e, int r, int w) { return WS_PB + (u32)((e * 8 + r) * 8 + w) * 384u; }
#define Q1S 2048
constexpr u32 WS_GQ1 = WS_GOF + (192 << 10);
constexpr u32 WS_END = WS_GQ1 + 256 * 2048;
static_assert(WS_END <= (2u << 20) && WS_CLR == 256 * 4096, "layout");
__device__ __forceinline__ u32 go_slot(int w) { return (u32)(w >> 2) * 16384u + (u32)(w & 3) * 2048u; }
__device__ __forceinline__ u32 gy_row(int i) { return go_slot(i); }
__device__ __forceinline__ u32 gy_chunk(int u) {
  const int i = (u * 205) >> 11;
  return gy_row(i) + (u32)(u - 10 * i) * 16u;
}
__device__ __forceinline__ u32 st_line(int g) { return A_GH + 768u + (u32)(g >> 1) * 16384u + (u32)(g & 1) * 128u; }
__device__ __forceinline__ u32 gy_rep(int r) { return (u32)(r & 3) * AREA + (u32)(r >> 2) * SIDE; }

constexpr int L_ZERO = 0;
constexpr int L_CNT = 16;
constexpr int L_RI = 28;
constexpr int L_ONE = 32;
constexpr int L_TAG = 48;
constexpr int L_ST = 64;
constexpr int L_VOTE = L_ST;
constexpr int L_ANC = 192;
constexpr int L_CLK = 200;
constexpr int L_Q = 256;
constexpr int L_CS = 640;
constexpr int L_HB = L_CS + 768;
constexpr int L_ACT = L_HB + D * 2;
constexpr int L_RED = L_ACT + 2 * 384;
constexpr int L_P = L_RED + 32 * 144;
constexpr int P_W = 1280, P_U = 592, P_PAR = 288;
constexpr int OM_S = 400, OM_P = 200;
constexpr int L_OM = L_P + 8 * P_W;
constexpr int L_LN = L_OM;
constexpr int L_LAND = (L_OM + 8 * OM_S * 4 + 255) / 256 * 256;
constexpr int L_WQ = L_LAND + 8 * 2048;
constexpr int L_WB = L_WQ + 6 * 5120;
constexpr int L_W2 = L_WB + 768 * 64;
constexpr int L_PF = L_W2 + 192 * 64;
constexpr int L_TAB = L_PF + 256;
constexpr int L_TOTAL = L_TAB + 8 * 2048;
static_assert(L_LAND % 256 == 0 && L_P % 16 == 0 && L_WQ % 16 == 0 && L_HB % 16 == 0 && L_RED % 16 == 0, "alignment");


struct Params {
  const u16* x0;
  const u16* kc;
  const u16* vc;
  const u16* wq;
  const u16* wo;
  const u16* w1;
  const u16* w3;
  const u16* w2;
  u16* out;
  u32* ws;
};

__device__ __forceinline__ u32 cvt2(float lo, float hi) {
  return __builtin_bit_cast(u32, __builtin_convertvector(f32x2{lo, hi}, bf16x2));
}
__device__ __forceinline__ float bflo(u32 w) { return __uint_as_float(w << 16); }
__device__ __forceinline__ float bfhi(u32 w) { return __uint_as_float(w & 0xffff0000u); }
__device__ __forceinline__ float bf16r(float x) { return bflo(cvt2(x, x)); }

template <int CTRL>
__device__ __forceinline__ float dpp(float x) {
  return __builtin_bit_cast(float, __builtin_amdgcn_mov_dpp(__builtin_bit_cast(int, x), CTRL, 0xf, 0xf, true));
}
constexpr int XOR1 = 0xB1, XOR2 = 0x4E, XOR7 = 0x141, ROR8 = 0x128;
__device__ __forceinline__ float fmx(float a, float b) {
  float r;
  asm("v_max_f32 %0, %1, %2" : "=v"(r) : "v"(a), "v"(b));
  return r;
}
__device__ __forceinline__ float xrow16_max(float x) {
  auto s = __builtin_amdgcn_permlane16_swap(__float_as_uint(x), __float_as_uint(x), false, false);
  x = fmx(__uint_as_float(s[0]), __uint_as_float(s[1]));
  auto t = __builtin_amdgcn_permlane32_swap(__float_as_uint(x), __float_as_uint(x), false, false);
  return fmx(__uint_as_float(t[0]), __uint_as_float(t[1]));
}
__device__ __forceinline__ float xrow16_sum(float x) {
  auto s = __builtin_amdgcn_permlane16_swap(__float_as_uint(x), __float_as_uint(x), false, false);
  x = __uint_as_float(s[0]) + __uint_as_float(s[1]);
  auto t = __builtin_amdgcn_permlane32_swap(__float_as_uint(x), __float_as_uint(x), false, false);
  return __uint_as_float(t[0]) + __uint_as_float(t[1]);
}
__device__ __forceinline__ float sum8(float v) {
  v += dpp<XOR1>(v);
  v += dpp<XOR2>(v);
  v += dpp<XOR7>(v);
  return v;
}
__device__ __forceinline__ float max8(float v) {
  asm("s_nop 1\n\tv_max_f32_dpp %0, %0, %0 quad_perm:[1,0,3,2] row_mask:0xf bank_mask:0xf\n\t"
      "s_nop 1\n\tv_max_f32_dpp %0, %0, %0 quad_perm:[2,3,0,1] row_mask:0xf bank_mask:0xf\n\t"
      "s_nop 1\n\tv_max_f32_dpp %0, %0, %0 row_half_mirror row_mask:0xf bank_mask:0xf"
      : "+v"(v));
  return v;
}
__device__ __forceinline__ float max_ror8(float v) {
  asm("s_nop 1\n\tv_max_f32_dpp %0, %0, %0 row_ror:8 row_mask:0xf bank_mask:0xf" : "+v"(v));
  return v;
}
__device__ __forceinline__ f32x4 sum4x4(f32x4 v) {
  float a = v[0], b = v[1], c = v[2], d = v[3];
#define S4(CTL)                                               \
  "v_add_f32_dpp %0, %0, %0 " CTL " row_mask:0xf bank_mask:0xf\n\t" \
  "v_add_f32_dpp %1, %1, %1 " CTL " row_mask:0xf bank_mask:0xf\n\t" \
  "v_add_f32_dpp %2, %2, %2 " CTL " row_mask:0xf bank_mask:0xf\n\t" \
  "v_add_f32_dpp %3, %3, %3 " CTL " row_mask:0xf bank_mask:0xf\n\t"
  asm("s_nop 1\n\t" S4("quad_perm:[1,0,3,2]") S4("quad_perm:[2,3,0,1]") : "+v"(a), "+v"(b), "+v"(c), "+v"(d));
#undef S4
  return f32x4{a, b, c, d};
}
__device__ __forceinline__ float wave_sum(float v) {
  v = sum8(v);
  v += dpp<ROR8>(v);
  return xrow16_sum(v);
}
__device__ __forceinline__ float tsum8(f32x4 a) {
  auto s0 = __builtin_amdgcn_permlane32_swap(__float_as_uint(a[0]), __float_as_uint(a[1]), false, false);
  auto s1 = __builtin_amdgcn_permlane32_swap(__float_as_uint(a[2]), __float_as_uint(a[3]), false, false);
  const float r0 = __uint_as_float(s0[0]) + __uint_as_float(s0[1]);
  const float r1 = __uint_as_float(s1[0]) + __uint_as_float(s1[1]);
  auto t = __builtin_amdgcn_permlane16_swap(__float_as_uint(r0), __float_as_uint(r1), false, false);
  float r = __uint_as_float(t[0]) + __uint_as_float(t[1]);
  r += dpp<ROR8>(r);
  return r;
}

__device__ __forceinline__ u32 tagv(float v) { return __float_as_uint(v) | 1u; }
template <int AUX = 16>
__device__ __forceinline__ void store_quad(__amdgpu_buffer_rsrc_t rs, u32 off, f32x4 v) {
  const u32x4 s = {tagv(v[0]), tagv(v[1]), tagv(v[2]), tagv(v[3])};
  __builtin_amdgcn_raw_buffer_store_b128(s, rs, off, 0, AUX);
}
template <int N>
__device__ __forceinline__ void gpoll4(u32x4 rs, const u32 (&off)[N], f32x4 (&v)[N]) {
  bool need = true;
  u32 budget = SPIN_CAP;
#pragma unroll
  for (int k = 0; k < N; ++k) asm volatile("" : "=v"(v[k]));
#pragma unroll 1
  for (;;) {
    if (need) {
#pragma unroll
      for (int k = 0; k < N; ++k)
        asm volatile("buffer_load_dwordx4 %0, %1, %2, 0 offen sc1" : "+v"(v[k]) : "v"(off[k]), "s"(rs) : "memory");
    }
    asm volatile("s_waitcnt vmcnt(0)" ::: "memory");
    u32 a = 1u;
#pragma unroll
    for (int k = 0; k < N; ++k) {
      asm volatile("" : "+v"(v[k]));
      a &= __float_as_uint(v[k][0]) & __float_as_uint(v[k][1]) & __float_as_uint(v[k][2]) & __float_as_uint(v[k][3]);
    }
    need = a == 0u;
    if (__builtin_amdgcn_ballot_w64(need) == 0ull || budget == 0u) break;
    --budget;
    __builtin_amdgcn_s_sleep(1);
  }
}
__device__ __forceinline__ u32 now() { return (u32)__builtin_amdgcn_s_memtime(); }
__device__ __forceinline__ void wait_until(u32 t) {
#pragma unroll 1
  while ((int)(now() - t) < -160) __builtin_amdgcn_s_sleep(1);
#pragma unroll 1
  while ((int)(now() - t) < 0) {
  }
}
__device__ __forceinline__ u32 rtnow() { return (u32)__builtin_amdgcn_s_memrealtime(); }
__device__ __forceinline__ void wait_rt(u32 t) {
  u32 spins = 0;
#pragma unroll 1
  while ((int)(rtnow() - t) < -8 && ++spins < 400u) __builtin_amdgcn_s_sleep(1);
#pragma unroll 1
  while ((int)(rtnow() - t) < 0 && ++spins < 440u) {
  }
}
template <int BITS>
__device__ __forceinline__ u32 youngest(u32 t);
__device__ __forceinline__ void lds_wait(volatile LAS u32* p, u32 want) {
  u32 spins = 0;
#pragma unroll 1
  while (__builtin_amdgcn_readfirstlane(*p) != want) {
    if (++spins > (SPIN_CAP << 6)) break;
    __builtin_amdgcn_s_sleep(1);
  }
  asm volatile("" ::: "memory");
}
__device__ __forceinline__ u32 lds_wait_nz(volatile LAS u32* p) {
  u32 spins = 0, v;
#pragma unroll 1
  while ((v = __builtin_amdgcn_readfirstlane(*p)) == 0u) {
    if (++spins > (SPIN_CAP << 6)) break;
    __builtin_amdgcn_s_sleep(1);
  }
  asm volatile("" ::: "memory");
  return v;
}
template <int BITS>
__device__ __forceinline__ u32 youngest(u32 t) {
  const u32 nw = rtnow();
  const float age = (float)((nw - t) & ((1u << BITS) - 1u));
  return nw - (u32)__builtin_amdgcn_readfirstlane((int)-xrow16_max(max_ror8(max8(-age))));
}
__device__ __forceinline__ void stamp_poll(unsigned char* smem, u32x4 rq, u32 so) {
  u32 sv, budget = SPIN_CAP;
#pragma unroll 1
  for (;;) {
    asm volatile("buffer_load_dword %0, %1, %2, 0 offen sc1\n\ts_waitcnt vmcnt(0)" : "=&v"(sv) : "v"(so), "s"(rq) : "memory");
    if (__builtin_amdgcn_ballot_w64((sv & 1u) == 0u) == 0ull || budget == 0u) break;
    --budget;
    __builtin_amdgcn_s_sleep(1);
  }
  *(volatile LAS u32*)(smem + L_ANC + 4) = (u32)max8((float)(sv >> 25));
  *(volatile LAS u32*)(smem + L_ANC) = youngest<24>(sv >> 1) | 1u;
}
__device__ __forceinline__ void lds_add(unsigned char* smem, int off, u32 v) {
  __hip_atomic_fetch_add((LAS u32*)(smem + off), v, __ATOMIC_RELAXED, __HIP_MEMORY_SCOPE_WORKGROUP);
}

__device__ __forceinline__ int fresh(int t) {
  asm volatile("" : "+v"(t));
  return t;
}
__device__ __forceinline__ f32x4 zero4() {
  f32x2 a, b;
  asm volatile("v_mov_b64 %0, 0\n\tv_mov_b64 %1, 0" : "=v"(a), "=v"(b));
  return f32x4{a[0], a[1], b[0], b[1]};
}
__device__ __forceinline__ void bar() { asm volatile("s_waitcnt lgkmcnt(0)\n\ts_barrier" ::: "memory"); }

__device__ __forceinline__ u32x2 trread(unsigned char* smem, int off) {
  return __builtin_bit_cast(u32x2, __builtin_amdgcn_ds_read_tr16_b64_v4i16((LAS s16x4*)(smem + off)));
}
__device__ __forceinline__ u32x4 wfrag(unsigned char* smem, int lb, int off) {
  const u32x2 a = trread(smem, lb + off), b = trread(smem, lb + off + 256);
  return u32x4{a[0], a[1], b[0], b[1]};
}
__device__ __forceinline__ int wlane(int lane, int cb) {
  const int i = lane & 15, G = lane >> 4;
  return (8 * G + (i >> 2)) * 64 + (i & 3) * 8 + ((cb ^ (G & 1)) * 32);
}
__device__ __forceinline__ u32x4 afrag(unsigned char* smem, int a) { return *reinterpret_cast<const u32x4*>(smem + a); }
__device__ __forceinline__ f32x4 mfma32(u32x4 w, u32x4 a, f32x4 c) {
  return __builtin_amdgcn_mfma_f32_16x16x32_bf16(__builtin_bit_cast(bf16x8, w), __builtin_bit_cast(bf16x8, a), c, 0, 0, 0);
}

__device__ __forceinline__ void dma_piece(const u16* src, int ld, unsigned char* smem, int ldsoff, int p, int lane) {
  const int row = 16 * p + (lane >> 2), part = lane & 3;
  const int cb = (part >> 1) ^ (lane >> 5);
  const u32 voff = (u32)(row * ld + cb * 16 + (part & 1) * 8) * 2u;
  const u32 dst = (u32)(uintptr_t)(LAS unsigned char*)smem + (u32)(ldsoff + p * 1024);
  u32 keep;
  asm volatile("s_mov_b32 %0, m0\n\ts_mov_b32 m0, %2\n\ts_nop 2\n\tglobal_load_lds_dwordx4 %1, %3 nt\n\ts_mov_b32 m0, %0"
               : "=&s"(keep)
               : "v"(voff), "s"(dst), "s"(src)
               : "memory");
}

template <int CTRL>
__device__ __forceinline__ u32 dppu(u32 x) {
  return (u32)__builtin_amdgcn_mov_dpp((int)x, CTRL, 0xf, 0xf, true);
}
__device__ __forceinline__ float sum4(f32x4 d) { return (d[0] + d[1]) + (d[2] + d[3]); }
__device__ __forceinline__ u32 and4(u32x4 v) { return v[0] & v[1] & v[2] & v[3]; }
template <int AUX = 16>
__device__ __forceinline__ void store16(__amdgpu_buffer_rsrc_t rs, u32 off, u32x4 v) {
  __builtin_amdgcn_raw_buffer_store_b128(v, rs, off, 0, AUX);
}
#define POLL16(dst, voff, imm) \
  asm volatile("buffer_load_dwordx4 %0, %1, %2, 0 offen offset:%3 sc1" : "+v"(dst) : "v"(voff), "s"(rq), "n"(imm) : "memory")

struct LnS {
  float mb, ri;
};
__device__ __forceinline__ LnS ln_stats(float s1, float s2) {
#define S2(CTL)                                                     \
  "v_add_f32_dpp %0, %0, %0 " CTL " row_mask:0xf bank_mask:0xf\n\t" \
  "v_add_f32_dpp %1, %1, %1 " CTL " row_mask:0xf bank_mask:0xf\n\ts_nop 0\n\t"
  asm("s_nop 1\n\t" S2("quad_perm:[1,0,3,2]") S2("quad_perm:[2,3,0,1]") S2("row_half_mirror") : "+v"(s1), "+v"(s2));
#undef S2
  const float dm = s1 * (1.0f / D);
  const u32 mv = cvt2(dm, __builtin_fmaxf(fmaf(-dm, dm, s2 * (1.0f / D)), 0.f));
  LnS r;
  r.mb = bflo(mv);
  r.ri = __builtin_amdgcn_rcpf(bf16r(__builtin_amdgcn_sqrtf(bf16r(bfhi(mv) + 1e-5f))));
  return r;
}

struct G80 {
  u32x4 w[6];
  int ra, rb, rc;
};
__device__ __forceinline__ void g80_load(G80& a, unsigned char* smem, int lane, int wb, int xs, int land) {
  const int m = lane & 15, G = lane >> 4;
  const int l0 = wb + wlane(lane, 0), l1 = wb + wlane(lane, 1), bk = 4096 - (G >> 1) * 1024;
  a.w[0] = wfrag(smem, l0, 0);
  a.w[1] = wfrag(smem, l1, 0);
  a.w[2] = wfrag(smem, l0, 2048);
  a.w[3] = wfrag(smem, l1, 2048);
  a.w[4] = wfrag(smem, l0 + bk, 0);
  a.w[5] = wfrag(smem, l1 + bk, 0);
  const bool vec = m == 8 || (m < 8 && land >= 0);
  const int st = vec ? 64 : 0;
  const int sa = vec ? (m == 8 ? xs : land + 256 * m) + 16 * G : (m == 15 ? L_ONE : L_ZERO);
  const int sw = (vec && m < 8) ? (m & 3) * 64 : 0;
  a.ra = fresh(sa ^ sw);
  a.rb = fresh((sa + st) ^ sw);
  a.rc = fresh(G < 2 ? (sa + 2 * st) ^ sw : L_TAG);
}
template <bool WAIT = true>
__device__ __forceinline__ void dma_poll(u32x4 rq, u32 voff, u32 land, u64 ea, u64 eb) {
  u32 keep;
  asm volatile("s_mov_b32 %0, m0\n\ts_mov_b32 m0, %3\n\ts_mov_b64 exec, %4\n\t"
               "buffer_load_dwordx4 %1, %2, 0 offen sc1 lds\n\t"
               "s_mov_b64 exec, %5\n\t"
               "buffer_load_dwordx4 %1, %2, 0 offen offset:1024 sc1 lds\n\t"
               "s_mov_b64 exec, -1\n\ts_mov_b32 m0, %0"
               : "=&s"(keep)
               : "v"(voff), "s"(rq), "s"(land), "s"(ea), "s"(eb)
               : "memory");
  if (WAIT) asm volatile("s_waitcnt vmcnt(0)" ::: "memory");
}
template <bool X4, bool NT = false>
__device__ __forceinline__ void ctl_load(u32x4 rq, u32 voff, u32 lds) {
  u32 keep;
  if (NT)
    asm volatile("s_mov_b32 %0, m0\n\ts_mov_b32 m0, %3\n\ts_nop 2\n\tbuffer_load_dwordx4 %1, %2, 0 offen sc1 nt lds\n\ts_mov_b32 m0, %0"
                 : "=&s"(keep) : "v"(voff), "s"(rq), "s"(lds) : "memory");
  else if (X4)
    asm volatile("s_mov_b32 %0, m0\n\ts_mov_b32 m0, %3\n\ts_nop 2\n\tbuffer_load_dwordx4 %1, %2, 0 offen sc1 lds\n\ts_mov_b32 m0, %0"
                 : "=&s"(keep) : "v"(voff), "s"(rq), "s"(lds) : "memory");
  else
    asm volatile("s_mov_b32 %0, m0\n\ts_mov_b32 m0, %3\n\ts_nop 2\n\tbuffer_load_dword %1, %2, 0 offen sc1 lds\n\ts_mov_b32 m0, %0"
                 : "=&s"(keep) : "v"(voff), "s"(rq), "s"(lds) : "memory");
}
template <int UN>
__device__ __forceinline__ void retry_masks(u64 need, int lane, u64& ea, u64& eb) {
  u32 m = (u32)need | (u32)(need >> 32);
  m |= m >> 16;
  m = (m | (m >> 8)) & 0xffu;
  const int ln = fresh(lane);
  ea = __builtin_amdgcn_ballot_w64(((m >> (ln / UN)) & 1u) != 0u);
  eb = __builtin_amdgcn_ballot_w64(((m >> ((ln + 64) / UN)) & 1u) != 0u);
}
#define MF "v_mfma_f32_16x16x32_bf16 "
#define F0 "v[52:55]"
#define F1 "v[56:59]"
#define F2 "v[60:63]"
#define FCLOB "v52", "v53", "v54", "v55", "v56", "v57", "v58", "v59", "v60", "v61", "v62", "v63"
#define AND3(a, b, c) "v_bitop3_b32 %[t], " a ", " b ", " c " bitop3:0x80\n\t"
template <bool POLL>
__device__ __forceinline__ void g80_run(G80& a, unsigned char* smem, u32x4 rq, int lane, u32 voff, u32 land, float& r0,
                                        float& r1, float& z) {
  f32x4 d0, d1;
  if (POLL) {
    f32x4 d2;
    constexpr u64 USED = 0xff30ff0330ff03ffull;
    u64 ea = USED, eb = USED;
    u32 budget = SPIN_CAP;
#pragma unroll 1
    for (;;) {
      dma_poll(rq, voff, land, ea, eb);
      u64 need;
      u32 t;
      asm volatile("ds_read_b128 " F0 ", %[ra]\n\tds_read_b128 " F1 ", %[rb]\n\tds_read_b128 " F2 ", %[rc]\n\t"
                   "s_waitcnt lgkmcnt(2)\n\t"
                   MF "%[d2], " F0 ", " F0 ", 0\n\t" AND3("v52", "v53", "v54")
                   MF "%[d0], " F0 ", %[w0], 0\n\t" MF "%[d1], " F0 ", %[w1], 0\n\t"
                   "s_waitcnt lgkmcnt(1)\n\t"
                   MF "%[d2], " F1 ", " F1 ", %[d2]\n\t" AND3("%[t]", "v55", "v56")
                   MF "%[d0], " F1 ", %[w2], %[d0]\n\t" AND3("%[t]", "v57", "v58")
                   MF "%[d1], " F1 ", %[w3], %[d1]\n\t"
                   "s_waitcnt lgkmcnt(0)\n\t"
                   MF "%[d2], " F2 ", " F2 ", %[d2]\n\t" AND3("%[t]", "v59", "v60") AND3("%[t]", "v61", "v62")
                   MF "%[d0], " F2 ", %[w4], %[d0]\n\t" AND3("%[t]", "1", "v63")
                   "v_cmp_eq_u32 %[need], 0, %[t]\n\t"
                   MF "%[d1], " F2 ", %[w5], %[d1]"
                   : [d0] "=&v"(d0), [d1] "=&v"(d1), [d2] "=&v"(d2), [t] "=&v"(t), [need] "=&s"(need)
                   : [ra] "v"(a.ra), [rb] "v"(a.rb), [rc] "v"(a.rc), [w0] "v"(a.w[0]), [w1] "v"(a.w[1]), [w2] "v"(a.w[2]),
                     [w3] "v"(a.w[3]), [w4] "v"(a.w[4]), [w5] "v"(a.w[5])
                   : "memory", FCLOB);
      need &= 0x00ff00ff00ff00ffull;
      if (need == 0ull || budget == 0u) break;
      --budget;
      retry_masks<16>(need, lane, ea, eb);
      ea &= USED;
      eb &= USED;
    }
    asm volatile("s_nop 1" : "+v"(d2));
    z = sum4(d2);
    asm volatile("s_nop 1" : "+v"(z), "+v"(d0), "+v"(d1));
    z = (lane & 15) == 15 ? 0.f : z;
    z = sum8(z);
    z += dpp<ROR8>(z);
  } else {
    const u32x4 f0 = afrag(smem, a.ra), f1 = afrag(smem, a.rb), f2 = afrag(smem, a.rc);
    d0 = d1 = f32x4{0.f, 0.f, 0.f, 0.f};
    d0 = mfma32(f0, a.w[0], d0);
    d1 = mfma32(f0, a.w[1], d1);
    d0 = mfma32(f1, a.w[2], d0);
    d1 = mfma32(f1, a.w[3], d1);
    d0 = mfma32(f2, a.w[4], d0);
    d1 = mfma32(f2, a.w[5], d1);
  }
  r0 = sum4(d0);
  r1 = sum4(d1);
}
__device__ __forceinline__ f32x4 quad4(float r) { return f32x4{r, dpp<0x55>(r), dpp<0xAA>(r), dpp<0xFF>(r)}; }

__device__ __forceinline__ int q_addr(int d) {
  const int w = d >> 1;
  return L_Q + ((w & 1) * 48 + (w >> 1)) * 4 + (d & 1) * 2;
}

__device__ __forceinline__ void q1_finish(unsigned char* smem, int lane, int wave) {
  const int e = wave * 32 + (lane & 7) * 4 + ((lane >> 4) & 1) * 2 + (lane >> 5);
  asm volatile("" ::: "memory");
  const u32x4 v = afrag(smem, L_LAND + wave * 2048 + lane * 16);
  const float t = tsum8(f32x4{bflo(v[0]), bflo(v[1]), bflo(v[2]), bflo(v[3])});
  *reinterpret_cast<u16*>(smem + q_addr(e)) = (u16)cvt2(t, t);
  *reinterpret_cast<float*>(smem + L_CS + e * 4) = tsum8(f32x4{bfhi(v[0]), bfhi(v[1]), bfhi(v[2]), bfhi(v[3])}) * (-1.0f / D);
}
template <bool LDS>
__device__ __forceinline__ void x_update(unsigned char* smem, u32x4 rq, int lane, u32 src, int i) {
  const int l = lane < 40 ? lane : 0;
  const u32 off = src + 4 * l;
  u32 v[8];
  if (LDS) {
#pragma unroll
    for (int k = 0; k < 8; ++k) v[k] = *reinterpret_cast<const u32*>(smem + (off ^ ((k & 3) * 64)) + 256 * k);
  } else {
    u32 budget = SPIN_CAP;
#pragma unroll 1
    for (;;) {
#pragma unroll
      for (int k = 0; k < 8; ++k)
        asm volatile("buffer_load_dword %0, %1, %2, 0 offen offset:%3 sc1" : "=v"(v[k]) : "v"(off), "s"(rq), "n"(256 * k) : "memory");
      asm volatile("s_waitcnt vmcnt(0)"
                   : "+v"(v[0]), "+v"(v[1]), "+v"(v[2]), "+v"(v[3]), "+v"(v[4]), "+v"(v[5]), "+v"(v[6]), "+v"(v[7])::"memory");
      const u32 a = v[0] & v[1] & v[2] & v[3] & v[4] & v[5] & v[6] & v[7] & 1u;
      if (__builtin_amdgcn_ballot_w64(a == 0u) == 0ull || budget == 0u) break;
      --budget;
      __builtin_amdgcn_s_sleep(2);
    }
  }
  float lo = 0.f, hi = 0.f;
#pragma unroll
  for (int k = 0; k < 8; ++k) {
    lo += bflo(v[k]);
    hi += bfhi(v[k]);
  }
  u32* px = reinterpret_cast<u32*>(smem + L_HB + 160 * i + 4 * l);
  const u32 xo = *px, y = cvt2(lo, hi);
  if (lane < 40) *px = cvt2(bflo(xo) + bflo(y), bfhi(xo) + bfhi(y));
}
template <int N>
__device__ __forceinline__ void col_poll(u32x4 rq, const u32 (&off)[N], float (&y)[N]) {
  u32x2 v[N];
  u32 budget = SPIN_CAP;
#pragma unroll 1
  for (;;) {
    u32 a = 1u;
#pragma unroll
    for (int k = 0; k < N; ++k)
      asm volatile("buffer_load_dwordx2 %0, %1, %2, 0 offen sc1" : "=v"(v[k]) : "v"(off[k]), "s"(rq) : "memory");
    asm volatile("s_waitcnt vmcnt(0)" ::: "memory");
#pragma unroll
    for (int k = 0; k < N; ++k) {
      asm volatile("" : "+v"(v[k]));
      a &= v[k][0] & v[k][1];
    }
    if (__builtin_amdgcn_ballot_w64(a == 0u) == 0ull || budget == 0u) break;
    --budget;
  }
#pragma unroll
  for (int k = 0; k < N; ++k) y[k] = bf16r(tsum8(f32x4{bflo(v[k][0]), bfhi(v[k][0]), bflo(v[k][1]), bfhi(v[k][1])}));
}

#define SJ 31
#define DEQ 1.0f
#define PFB 16
#define AUXQ 18
#define UQ 1
#define UAD 3
#define UB 3
#define UL 5
#define UG 6
#define UK 7
#define UC 8
#define UW 8
#define UF 10
#define UX 11
#define TSTEP 236
#define TSV 232
#define USW 4
#define TSQ 192
#define WOFS 21
#define WBK 60
#define NINO 4
#define NINF 1
#define NINS 2
#define ORDO 0x543210u
#define ORDF 0x34210u
#define NPRE 2
#define NPF 2
#define TPF 220
#define T_FW 30
#define T_FD 12
#define WU0 3
#define WSP 2
#define WPC 100
#define NUN 1
static_assert(NINS + NPF <= 5 && NINS <= NINO && NINO + NPRE == 6 && NINF + NPF <= 5 && NPF <= NPRE, "counts");
static_assert(WU0 + NINO * WSP <= UX + 1, "pieces before the scores");
#define TPV 220
#define TSE TSV
constexpr int wcnt(int u) { return u < WU0 || u >= WU0 + NINO * WSP ? 0 : (20 + ((u - WU0) / WSP < NINF ? 12 : ((u - WU0) / WSP < NINS ? 2 : 0))) / WSP; }
#define PQ(u) ((((u) < USW ? TSTEP : TSV) * (9600 + wcnt(u) * WPC)) / 12800)
#define ROW0 ((wave * 8 + c) * 8)
#define GST 512
#define ROT h
#define PACE_ASM                                                                                                     \
  "1:\n\ts_memrealtime s[98:99]\n\ts_waitcnt lgkmcnt(0)\n\ts_lshl_b32 s98, s98, 3\n\ts_sub_i32 s98, s98, %[tn]\n\t"  \
  "s_cmp_lt_i32 s98, 0\n\ts_cbranch_scc0 2f\n\ts_sleep 1\n\ts_branch 1b\n2:\n\ts_add_i32 %[tn], %[tn], %[ts]\n\t"
__device__ __forceinline__ void pace(u32& tnext, u32 tstep) {
  asm volatile(PACE_ASM : [tn] "+s"(tnext) : [ts] "s"(tstep) : "scc", "s98", "s99");
}
constexpr int kreg(int i) { return i % 3 < 2 ? 64 + 8 * (i / 3) + 4 * (i % 3) : 128 + 4 * (i / 3); }
constexpr int vreg(int i) { return 160 + 24 * (i / 6) + 8 * (i % 3) + 4 * ((i / 3) & 1); }
constexpr int treg(int t) { return t < 24 ? kreg(t) : vreg(t - 24); }
template <int V>
struct Cn {
  static constexpr int v = V;
};
template <int A, int B, class Fn>
__device__ __forceinline__ void slots(Fn& f) {
  if constexpr (A < B) {
    f(Cn<A>{});
    slots<A + 1, B>(f);
  }
}
#define PRT 200
#define STR2(x) #x
#define STR(x) STR2(x)
#define PRIO_ASM                                                                                                     \
  "s_setprio 0\n\ts_cmpk_lt_i32 s98, " STR(PRT) "\n\ts_cbranch_scc1 4f\n\ts_setprio 1\n\ts_cmpk_lt_i32 s98, 3*" STR(PRT) "\n\t"  \
  "s_cbranch_scc1 4f\n\ts_setprio 2\n\ts_cmpk_lt_i32 s98, 6*" STR(PRT) "\n\ts_cbranch_scc1 4f\n\ts_setprio 3\n4:\n\t"
#define LD1(r) "global_load_dwordx4 v[%[" #r "]:%[" #r "]+3], %[p], %[sb] offset:"
#define BURST(u, voff, sbase)                                                                                       \
  asm volatile(PACE_ASM PRIO_ASM LD1(a) "0 nt\n\t" LD1(b) "128 nt\n\t" LD1(c) "256 nt\n\t"                                    \
               "s_waitcnt vmcnt(%[w]+2)\n\ts_cmp_gt_u32 %[ex], 1\n\ts_cbranch_scc1 3f\n\t"                          \
               "s_waitcnt vmcnt(%[w]+1)\n\ts_cmp_gt_u32 %[ex], 0\n\ts_cbranch_scc1 3f\n\ts_waitcnt vmcnt(%[w])\n3:"  \
               : [tn] "+s"(tnext)                                                                                   \
               : [a] "n"(treg(3 * (u))), [b] "n"(treg(3 * (u) + 1)), [c] "n"(treg(3 * (u) + 2)), [p] "v"(voff),     \
                 [sb] "s"(sbase), [ts] "s"(tstep), [ex] "s"(exa), [w] "n"(6)                                        \
               : "scc", "s98", "s99");                                                                              \
  exa = exl;                                                                                                        \
  exl = 0u
#define LOADW(t, voff, sbase)                                                                                       \
  asm volatile(PACE_ASM PRIO_ASM LD1(a) "%[im] nt\n\ts_waitcnt vmcnt(9)"                                                    \
               : [tn] "+s"(tnext)                                                                                   \
               : [a] "n"(treg(t)), [p] "v"(voff), [sb] "s"(sbase), [im] "n"(128 * ((t) % 3)), [ts] "s"(tstep)       \
               : "scc", "s98", "s99")

#define HS_WAIT(COND)                                                                                \
  _Pragma("unroll 1") for (;;) {                                                                     \
    gd = CTL[512 + lane0] - CTL[576 + lane0];                                                        \
    const u64 ch = __builtin_amdgcn_ballot_w64(gd != 0u) & 0x1ffull;                                 \
    if (ch > 0xffull || (COND) || tmo == 0u) break;                                                  \
    if ((tmo & 3u) == 0u && lane0 == 0 && cnt[1] >= 7u)                                              \
      __builtin_amdgcn_raw_buffer_store_b32(hb, rs, WS_HB + h * HBS + c * 4, 0, 16);                 \
    hb += 4u;                                                                                        \
    --tmo;                                                                                           \
    __builtin_amdgcn_s_sleep(3);                                                                     \
    ctl_load<false>(rq, hso, (u32)(smb + L_CTL + 2048));                                             \
    asm volatile("s_waitcnt vmcnt(0)" ::: "memory");                                                 \
  }

struct QKAddr {
  int qad[6], idxq, wa;
  f32x4 D0, D1;
};
struct PVAddr {
  int ra, idx, oa, sa, la, ma;
  u32 dst;
  f32x4 O0, O1, O2;
  u32x4 A0, A1, A2, A3;
};
template <int K>
__device__ __forceinline__ void att_table(unsigned char* smem, int lane, int wave, u32 go) {
  const int n = lane & 15, G = lane >> 4;
  const int seg = n & 1, pr = (n >> 1) & 1, tq = n >> 2, xy = G >> 1;
  const int qa = L_Q + (pr * 48 + seg * 8 + (G & 1) * 4) * 4;
  u32 t[16];
#pragma unroll
  for (int k = 0; k < 4; ++k) t[k] = tq == k ? qa + xy * 64 : L_ZERO;
#pragma unroll
  for (int k = 0; k < 2; ++k) t[4 + k] = tq == 2 * k + xy ? qa + 128 : L_ZERO;
  t[6] = 0x4444 + pr * 0xAAAA;
  t[7] = L_P + wave * P_W + ((n >> 1) & 1) * P_PAR + ((((G & 1) * 2 + (n >> 3)) * 4 + (n & 1) * 2 + (G >> 1)) * 16) + ((n >> 2) & 1) * 8;
  t[8] = L_P + wave * P_W + ((n >> 2) & 1) * P_U + (n >> 3) * P_PAR + G * 64;
  t[9] = (n & 3) * 0x5555;
  t[10] = L_OM + (wave * OM_S + (n >> 3) * OM_P + (n & 7) * 4 + (G & 1) * 32) * 4;
  t[11] = L_OM + (wave * OM_S + DH + G) * 4;
  t[12] = L_OM + ((lane & 3) * OM_S + (wave & 3) * 64 + (lane >> 3) * 4 + (lane & 4) * 8) * 4;
  t[13] = L_OM + ((lane & 3) * OM_S + DH + 4) * 4;
  const int d0 = wave * 64 + (lane >> 3) * 8;
  t[14] = (go + (wave < 3 ? (d0 >= 96 ? 2048 - 192 : 0) + d0 * 2 : lane * 2048 + 192)) & 0xffffu;
  t[15] = q_addr(wave * 32 + (lane & 7) * 4 + ((lane >> 4) & 1) * 2 + (lane >> 5));
  *reinterpret_cast<u32x4*>(smem + L_TAB + wave * 2048 + K * 1024 + lane * 16) =
      u32x4{t[8 * K] | (t[8 * K + 4] << 16), t[8 * K + 1] | (t[8 * K + 5] << 16), t[8 * K + 2] | (t[8 * K + 6] << 16),
            t[8 * K + 3] | (t[8 * K + 7] << 16)};
}
__device__ __forceinline__ void att_setup(QKAddr& r, PVAddr& v, unsigned char* smem, int lane, int wave, int& qw) {
  const u32x4 a = afrag(smem, L_TAB + wave * 2048 + lane * 16), b = afrag(smem, L_TAB + wave * 2048 + 1024 + lane * 16);
  r.D0 = zero4();
  r.D1 = zero4();
  qw = fresh(b[3] >> 16);
#pragma unroll
  for (int k = 0; k < 4; ++k) r.qad[k] = fresh(a[k] & 0xffffu);
  r.qad[4] = fresh(a[0] >> 16);
  r.qad[5] = fresh(a[1] >> 16);
  r.idxq = fresh(a[2] >> 16);
  r.wa = fresh(a[3] >> 16);
  v.ra = fresh(b[0] & 0xffffu);
  v.idx = fresh(b[1] & 0xffffu);
  v.oa = fresh(b[2] & 0xffffu);
  v.la = fresh(b[3] & 0xffffu);
  v.sa = fresh(b[0] >> 16);
  v.ma = fresh(b[1] >> 16);
  v.dst = fresh(b[2] >> 16);
  asm volatile("" : "+v"(r.D0), "+v"(r.D1));
}
#define SM "v_smfmac_f32_16x16x64_bf16 "
#define P_WRITE_READ(QA)                                                                                              \
  asm volatile("ds_write2_b32 %[wa], %[p], %[p] offset1:%[o1]\n\t"                                                    \
               "ds_read_b128 %[a0], %[ra]\n\tds_read_b128 %[a1], %[ra] offset:16\n\t"                                 \
               "ds_read_b128 %[a2], %[ra] offset:32\n\tds_read_b128 %[a3], %[ra] offset:48"                           \
               : [a0] "=&v"(pv.A0), [a1] "=&v"(pv.A1), [a2] "=&v"(pv.A2), [a3] "=&v"(pv.A3), "+v"(sk)                 \
               : [wa] "v"(QA.wa + smb), [p] "v"(cvt2(pend, 0.f)), [ra] "v"(pv.ra + smb), [o1] "n"(P_U / 4 + 1)        \
               : "memory");
#define QK_SOFTMAX(QA)                                                                                                \
  {                                                                                                                   \
    u32x4 A0, A1, A2, A3, A4, A5;                                                                                     \
    float scl;                                                                                                        \
    asm volatile("ds_read_b128 %[a0], %[q0]\n\tds_read_b128 %[a1], %[q1]\n\tds_read_b128 %[a2], %[q2]\n\t"            \
                 "ds_read_b128 %[a3], %[q3]\n\tds_read_b128 %[a4], %[q4]\n\tds_read_b128 %[a5], %[q5]"                \
                 : [a0] "=&v"(A0), [a1] "=&v"(A1), [a2] "=&v"(A2), [a3] "=&v"(A3), [a4] "=&v"(A4), [a5] "=&v"(A5)     \
                 : [q0] "v"(QA.qad[0] + smb), [q1] "v"(QA.qad[1] + smb), [q2] "v"(QA.qad[2] + smb),                   \
                   [q3] "v"(QA.qad[3] + smb), [q4] "v"(QA.qad[4] + smb), [q5] "v"(QA.qad[5] + smb)                    \
                 : "memory");                                                                                         \
    asm volatile("ds_read_b32 %[s], %[z] offset:%[ri]\n\t"                                                            \
                 "s_waitcnt lgkmcnt(6)\n\t" SM "%[d0], %[a0], v[64:71], %[ix]\n\t" SM "%[d1], %[a0], v[96:103], %[ix]\n\t"   \
                 "s_waitcnt lgkmcnt(5)\n\t" SM "%[d0], %[a1], v[72:79], %[ix]\n\t" SM "%[d1], %[a1], v[104:111], %[ix]\n\t"   \
                 "s_waitcnt lgkmcnt(4)\n\t" SM "%[d0], %[a2], v[80:87], %[ix]\n\t" SM "%[d1], %[a2], v[112:119], %[ix]\n\t"   \
                 "s_waitcnt lgkmcnt(3)\n\t" SM "%[d0], %[a3], v[88:95], %[ix]\n\t" SM "%[d1], %[a3], v[120:127], %[ix]\n\t"   \
                 "s_waitcnt lgkmcnt(2)\n\t" SM "%[d0], %[a4], v[128:135], %[ix]\n\t" SM "%[d1], %[a4], v[144:151], %[ix]\n\t"   \
                 "s_waitcnt lgkmcnt(0)\n\t" SM "%[d0], %[a5], v[136:143], %[ix]\n\t" SM "%[d1], %[a5], v[152:159], %[ix]\n\t"   \
                                      \
                 "ds_read_b128 %[a0], %[z]\n\tds_read_b128 %[a1], %[z]\n\tds_read_b128 %[a2], %[z]\n\ts_nop 4"        \
                 : [d0] "+v"(QA.D0), [d1] "+v"(QA.D1), [s] "=&v"(scl), [a0] "+v"(A0), [a1] "+v"(A1), [a2] "+v"(A2),   \
                   [a3] "+v"(A3), [a4] "+v"(A4), [a5] "+v"(A5)                                                        \
                 : [ix] "v"(QA.idxq), [z] "v"(smb), [ri] "n"(L_RI), "{v[64:95]}"(kv0), "{v[96:127]}"(kv1),           \
                   "{v[128:159]}"(kv2)                                                                                \
                 : "memory");                                                                                         \
    pv.O0 = __builtin_bit_cast(f32x4, A0);                                                                            \
    pv.O1 = __builtin_bit_cast(f32x4, A1);                                                                            \
    pv.O2 = __builtin_bit_cast(f32x4, A2);                                                                            \
    const f32x2 e0 = f32x2{QA.D0[0], QA.D0[1]} + f32x2{QA.D0[2], QA.D0[3]};                                           \
    const f32x2 e1 = f32x2{QA.D1[0], QA.D1[1]} + f32x2{QA.D1[2], QA.D1[3]};                                           \
    const float s0 = e0[0] + dpp<XOR1>(e0[1]), s1 = e1[1] + dpp<XOR1>(e1[0]);                                         \
    const float sc = ((lane0 & 1) ? s1 : s0) * scl;                                                                   \
    pend = __builtin_amdgcn_exp2f(sc);                                                                                \
    pmax = 0.f;                                                                                                       \
    float sk = sc;                                                                                                    \
    P_WRITE_READ(QA)                                                                                                  \
    const bool unsafe =                                                                                               \
        (__builtin_amdgcn_ballot_w64(!(sk <= 24.f)) != 0ull) | (__builtin_amdgcn_ballot_w64(sk >= -24.f) == 0ull);    \
    if (__builtin_expect(unsafe, 0)) {                                                                                \
      pmax = xrow16_max(max_ror8(max8(sk)));                                                                          \
      pend = __builtin_amdgcn_exp2f(sk - pmax);                                                                       \
      asm volatile("s_waitcnt lgkmcnt(0)" : "+v"(pv.A0), "+v"(pv.A1), "+v"(pv.A2), "+v"(pv.A3));                      \
      P_WRITE_READ(QA)                                                                                                \
      asm volatile("s_waitcnt lgkmcnt(0)" : "+v"(pv.A0), "+v"(pv.A1), "+v"(pv.A2), "+v"(pv.A3));                      \
    }                                                                                                                 \
  }

using u32x16 = __attribute__((ext_vector_type(16))) unsigned;
using u32x32 = __attribute__((ext_vector_type(32))) unsigned;
template <int OFF>
__device__ __forceinline__ u16* karg() {
  u64 p;
  asm volatile("s_load_dwordx2 %0, %1, %2\n\ts_waitcnt lgkmcnt(0)" : "=s"(p) : "s"(__builtin_amdgcn_kernarg_segment_ptr()), "n"(OFF));
  return (u16*)p;
}
template <class T>
__device__ __forceinline__ T* mkptr(u32 lo, u32 hi) {
  return (T*)(((u64)hi << 32) | lo);
}

__global__ __launch_bounds__(NT, 2) void mega_kernel(Params) {
  __shared__ __attribute__((aligned(16))) unsigned char smem[L_TOTAL];
  u32x16 ka;
  u32x4 kb4;
  u32 rtt;
  u64 ts64;
  {
    static_assert(AREA == 1 << 18 && A_GY == 1 << 17 && SIDE == 1 << 13, "clear addresses");
    asm volatile("s_load_dwordx16 s[36:51], %[kp], 0x0\n\ts_load_dwordx4 s[52:55], %[kp], 0x40\n\t"
                 "s_getpc_b64 s[58:59]\n.Lmk_pc:\n\t"
                 "s_mov_b32 s62, m0\n\ts_mov_b32 m0, %[dst]\n\t"
                 "s_cmpk_lt_u32 %[b], %[pfb]\n\ts_cselect_b32 s57, 0x140, 0\n\t"
                 "v_lshlrev_b32 v1, 7, %[tid]\n\tv_min_u32 v1, .Lmk_code_end-.Lmk_pc-4, v1\n\t"
                 "v_cmp_gt_u32 vcc, s57, %[tid]\n\ts_mov_b64 exec, vcc\n\t"
                 "global_load_lds_dword v1, s[58:59]\n\ts_mov_b64 exec, -1\n\t"
                 "v_readfirstlane_b32 s56, %[tid]\n\t"
                 "v_lshrrev_b32 v1, 2, %[tid]\n\tv_cmp_eq_u32 vcc, 3, v1\n\tv_mov_b32 v3, 0x3f803f80\n\ts_lshr_b32 s57, %[b], 5\n\t"
                 "v_cndmask_b32 v2, 0, 1, vcc\n\tv_cmp_eq_u32 vcc, 2, v1\n\tv_lshlrev_b32 v1, 2, %[tid]\n\ts_bfe_u32 s60, %[b], 0x20003\n\t"
                 "v_cndmask_b32 v2, v2, v3, vcc\n\tv_cmp_eq_u32 vcc, 7, %[tid]\n\tv_mov_b32 v3, %[scl]\n\t"
                 "v_min_u32 v1, 96, v1\n\tv_cndmask_b32 v2, v2, v3, vcc\n\t"
                 "ds_write_b32 v1, v2 offset:%[sm]\n\t"
                 "v_lshlrev_b32 v2, 4, %[tid]\n\tv_and_b32 v1, 0x3ff, v2\n\t"
                 "s_cmpk_ge_u32 s56, 448\n\ts_cbranch_scc1 1f\n\t"
                 "s_mul_i32 s57, s57, 0xf0000\n\ts_and_b32 s61, %[b], 7\n\ts_lshl2_add_u32 s60, s61, s60\n\t"
                 "v_bfe_u32 v3, %[tid], 2, 4\n\ts_mul_i32 s60, s60, 0x180\n\tv_mul_u32_u24 v3, 0x3000, v3\n\t"
                 "s_add_u32 s57, s57, s56\n\tv_bitop3_b32 v2, v2, %[tid], 32 bitop3:0x78\n\ts_add_u32 s57, s57, s60\n\t"
                 "v_and_or_b32 v3, v2, 48, v3\n\ts_mul_i32 s60, s56, 80\n\tv_add_u32 v3, s57, v3\n\t"
                 "s_add_u32 s60, s60, %[lwq]\n\ts_cmpk_eq_u32 s56, 384\n\ts_cselect_b64 vcc, -1, 0\n\ts_cselect_b32 m0, %[lhb], s60\n\t"
                 "v_cndmask_b32 v3, v3, v1, vcc\n\ts_mov_b32 %[rtt], 0\n\t"
                 "s_waitcnt lgkmcnt(0)\n\ts_cselect_b64 s[58:59], s[36:37], s[42:43]\n\ts_barrier\n\t"
                 "global_load_lds_dwordx4 v3, s[58:59] nt\n\t"
                 "s_memrealtime %[ts]\n\ts_branch 2f\n"
                 "1:\n\ts_lshl_b32 s57, s57, 17\n\ts_lshl_b32 s60, s60, 13\n\ts_and_b32 s61, %[b], 15\n\ts_or_b32 s57, s57, s60\n\t"
                 "s_lshl_b32 s61, s61, 9\n\t"
                 "v_mov_b64 v[4:5], 0\n\ts_or_b32 s57, s57, s61\n\tv_mov_b64 v[6:7], 0\n\t"
                 "v_or_b32 v1, s57, v1\n\tv_xor_b32 v2, 0x2000, v1\n\ts_mov_b32 exec_hi, 0\n\t"
                 "s_waitcnt lgkmcnt(0)\n\t"
                 "s_add_u32 s58, s54, 0x1fffff\n\ts_addc_u32 s59, s55, 0\n\ts_and_b32 s58, s58, 0xffe00000\n\t"
                 "global_store_dwordx4 v1, v[4:7], s[58:59] sc1\n\tglobal_store_dwordx4 v2, v[4:7], s[58:59] sc1\n\t"
                 "s_barrier\n\ts_mov_b64 exec, -1\n\ts_memrealtime %[ts]\n\t"
                 "s_waitcnt vmcnt(1)\n\ts_memtime s[58:59]\n\ts_waitcnt vmcnt(0)\n\ts_memtime s[60:61]\n\ts_waitcnt lgkmcnt(0)\n\t"
                 "s_sub_u32 %[rtt], s60, s58\n"
                 "2:\n\ts_mov_b32 m0, s62\n\ts_waitcnt lgkmcnt(0)"
                 : "=&{s[36:51]}"(ka), "=&{s[52:55]}"(kb4), [rtt] "=&s"(rtt), [ts] "=&s"(ts64)
                 : [kp] "s"(__builtin_amdgcn_kernarg_segment_ptr()), [tid] "v"(threadIdx.x), [b] "s"(blockIdx.x), [dst] "n"(L_PF),
                   [scl] "n"(__builtin_bit_cast(u32, SCL)), [sm] "n"(0), [lwq] "n"(L_WQ), [lhb] "n"(L_HB), [pfb] "n"(PFB)
                 : "memory", "vcc", "scc", "v1", "v2", "v3", "v4", "v5", "v6", "v7", "s56", "s57", "s58", "s59", "s60", "s61", "s62");
  }
  const u32 tstart = (u32)ts64;
  const int tid = threadIdx.x, lane0 = tid & 63;
  const int wave = __builtin_amdgcn_readfirstlane(tid >> 6);
  const int b = blockIdx.x;
  const int g = b & 7, j = b >> 3, hsub = j & 3, c = j >> 2, h = g * 4 + hsub;
  const bool isO = j < 20;
  const bool leader = b == 0 && wave == 6;
  const u32 fslot = (u32)((j & 1) * 16384 + ((j - 20) >> 1) * 128);
  u32 tmo = SPIN_CAP;
  volatile LAS u32* cnt = (volatile LAS u32*)(smem + L_CNT);
  Params P;
  P.x0 = mkptr<const u16>(ka[0], ka[1]);
  P.kc = mkptr<const u16>(ka[2], ka[3]);
  P.vc = mkptr<const u16>(ka[4], ka[5]);
  P.wq = mkptr<const u16>(ka[6], ka[7]);
  P.wo = mkptr<const u16>(ka[8], ka[9]);
  P.w1 = mkptr<const u16>(ka[10], ka[11]);
  P.w3 = mkptr<const u16>(ka[12], ka[13]);
  P.ws = mkptr<u32>(kb4[2], kb4[3]);
  u32* const wsb = (u32*)(((uintptr_t)P.ws + 0x1fffffu) & ~(uintptr_t)0x1fffffu);
  const __amdgpu_buffer_rsrc_t rs = __builtin_amdgcn_make_buffer_rsrc(wsb, 0, (int)WS_END, 0x00020000);
  const u32x4 rq = {(u32)(uintptr_t)wsb, (u32)((uintptr_t)wsb >> 32) & 0xffffu, WS_END, 0x00020000u};

  u32 anc = 0u, asc = 0u;
  u32 tref = 0u, ar = 0u;
  u32x32 kv0, kv1, kv2, kv3, kv4, kv5;
  asm volatile(""
               : "={v[64:95]}"(kv0), "={v[96:127]}"(kv1), "={v[128:159]}"(kv2), "={v[160:191]}"(kv3), "={v[192:223]}"(kv4),
                 "={v[224:255]}"(kv5));
  const int smb = (int)(uintptr_t)(LAS unsigned char*)smem;
  const u32 gop = A_GO + go_slot(hsub * 2) + c * 256;
  u32 tstep = 4 * PQ(0);
  u32 tnext = tstart * 8u + 5 * TSQ + (u32)(hsub * PQ(0) + wave * WOFS);
  const size_t rowbase = (size_t)h * SK + (SK - WIN) + ROW0;
  const u16* vb = P.vc + rowbase * DH;
  const int rot = ROT;
  const u16* ws0 = isO ? P.wo + (size_t)(768 * g) * D + 32 * j : ((j & 1) ? P.w3 : P.w1) + 192 * g + 32 * ((j - 20) >> 1);
  const int nin = isO ? NINO : ((((uintptr_t)ws0 >> 7) & 7) == 3 ? NINS : NINF);
  const u32 word = (((uintptr_t)ws0 >> 6) & 1) ? (isO ? ORDO : ORDF) : 0x543210u;
  int wn = 0;
  u32 hb = 0u;
  u32 exa = 0u, exl = 0u;
  {
    constexpr int L_CTL = L_LAND + 6 * 2048;
    volatile LAS u32* const CTL = (volatile LAS u32*)(smem + L_CTL);
    u32 gd;
    const u32 lho = WS_HB + (lane0 >> 1) * HBS + (lane0 & 1) * 16;
    const u32 hso = lane0 < 8 ? WS_HB + (u32)h * HBS + lane0 * 4 : WS_GOF + g * HBS;
    if (wave < 6) {
      const u16* s0 = P.wq + (size_t)(80 * c) * (H * DH) + h * DH + wave * 32;
      u32 tq = tstart * 8u + TSQ + (u32)wave * (TSTEP / 8);
#pragma unroll 1
      for (int p = 1; p < 5; ++p) {
        pace(tq, TSQ);
        dma_piece(s0, H * DH, smem, L_WQ + wave * 5120, p, lane0);
      }
    } else if (wave == 6) {
      u32 keep;
      asm volatile("s_mov_b32 %0, m0\n\ts_mov_b32 m0, %3\n\ts_nop 2\n\tglobal_load_lds_dword %1, %2 offset:1024\n\ts_mov_b32 m0, %0"
                   : "=&s"(keep)
                   : "v"(lane0 * 4), "s"(P.x0), "s"((u32)(smb + L_HB))
                   : "memory");
      if (j == SJ) store16(rs, rp_line(0, lane0 >> 3, g) + (lane0 & 7) * 16, u32x4{tstart, tstart, tstart, tstart});
      if (leader) {
        ctl_load<true>(rq, lho, (u32)(smb + L_CTL));
        ctl_load<false>(rq, WS_GOF + (lane0 & 7) * HBS, (u32)(smb + L_CTL + 2560));
      }
      asm volatile("s_waitcnt vmcnt(0)" ::: "memory");
      const u32x4 lv = afrag(smem, L_HB + lane0 * 16);
      const u32 xb = *reinterpret_cast<const u32*>(smem + L_HB + 1024 + lane0 * 4);
      float v[10];
#pragma unroll
      for (int k = 0; k < 4; ++k) {
        v[2 * k] = bflo(lv[k]);
        v[2 * k + 1] = bfhi(lv[k]);
      }
      v[8] = bflo(xb);
      v[9] = bfhi(xb);
      float sm = 0.f;
#pragma unroll
      for (int k = 0; k < 10; ++k) sm += v[k];
      const float mean = wave_sum(sm) * (1.0f / D);
      float q = 0.f;
#pragma unroll
      for (int k = 0; k < 10; ++k) q = fmaf(v[k] - mean, v[k] - mean, q);
      const float var = wave_sum(q) * (1.0f / D);
      const float mb = bf16r(mean);
      const float ri = __builtin_amdgcn_rcpf(bf16r(__builtin_amdgcn_sqrtf(bf16r(bf16r(var) + 1e-5f))));
      u32 hp[5];
#pragma unroll
      for (int k = 0; k < 5; ++k) {
        const u32 cc = cvt2(v[2 * k] - mb, v[2 * k + 1] - mb);
        hp[k] = cvt2(bflo(cc) * ri, bfhi(cc) * ri);
      }
      *reinterpret_cast<u32x4*>(smem + L_LN + lane0 * 16) = u32x4{hp[0], hp[1], hp[2], hp[3]};
      *reinterpret_cast<u32*>(smem + L_LN + 1024 + lane0 * 4) = hp[4];
      asm volatile("s_waitcnt lgkmcnt(0)" ::: "memory");
      *(volatile LAS u32*)(smem + L_VOTE + 4) = 1u;
    } else {
      ctl_load<false>(rq, hso, (u32)(smb + L_CTL + 2304));
      hb = rtt > PTHR ? 1u << ((g >> 2) ^ (j & 1)) : 0u;
    }
    if (tid == 0) *(volatile LAS u32*)(smem + L_ANC) = 0u;
    if (wave == 1) *(volatile LAS u32x2*)(smem + L_CLK) = u32x2{now(), rtnow()};
    *reinterpret_cast<u32x4*>(smem + L_P + wave * P_W + lane0 * 16) = u32x4{0u, 0u, 0u, 0u};
    *reinterpret_cast<u32x4*>(smem + (lane0 < 16 ? L_P + wave * P_W + 1024 + lane0 * 16 : L_OM + (wave * OM_S + OM_P + DH) * 4)) =
        u32x4{0u, 0u, 0u, 0u};
    const u16* kb = P.kc + rowbase * DH;
    const u32 vo = (lane0 >> 3) * 384 + 16 * (lane0 & 7);
    const u32 kw = (u32)(smb + L_W2 + wave * 1024 + lane0 * 16), kr = (u32)(smb + L_W2 + wave * 1024 + (lane0 & 15) * 64 + (lane0 >> 4) * 16);
#define KPERM(i)                                                                          \
  asm volatile("ds_write_b128 %[w], v[%[r0]:%[r1]]\n\tds_read_b128 v[%[r0]:%[r1]], %[r]" \
               :                                                                          \
               : [w] "v"(kw), [r] "v"(kr), [r0] "n"(kreg(i)), [r1] "n"(kreg(i) + 3)       \
               : "memory")
#define WNEXT                                                                                  \
  dma_piece(ws0, isO ? D : F, smem, L_WB, wave * (isO ? 6 : 5) + ((word >> (4 * wn)) & 7), fresh(lane0)); \
  wn = wn == (isO ? 5 : 4) ? 0 : wn + 1;
    const u32 qo = WS_GQ1 + (u32)(h * 8 + (lane0 >> 3)) * Q1S + wave * 128 + (lane0 & 7) * 16;
    auto step = [&](auto uc) __attribute__((always_inline)) {
      constexpr int u = decltype(uc)::v;
      if (u == UAD + 1) tstep += __builtin_amdgcn_readfirstlane(cnt[2]);
      BURST(u, vo, (u < 8 ? kb : vb) + ((u + rot) & 7) * GST * DH);
      if (wcnt(u + 1) != wcnt(u) || u + 1 == USW) {
        tnext += hsub * (PQ(u + 1) - PQ(u));
        tstep += 4 * (PQ(u + 1) - PQ(u));
      }
      if (u >= 2 && u < 10) {
        KPERM((3 * u + 18) % 24);
        KPERM((3 * u + 19) % 24);
        KPERM((3 * u + 20) % 24);
      }
      if (u == 0) {
        if (wave == 7) {
          ctl_load<false>(rq, rp_line(0, g, lane0 & 7), (u32)(smb + L_CTL + 2816));
          ++exa;
        }
        att_table<0>(smem, fresh(lane0), wave, gop);
        att_table<1>(smem, fresh(lane0), wave, gop);
      }
      if (u == UQ) {
        if (wave < 6) {
          const int lane = fresh(lane0), m = lane & 15, G = lane >> 4;
          const int q_l0 = L_WQ + wave * 5120 + wlane(lane, 0), q_l1 = L_WQ + wave * 5120 + wlane(lane, 1);
          const int q_bk = 4096 - (G >> 1) * 1024;
          const int st = m == 8 ? 64 : 0;
          const int sa = m == 8 ? L_LN + 160 * c + 16 * G : (m == 15 ? L_ONE : L_ZERO);
          const u32 q_dst = WS_GQ1 + (u32)(h * 8 + c) * Q1S + wave * 128 + (lane >> 5) * 64 + m * 4;
          asm volatile("" ::: "memory");
          lds_wait((volatile LAS u32*)(smem + L_VOTE + 4), 1u);
          const u32x4 f0 = afrag(smem, sa), f1 = afrag(smem, sa + st), f2 = afrag(smem, G < 2 ? sa + 2 * st : L_TAG);
          f32x4 q_d0 = {0.f, 0.f, 0.f, 0.f}, q_d1 = q_d0;
          q_d0 = mfma32(f0, wfrag(smem, q_l0, 0), q_d0);
          q_d1 = mfma32(f0, wfrag(smem, q_l1, 0), q_d1);
          q_d0 = mfma32(f1, wfrag(smem, q_l0, 2048), q_d0);
          q_d1 = mfma32(f1, wfrag(smem, q_l1, 2048), q_d1);
          q_d0 = mfma32(f2, wfrag(smem, q_l0 + q_bk, 0), q_d0);
          q_d1 = mfma32(f2, wfrag(smem, q_l1 + q_bk, 0), q_d1);
          auto sw = __builtin_amdgcn_permlane32_swap(__float_as_uint(sum4(q_d0)), __float_as_uint(sum4(q_d1)), false, false);
          auto sx = __builtin_amdgcn_permlane16_swap(sw[1], sw[1], false, false);
          const f32x4 qd = quad4(__uint_as_float(cvt2(__uint_as_float(sx[0]), __uint_as_float(sx[1]))));
          if ((lane0 & 19) == 0) store16<AUXQ>(rs, q_dst, __builtin_bit_cast(u32x4, qd));
          ++exa;
        }
      }
      if (u == UAD - 1 && wave == 7) {
        asm volatile("" ::: "memory");
        const int d = (int)(CTL[704 + lane0] - tstart);
        const float sd = sum8((u32)(d + 100) < 200u ? (float)d : 0.f);
        cnt[2] = (u32)(int)__builtin_rintf(sd * (DEQ / (15 - NUN - UAD)));
      }
      if ((u == UB && wave < 6) || (u == UB - 1 && wave == 7)) {
        asm volatile("" ::: "memory");
        if (u == UB - 1) {
          hb += (__builtin_amdgcn_readfirstlane(CTL[576 + c]) & ~3u) + 4u;
          *(volatile LAS u32*)(smem + L_VOTE + 8) = hb;
          hb += 4u;
        }
        u32 t0, t1, last;
        asm volatile("s_mov_b64 exec, 1\n\tv_mov_b32 %0, 1\n\tv_mov_b32 %1, %[sm]\n\tds_add_rtn_u32 %0, %1, %0 offset:%[c]\n\t"
                     "ds_read_b32 %1, %1 offset:%[v]\n\ts_waitcnt lgkmcnt(0)\n\tv_readfirstlane_b32 %2, %0\n\t"
                     "s_cmp_eq_u32 %2, 6\n\ts_cbranch_scc0 1f\n\tbuffer_store_dword %1, off, %[rq], %[o] sc1\n1:\n\t"
                     "s_cselect_b32 %2, 1, 0\n\ts_mov_b64 exec, -1"
                     : "=&v"(t0), "=&v"(t1), "=&s"(last)
                     : [sm] "s"(smb), [c] "n"(L_CNT + 4), [v] "n"(L_VOTE + 8), [rq] "s"(rq), [o] "s"(WS_HB + h * HBS + c * 4)
                     : "memory", "scc");
        exa += last;
        exl += last;
      }
      if (u == UL && wave == 7) {
        ctl_load<false>(rq, hso, (u32)(smb + L_CTL + 2048));
        ++exa;
        if (UL + 2 < UK) ++exl;
      }
      if (u == UG && leader) {
        ctl_load<true>(rq, lho, (u32)(smb + L_CTL + 1024));
        ++exa;
      }
      if (u == UK && wave == 7) {
        HS_WAIT((u32)ch == 0xffu)
        if (lane0 == 0) cnt[1] = 15u;
      }
      if (u == UC && wave < 6) {
        lds_wait(cnt + 1, 15u);
        ctl_load<true, true>(rq, qo, (u32)(smb + L_LAND + wave * 2048));
        ++exa;
      }
      if (u == UW && leader) {
        const u32x4 xa = afrag(smem, L_CTL + lane0 * 16);
        u32x4 lv;
#pragma unroll 1
        for (;;) {
          asm volatile("" ::: "memory");
          lv = afrag(smem, L_CTL + 1024 + lane0 * 16);
          const bool same = lv[0] == xa[0] || lv[1] == xa[1] || lv[2] == xa[2] || lv[3] == xa[3];
          if (__builtin_amdgcn_ballot_w64(same) == 0ull || tmo == 0u) break;
          --tmo;
          __builtin_amdgcn_s_sleep(4);
          ctl_load<true>(rq, lho, (u32)(smb + L_CTL + 1024));
          asm volatile("s_waitcnt vmcnt(0)" ::: "memory");
        }
        u32 nv = 256u;
#pragma unroll
        for (int e = 0; e < 4; ++e)
          nv += (u32)__builtin_popcountll(__builtin_amdgcn_ballot_w64((lv[e] & 2u) != 0u)) -
                (u32)__builtin_popcountll(__builtin_amdgcn_ballot_w64((lv[e] & 1u) != 0u));
        if (lane0 < 8) __builtin_amdgcn_raw_buffer_store_b32(CTL[640 + lane0] + (nv > 256u ? 2u : 1u), rs, WS_GOF + lane0 * HBS, 0, 16);
        ++exa;
        ++exl;
      }
      if (u == UX && wave == 7) {
        ctl_load<false>(rq, hso, (u32)(smb + L_CTL + 2048));
        ++exa;
      }
      if (u == UF) {
        if (wave < 6) {
          q1_finish(smem, fresh(lane0), wave);
          asm volatile("s_waitcnt lgkmcnt(0)" ::: "memory");
          if (lane0 == 0) lds_add(smem, L_CNT, 1u);
        }
      }
      if (wcnt(u) != 0 && (u - WU0) / WSP < nin && (wave & (WSP - 1)) == (u - WU0) % WSP) {
        u32 tw = tnext - WBK - hsub * PQ(u + 1);
        pace(tw, 0u);
        WNEXT
        ++exa;
        ++exl;
      }
    };
    static_assert(UQ >= 1 && UQ + 2 <= UB && UB < UL && UL + 2 <= UK && UK <= UC && UG + 2 <= UW && UC + 2 <= UF && UF <= UX && UX >= 9 && UX + 2 < 16 - NUN && UAD >= 3 && WU0 >= UB, "order");
    slots<0, UX + 1>(step);
  }

#pragma unroll 1
  for (int half = 0;; ++half) {
    {
      float pend, pmax;
      PVAddr pv;
      QKAddr qa;
      const int lane = fresh(lane0);
      const u32 off[1] = {ar + A_GQ + hsub * 16384 + (wave < 6 ? (lane >> 3) * 896 + wave * 128 + (lane & 7) * 16 : (lane & 7) * 896 + 768)};
      f32x4 qv[1];
      asm volatile("" : "=v"(qv[0]));
      if (half == 1) {
        anc = lds_wait_nz((volatile LAS u32*)(smem + L_ANC));
        asc = __builtin_amdgcn_readfirstlane(*(volatile LAS u32*)(smem + L_ANC + 4));
      } else {
        BURST(UX + 1, (u32)((lane >> 3) * 384 + 16 * (lane & 7)), vb + ((UX + 1 + rot) & 7) * GST * DH);
      }
      if (half == 1 && wave < 7) {
        wait_rt(SCHED(A_Q));
        POLL16(qv[0], off[0], 0);
      }
      int qw;
      att_setup(qa, pv, smem, lane, wave, qw);
      if (half == 0) {
        lds_wait(cnt, 6u);
      } else {
        if (wave < 7) {
          asm volatile("s_waitcnt vmcnt(0)" : "+v"(qv[0])::"memory");
          if (__builtin_expect(__builtin_amdgcn_ballot_w64((and4(__builtin_bit_cast(u32x4, qv[0])) & 1u) == 0u) != 0ull, 0))
            gpoll4<1>(rq, off, qv);
          if (wave < 6) {
            const float t = tsum8(qv[0]);
            *reinterpret_cast<u16*>(smem + qw) = (u16)cvt2(t, t);
          } else {
            const LnS ls = ln_stats(qv[0][3], qv[0][0] + qv[0][1] + qv[0][2]);
            *reinterpret_cast<float*>(smem + L_RI) = ls.ri * SCL;
          }
        }
        if (wave < 4) __builtin_amdgcn_s_setprio(2);
        bar();
      }
      QK_SOFTMAX(qa)
      if (half == 0) {
        const int lv_ = fresh(lane0);
        const u32 vo = (lv_ >> 3) * 384 + 16 * (lv_ & 7);
#pragma unroll
        for (int u = UX + 2; u < 16 - NUN; ++u) {
          BURST(u, vo, vb + ((u + rot) & 7) * GST * DH);
          if (u == UX + 2 && wave == 7) {
            constexpr int L_CTL = L_LAND + 6 * 2048;
            volatile LAS u32* const CTL = (volatile LAS u32*)(smem + L_CTL);
            const u32 hso = lane0 < 8 ? WS_HB + (u32)h * HBS + lane0 * 4 : WS_GOF + g * HBS;
            u32 gd;
            HS_WAIT(false)
            if (lane0 == 8) *(volatile LAS u32*)(smem + L_VOTE) = gd >> 1;
          }
        }
        tnext += wave * (TSE / 8 - WOFS) - hsub * PQ(15);
        tstep = TSE;
#pragma unroll
        for (int t = 48 - 3 * NUN; t < 48; ++t) {
          LOADW(t, vo, vb + ((t / 3 + rot) & 7) * GST * DH);
        }
        if (wave == 7) {
          const u32 a7 = (u32)(g & 3) * AREA + (((u32)(g >> 2) ^ __builtin_amdgcn_readfirstlane(*(volatile LAS u32*)(smem + L_VOTE))) & 1u) * SIDE +
                         (u32)(j >> 3) * 16384u + (u32)(j & 7) * 1024u;
          const u32x4 zz = __builtin_bit_cast(u32x4, zero4());
          store16(rs, lv_ * 16 + a7 + 2 * 16384, zz);
          store16(rs, lv_ * 16 + a7 + (j < 16 ? 6 : 8) * 16384, zz);
          store16(rs, lv_ * 16 + a7 + 12 * 16384, zz);
        }
#pragma unroll 1
        for (int p = 0; p < (isO ? NPRE : NPF); ++p) {
          pace(tnext, isO ? TPV : TPF);
          WNEXT
        }
      }
#define DA(CTL) "v_add_f32_dpp %[l], %[l], %[l] " CTL " row_mask:0xf bank_mask:0xf\n\t"
#define PW(W) "s_waitcnt vmcnt(6*" #W "+%[no]) lgkmcnt(" #W ")\n\ts_cbranch_scc0 1f\n\ts_waitcnt vmcnt(6*" #W "+%[nf])\n1:\n\t"
#define PX(o, v, a) SM "%[o" #o "], %[a" #a "], v[160+24*" #a "+8*" #o ":167+24*" #a "+8*" #o "], %[ix]\n\t"
      asm volatile("s_cmp_gt_i32 %[y], 19\n\t"
                   PW(3) PX(0, x, 0) DA("quad_perm:[1,0,3,2]") PX(1, y, 0) PX(2, z, 0)
                   PW(2) PX(0, x, 1) DA("quad_perm:[2,3,0,1]") PX(1, y, 1) PX(2, z, 1)
                   PW(1) PX(0, x, 2) DA("row_half_mirror") PX(1, y, 2) PX(2, z, 2)
                   PW(0) PX(0, x, 3) DA("row_ror:8") PX(1, y, 3) PX(2, z, 3) "s_setprio 0"
                   : [o0] "+v"(pv.O0), [o1] "+v"(pv.O1), [o2] "+v"(pv.O2), [a0] "+v"(pv.A0), [a1] "+v"(pv.A1),
                     [a2] "+v"(pv.A2), [a3] "+v"(pv.A3), [l] "+v"(pend)
                   : [ix] "v"(pv.idx), [no] "n"(NPRE), [nf] "n"(NPF), [y] "s"(j - 32 * half), "{v[160:191]}"(kv3),
                     "{v[192:223]}"(kv4), "{v[224:255]}"(kv5)
                   : "memory", "scc");
      asm volatile("ds_write2_b32 %[la], %[l], %[mx] offset1:4\n\ts_mov_b64 exec, %[m]\n\ts_nop 2\n\t"
                   "ds_write_b128 %[a], %[o0]\n\tds_write_b128 %[a], %[o1] offset:256\n\t"
                   "ds_write_b128 %[a], %[o2] offset:512\n\t"
                   "s_mov_b64 exec, -1\n\ts_waitcnt lgkmcnt(0)\n\ts_barrier"
                   :
                   : [m] "s"(0xff00ff0000ff00ffull), [a] "v"(pv.oa + smb), [o0] "v"(pv.O0), [o1] "v"(pv.O1), [o2] "v"(pv.O2),
                     [la] "v"(pv.la + smb), [l] "v"(pend), [mx] "v"(pmax)
                   : "memory");
      tref = now();
      if (half == 0) ar = (u32)(g & 3) * AREA + (((u32)(g >> 2) ^ __builtin_amdgcn_readfirstlane(*(volatile LAS u32*)(smem + L_VOTE))) & 1u) * SIDE;
      if (wave < 4) {
        const float* mp = reinterpret_cast<const float*>(smem + pv.ma);
        float m0 = mp[0], m1 = mp[4 * OM_S];
        f32x4 v = *reinterpret_cast<const f32x4*>(smem + pv.sa), v1 = *reinterpret_cast<const f32x4*>(smem + pv.sa + OM_P * 4);
        f32x4 u = *reinterpret_cast<const f32x4*>(smem + pv.sa + OM_S * 16);
        f32x4 u1 = *reinterpret_cast<const f32x4*>(smem + pv.sa + OM_S * 16 + OM_P * 4);
        asm volatile("" : "+v"(m0), "+v"(m1), "+v"(v), "+v"(v1), "+v"(u), "+v"(u1));
        v += v1;
        u += u1;
        float M = 0.f;
        if (__builtin_expect(__builtin_amdgcn_ballot_w64((__float_as_uint(m0) | __float_as_uint(m1)) != 0u) != 0ull, 0)) {
          M = fmx(m0, m1);
          M = fmx(M, dpp<XOR1>(M));
          M = fmx(M, dpp<XOR2>(M));
          v *= __builtin_amdgcn_exp2f(m0 - M);
          u *= __builtin_amdgcn_exp2f(m1 - M);
        }
        v = sum4x4(v + u);
        u32 p0 = cvt2(v[0], v[1]), p1 = cvt2(v[2], v[3]);
        if (wave == 3) {
          p0 = __float_as_uint(M);
          p1 = __float_as_uint(sum4(v));
        }
        p0 |= 1u;
        p1 |= 1u;
        const u32x4 pk = {p0, p1, dppu<XOR7>(p0), dppu<XOR7>(p1)};
        asm volatile("s_mov_b64 exec, %2\n\tbuffer_store_dwordx4 %0, %1, %3, %4 offen sc1\n\ts_mov_b64 exec, -1"
                     :
                     : "v"(pk), "v"(pv.dst), "s"(wave < 3 ? 0x0101010101010101ull : 0x3ull), "s"(rq), "s"(ar + (u32)half * GO_SZ)
                     : "memory");
      }
    }
    const u32 go = ar + A_GO + (u32)half * GO_SZ;
    const u32 gyw = A_GY + (u32)half * GY_PAIR;
    const u32 gy = gyw + ar;
    const u32 gh = go + A_GH;
    const int land = L_LAND + wave * 2048;
    const u32 landa = (u32)(uintptr_t)(LAS unsigned char*)smem + (u32)land;
    if (isO) {
      {
        const int lane = fresh(lane0);
        const u32 voff = fresh(go + go_slot(wave) + ((lane * 16) ^ ((lane >> 4) * 64)));
        if (half)
          wait_rt(SCHED(A_O));
        else
          wait_until(tref + 24 * T_O);
        dma_poll<false>(rq, voff, landa, ~0ull, ~0ull);
        const int n = lane & 15, G = lane >> 4;
        const int r0_ = land + (n & 7) * 256 + (n & 3) * 64;
        const int ra = fresh(n < 8 ? r0_ + G * 16 : r0_ ^ 192), rb = fresh((r0_ + G * 16) ^ 64), rc = fresh((r0_ + G * 16) ^ 128);
        const int l0 = fresh(L_WB + wave * 6144 + wlane(lane, 0)), l1 = fresh(L_WB + wave * 6144 + wlane(lane, 1));
        asm volatile("s_waitcnt vmcnt(2)" ::: "memory");
        u32x4 wf[6];
#pragma unroll
        for (int k = 0; k < 3; ++k) {
          wf[2 * k] = wfrag(smem, l0, 2048 * k);
          wf[2 * k + 1] = wfrag(smem, l1, 2048 * k);
        }
        const int rw = fresh(L_RED + wave * 144 + lane * 4);
        const int rr = fresh(L_RED + (lane & 3) * 144 + ((lane >> 2) & 7) * 16);
        const u32 dst = fresh(gyw + gy_rep((lane & 3) + (lane >> 5) * 4) + gy_chunk(4 * j + ((lane >> 3) & 3)) + g * 256);
        f32x4 d0, d1;
        float mc, lc, t1_;
        u32 budget = SPIN_CAP;
#pragma unroll 1
        for (;;) {
          u64 need;
          u32 t;
          asm volatile("s_waitcnt vmcnt(0)\n\t"
                       "ds_read_b128 " F0 ", %[ra]\n\tds_read_b128 " F1 ", %[rb]\n\tds_read_b128 " F2 ", %[rc]\n\t"
                       "s_waitcnt lgkmcnt(2)\n\t"
                       MF "%[d0], " F0 ", %[w0], 0\n\t"
                       "v_add_f32_dpp %[l], v53, v53 quad_perm:[1,0,3,2] row_mask:0xf bank_mask:0xf\n\t"
                       MF "%[d1], " F0 ", %[w1], 0\n\t" AND3("v52", "v53", "v54")
                       "s_waitcnt lgkmcnt(1)\n\t"
                       MF "%[d0], " F1 ", %[w2], %[d0]\n\t"
                       "v_add_f32_dpp %[l], %[l], %[l] quad_perm:[2,3,0,1] row_mask:0xf bank_mask:0xf\n\t" AND3("%[t]", "v55", "v56")
                       MF "%[d1], " F1 ", %[w3], %[d1]\n\t" AND3("%[t]", "v57", "v58")
                       "v_add_f32_dpp %[l], %[l], %[l] row_half_mirror row_mask:0xf bank_mask:0xf\n\t"
                       "s_waitcnt lgkmcnt(0)\n\t"
                       MF "%[d0], " F2 ", %[w4], %[d0]\n\t" AND3("%[t]", "v59", "v60") AND3("%[t]", "v61", "v62")
                       MF "%[d1], " F2 ", %[w5], %[d1]\n\t" AND3("%[t]", "1", "v63")
                       "v_rcp_f32 %[l], %[l]\n\tv_cmp_eq_u32 %[need], 0, %[t]"
                       : [d0] "=&v"(d0), [d1] "=&v"(d1), [t] "=&v"(t), [need] "=&s"(need), [l] "=&v"(lc), "=&{v52}"(mc), "=&{v53}"(t1_)
                       : [ra] "v"(ra), [rb] "v"(rb), [rc] "v"(rc), [w0] "v"(wf[0]), [w1] "v"(wf[1]), [w2] "v"(wf[2]), [w3] "v"(wf[3]),
                         [w4] "v"(wf[4]), [w5] "v"(wf[5])
                       : "memory", "v54", "v55", "v56", "v57", "v58", "v59", "v60", "v61", "v62", "v63");
          if (__builtin_expect(need == 0ull || budget == 0u, 1)) break;
          --budget;
          u64 ea, eb;
          retry_masks<16>(need, lane, ea, eb);
          dma_poll<false>(rq, voff, landa, ea, eb);
        }
        float rl = __uint_as_float(__builtin_amdgcn_readlane(__float_as_uint(lc), 8));
        asm volatile("s_nop 1" : "+v"(d0), "+v"(d1));
        if (__builtin_expect((__builtin_amdgcn_ballot_w64(__float_as_uint(mc) != 1u) & 0xff00ull) != 0ull, 0)) {
          const float fc = __builtin_amdgcn_exp2f(mc - max8(mc));
          const u32 sc = __float_as_uint(fc * __builtin_amdgcn_rcpf(sum8(fc * t1_)));
#pragma unroll
          for (int e = 0; e < 4; ++e) {
            const float se = __uint_as_float(G == 0 ? __builtin_amdgcn_readlane(sc, 8 + e) : __builtin_amdgcn_readlane(sc, 12 + e));
            d0[e] *= se;
            d1[e] *= se;
          }
          rl = 1.0f;
        }
        {
          auto sw = __builtin_amdgcn_permlane16_swap(__float_as_uint(sum4(d0)), __float_as_uint(sum4(d1)), false, false);
          const float r = (__uint_as_float(sw[0]) + __uint_as_float(sw[1])) * rl;
          if (lane < 32) *reinterpret_cast<float*>(smem + rw) = r;
        }
        bar();
        if (wave == 0) {
          const f32x4 y = sum4x4(*reinterpret_cast<const f32x4*>(smem + rr) + *reinterpret_cast<const f32x4*>(smem + rr + 576));
          const u32 p0 = cvt2(y[0], y[1]) | 1u, p1 = cvt2(y[2], y[3]) | 1u;
          const u32x4 pk = {p0, p1, dppu<XOR7>(p0), dppu<XOR7>(p1)};
          asm volatile("s_mov_b64 exec, %2\n\tbuffer_store_dwordx4 %0, %1, %3, 0 offen sc1\n\ts_mov_b64 exec, -1"
                       :
                       : "v"(pk), "v"(dst), "s"(0x0f0f0f0f0f0f0f0full), "s"(rq)
                       : "memory");
        }
      }
      tref = now();
      if (half == 0) {
        if (wave >= 1 && wave < 7) {
          const u16* s0 = karg<56>() + (size_t)(192 * g) * D + 32 * j;
#pragma unroll
          for (int p = 0; p < 2; ++p) dma_piece(s0, D, smem, L_W2, (wave - 1) * 2 + p, fresh(lane0));
          asm volatile("s_waitcnt vmcnt(0)" ::: "memory");
        }
        bar();
        if (wave == 4) {
          wait_until(tref + 24 * T_H);
          stamp_poll(smem, rq, ar + st_line(fresh(lane0) & 7));
        }
        if (wave == 5) {
          wait_until(tref + 24 * T_LAZY);
          x_update<false>(smem, rq, fresh(lane0), gy + gy_row(c), c);
        }
      } else if (g == 0 && wave == 2) {
        const int lane = fresh(lane0);
        const int col = j * 32 + (lane & 7) * 4 + ((lane >> 4) & 1) * 2 + (lane >> 5);
        u32 x0v, dummy;
        u16* const po = karg<64>() + col;
        asm volatile("global_load_ushort %0, %2, off\n\tglobal_load_ushort %1, %3, off\n\ts_waitcnt vmcnt(0)"
                     : "=&v"(x0v), "=&v"(dummy)
                     : "v"(karg<0>() + col), "v"(po)
                     : "memory");
        wait_until(tref + 24 * T_LAZY);
        const u32 o = gy + gy_chunk(4 * j + ((lane >> 1) & 3)) + (lane >> 3) * 256 + (lane & 1) * 8;
        const u32 off[3] = {o - GY_PAIR, o - GY_PAIR + GY_INST, o};
        float y[3];
        col_poll<3>(rq, off, y);
        const float x3 = bf16r(bf16r(bf16r(bflo(x0v) + y[0]) + y[1]) + y[2]);
        const u32 off2[1] = {o + GY_INST};
        wait_rt(SCHED(A_F));
        float y4[1];
        col_poll<1>(rq, off2, y4);
        const float x4 = x3 + y4[0];
        if ((lane & 8) == 0) asm volatile("global_store_short %0, %1, off" ::"v"(po), "v"(cvt2(x4, x4)) : "memory");
      }
      if (wave >= 6) {
        const int lane = fresh(lane0), wv = wave - 6;
        const int n = lane & 15, G = lane >> 4;
        const int l0 = fresh(L_W2 + wlane(lane, wv));
        u32x4 wf[6];
#pragma unroll
        for (int k = 0; k < 6; ++k) wf[k] = wfrag(smem, l0, 2048 * k);
        const int l48 = lane < 48 ? lane : 0;
        const u32 off[2] = {gh + (u32)l48 * 16, gh + (u32)l48 * 16 + 16384};
        const int aw = fresh(L_ACT + wv * 384 + l48 * 8);
        const int ab = fresh(L_ACT + wv * 384 + 16 * G);
        const u32 dst = fresh(gyw + GY_INST + gy_rep(n & 7) + gy_chunk(4 * j + wv * 2 + (G >> 1)) + g * 256);
        f32x4 v[2];
        if (half)
          wait_rt(SCHED(A_H));
        else
          wait_until(tref + 24 * T_H);
        gpoll4<2>(rq, off, v);
        {
          f32x4 a, u;
#pragma unroll
          for (int e = 0; e < 4; ++e) {
            a[e] = bfhi(__float_as_uint(v[0][e]));
            u[e] = bfhi(__float_as_uint(v[1][e]));
          }
          const f32x4 hh = a * u;
          *reinterpret_cast<u32x2*>(smem + aw) = u32x2{cvt2(hh[0], hh[1]), cvt2(hh[2], hh[3])};
        }
        u32x4 af[4];
#pragma unroll
        for (int k = 0; k < 4; ++k) af[k] = afrag(smem, ab + 64 * k);
        asm volatile("" : "+v"(af[0]), "+v"(af[1]), "+v"(af[2]), "+v"(af[3]));
        f32x4 d = {0.f, 0.f, 0.f, 0.f};
        d = mfma32(wf[0], af[0], d);
        d = mfma32(wf[1], af[1], d);
        asm volatile("" : "+v"(d)::"memory");
        af[0] = afrag(smem, ab + 256);
        af[1] = afrag(smem, ab + 320);
        d = mfma32(wf[2], af[2], d);
        d = mfma32(wf[3], af[3], d);
        d = mfma32(wf[4], af[0], d);
        d = mfma32(wf[5], af[1], d);
        const u32 p0 = cvt2(d[0], d[1]) | 1u, p1 = cvt2(d[2], d[3]) | 1u;
        auto s0 = __builtin_amdgcn_permlane16_swap(p0, p0, false, false);
        auto s1 = __builtin_amdgcn_permlane16_swap(p1, p1, false, false);
        if ((lane & (half ? 27 : 24)) == 0) store16(rs, dst, u32x4{s0[0], s1[0], s0[1], s1[1]});
      }
    } else {
      const int lane = fresh(lane0);
      const int n = lane & 15, G = lane >> 4;
      if (half == 1) {
        x_update<false>(smem, rq, lane, gy - GY_PAIR + GY_INST + gy_row(wave), wave);
      } else {
        if (wave == 1) {
          const u32x2 c0 = *(volatile LAS u32x2*)(smem + L_CLK);
          const float r = (float)(rtnow() - c0[1]) * (24.2f * 512.f) * __builtin_amdgcn_rcpf((float)(now() - c0[0])) - 511.5f;
          *(volatile LAS u32*)(smem + L_CLK) = ((u32)__builtin_fminf(__builtin_fmaxf(r, 0.f), 127.f) << 25) | 1u;
        }
#pragma unroll 1
        for (u32 tw = tref + 24 * T_FW; wn != 0; tw += 24 * T_FD) {
          wait_until(tw);
          WNEXT
        }
      }
      asm volatile("s_waitcnt vmcnt(0) lgkmcnt(0)" ::: "memory");
      G80 a;
      g80_load(a, smem, lane, L_WB + wave * 5120, L_HB + 160 * wave, land);
      const u32 voff = fresh(gy + gy_row(wave) + ((lane * 16) ^ ((lane >> 4) * 64)));
      const int rw = fresh(L_RED + n * 144 + wave * 16 + G * 4);
      const int sw_ = fresh(n == 0 ? L_ST + wave * 16 + G * 4 : L_ACT);
      float r0, r1, z;
      if (half)
        wait_rt(SCHED(A_Y));
      else
        wait_until(tref + 24 * T_YF);
      g80_run<true>(a, smem, rq, lane, voff, landa, r0, r1, z);
      *reinterpret_cast<float*>(smem + rw) = r0;
      *reinterpret_cast<float*>(smem + rw + 16 * 144) = r1;
      *reinterpret_cast<float*>(smem + sw_) = z;
      const int ln = fresh(lane0);
      const int rr = fresh(L_RED + (ln & 31) * 144 + (ln >> 5) * 64), rs_ = fresh(L_ST + 16 * (ln & 7));
      const u32 dst = fresh(gh + fslot + ln * 4);
      bar();
      tref = now();
      if (half == 0 && wave == 1 && j == 20 && ln < 8)
        __builtin_amdgcn_raw_buffer_store_b32(((rtnow() & 0xffffffu) << 1) | *(volatile LAS u32*)(smem + L_CLK), rs, gy_rep(ln) + st_line(g), 0, 16);
      if (wave == 0) {
        f32x4 st = *reinterpret_cast<const f32x4*>(smem + rs_);
        f32x4 q0 = *reinterpret_cast<const f32x4*>(smem + rr), q1 = *reinterpret_cast<const f32x4*>(smem + rr + 16);
        f32x4 q2 = *reinterpret_cast<const f32x4*>(smem + rr + 32), q3 = *reinterpret_cast<const f32x4*>(smem + rr + 48);
        asm volatile("" : "+v"(st), "+v"(q0), "+v"(q1), "+v"(q2), "+v"(q3));
        const LnS ls = ln_stats(st[3], st[0] + st[1] + st[2]);
        const f32x4 qs = (q0 + q1) + (q2 + q3);
        float r = fmaf(-ls.mb, qs[3], qs[0] + qs[1] + qs[2]);
        auto sw = __builtin_amdgcn_permlane32_swap(__float_as_uint(r), __float_as_uint(r), false, false);
        r = bf16r((__uint_as_float(sw[0]) + __uint_as_float(sw[1])) * ls.ri);
        if ((j & 1) == 0) r = bf16r(r * __builtin_amdgcn_rcpf(1.0f + __builtin_amdgcn_exp2f(r * -1.4426950408889634f)));
        const f32x4 qd = quad4(r);
        if ((ln & 35) == 0) store_quad(rs, dst, qd);
      }
      if (half == 0) {
        x_update<true>(smem, rq, fresh(lane0), land, wave);
        if (wave == 7) {
          wait_until(tref + 24 * T_AN);
          stamp_poll(smem, rq, ar + st_line(ln & 7));
        }
      }
    }
    if (half == 1) break;
    {
      if (wave < 6) {
        const int lane = fresh(lane0);
        const int n = lane & 15;
        G80 a;
        g80_load(a, smem, lane, L_WQ + wave * 5120, L_HB + 160 * c, land);
        const u32 voff = fresh(gy + GY_INST + gy_row(c) + ((lane * 16) ^ ((lane >> 4) * 64)));
        const float cc = *reinterpret_cast<const float*>(smem + L_CS + wave * 128 + (lane >> 5) * 64 + n * 4);
        const u32 gq = ar + A_GQ + hsub * 16384 + c * 896;
        const u32 dst = fresh(gq + wave * 128 + (lane >> 5) * 64 + n * 4);
        float r0, r1, z;
        bar();
        anc = __builtin_amdgcn_readfirstlane(*(volatile LAS u32*)(smem + L_ANC));
        asc = __builtin_amdgcn_readfirstlane(*(volatile LAS u32*)(smem + L_ANC + 4));
        wait_rt(SCHED(A_E));
        g80_run<true>(a, smem, rq, lane, voff, landa, r0, r1, z);
        auto sw = __builtin_amdgcn_permlane32_swap(__float_as_uint(r0), __float_as_uint(r1), false, false);
        const float sh = cc * __uint_as_float(__builtin_amdgcn_readlane(__float_as_uint(z), 48));
        float r = __uint_as_float(sw[0]) +
                  __uint_as_float((u32)__builtin_amdgcn_update_dpp((int)sw[1], (int)__float_as_uint(sh), 0xE4, 0xA, 0xf, false));
        auto sx = __builtin_amdgcn_permlane16_swap(__float_as_uint(r), __float_as_uint(r), false, false);
        r = __uint_as_float(sx[0]) + __uint_as_float(sx[1]);
        const f32x4 qd = quad4(r);
        if ((lane & 19) == 0) store_quad(rs, dst, qd);
        if (wave == 5) {
          const u32 zu = __float_as_uint(z);
          const f32x4 zq = {__uint_as_float(__builtin_amdgcn_readlane(zu, 0)), __uint_as_float(__builtin_amdgcn_readlane(zu, 16)),
                            __uint_as_float(__builtin_amdgcn_readlane(zu, 32)), __uint_as_float(__builtin_amdgcn_readlane(zu, 48))};
          if (lane == 0) store_quad(rs, gq + 768, zq);
        }
      } else {
        bar();
      }
    }
  }
  asm volatile(".Lmk_code_end:");
}

}

extern "C" void kernel_launch(void* const* d_in, const int* in_sizes, int n_in, void* d_out, int out_size, void* d_ws,
                              size_t ws_size, hipStream_t stream) {
  Params P;
  P.x0 = (const u16*)d_in[0];
  P.kc = (const u16*)d_in[1];
  P.vc = (const u16*)d_in[2];
  P.wq = (const u16*)d_in[3];
  P.wo = (const u16*)d_in[4];
  P.w1 = (const u16*)d_in[6];
  P.w3 = (const u16*)d_in[7];
  P.w2 = (const u16*)d_in[8];
  P.out = (u16*)d_out;
  P.ws = (u32*)d_ws;
  mega_kernel<<<NWG, NT, 0, stream>>>(P);
}
```

```cpp
#include <hip/hip_runtime.h>
#include <hip/hip_bf16.h>
#include <stdint.h>

namespace {

typedef unsigned int u32;
typedef unsigned short u16;
typedef unsigned long long u64;
using u32x2 = __attribute__((ext_vector_type(2))) unsigned;
using u32x4 = __attribute__((ext_vector_type(4))) unsigned;
using f32x2 = __attribute__((ext_vector_type(2))) float;
using f32x4 = __attribute__((ext_vector_type(4))) float;
using s16x4 = __attribute__((ext_vector_type(4))) short;
using bf16x2 = __attribute__((ext_vector_type(2))) __bf16;
using bf16x8 = __attribute__((ext_vector_type(8))) __bf16;
#define LAS __attribute__((address_space(3)))

constexpr int D = 640, H = 32, DH = 192, WIN = 4096, SK = 32768, F = 1536;
constexpr int NWG = 256, NT = 512;
constexpr u32 SPIN_CAP = 1u << 16;
constexpr float SCL = 0.07216878364870322f * 1.4426950408889634f;

#define T_O 40
#define T_YF 126
#define T_H 128
#define T_AN 30
#define T_AO 115
#define PTHR 60
#define T_LAZY 60

#define D_E 850
#define D_Q 510
#define T_O2 100
#define D_Y 1690
#define D_H 600
#define D_F 400
#define A_E D_E
#define A_Q (A_E + D_Q)
#define A_Y (A_Q + D_Y)
#define A_H (A_Y + D_H)
#define A_F (A_H + D_F)

constexpr u32 SIDE = 8192, AREA = 16 * 16384;
constexpr u32 A_GO = 0;
constexpr u32 A_GQ = 4 * 16384;
constexpr u32 A_GH = A_GQ + 7168;
constexpr u32 A_GY = 8 * 16384;
constexpr u32 GO_SZ = 32768, GY_PAIR = 65536, GY_INST = 32768;
constexpr u32 WS_CLR = 4 * AREA;
constexpr u32 WS_PB = WS_CLR;
#define HBS 4352
constexpr u32 WS_GOF = WS_PB + (128 << 10), WS_HB = WS_GOF + 8 * HBS;
__device__ __forceinline__ u32 rp_line(int e, int r, int w) { return WS_PB + (u32)((e * 8 + r) * 8 + w) * 384u; }
#define Q1S 2048
constexpr u32 WS_GQ1 = WS_GOF + (192 << 10);
constexpr u32 WS_END = WS_GQ1 + 256 * 2048;
static_assert(WS_END <= (2u << 20) && WS_CLR == 256 * 4096, "layout");
__device__ __forceinline__ u32 go_slot(int w) { return (u32)(w >> 2) * 16384u + (u32)(w & 3) * 2048u; }
__device__ __forceinline__ u32 gy_row(int i) { return go_slot(i); }
__device__ __forceinline__ u32 gy_chunk(int u) {
  const u32 i = __umul24(u, 205) >> 11;
  return (i >> 2) * 8192u + __umul24(i, 2048 - 160) + (u32)u * 16u;
}
__device__ __forceinline__ u32 st_line(int g) { return A_GH + 768u + (u32)(g >> 1) * 16384u + (u32)(g & 1) * 128u; }
__device__ __forceinline__ u32 gy_rep(int r) { return (u32)(r & 3) * AREA + (u32)(r >> 2) * SIDE; }

constexpr int L_ZERO = 0;
constexpr int L_CNT = 16;
constexpr int L_ONE = 32;
constexpr int L_TAG = 48;
constexpr int L_ST = 64;
constexpr int L_VOTE = L_ST;
constexpr int L_ANC = 192;
constexpr int L_CLK = 200;
constexpr int L_SUM = 208;
constexpr int L_AR = 224;
constexpr int L_Q = 256;
constexpr int L_CS = 640;
constexpr int L_HB = L_CS + 768;
constexpr int L_ACT = L_HB + D * 2;
constexpr int L_RED = L_ACT + 2 * 384;
constexpr int L_P = L_RED + 32 * 144;
constexpr int P_W = 1280, P_U = 592, P_PAR = 288;
constexpr int OM_S = 400, OM_P = 200;
constexpr int L_OM = L_P + 8 * P_W;
constexpr int L_LN = L_OM;
constexpr int L_LAND = (L_OM + 8 * OM_S * 4 + 255) / 256 * 256;
constexpr int L_WQ = L_LAND + 8 * 2048;
constexpr int L_WB = L_WQ + 6 * 5120;
constexpr int L_W2 = L_WB + 768 * 64;
constexpr int L_PF = L_W2 + 192 * 64;
constexpr int L_TAB = L_PF + 256;
constexpr int L_TOTAL = L_TAB + 8 * 2048;
static_assert(L_LAND % 256 == 0 && L_WB % 64 == 0 && L_P % 16 == 0 && L_WQ % 16 == 0 && L_HB % 16 == 0 && L_RED % 16 == 0, "alignment");


struct Params {
  const u16* x0;
  const u16* kc;
  const u16* vc;
  const u16* wq;
  const u16* wo;
  const u16* w1;
  const u16* w3;
  const u16* w2;
  u16* out;
  u32* ws;
};

__device__ __forceinline__ u32 cvt2(float lo, float hi) {
  return __builtin_bit_cast(u32, __builtin_convertvector(f32x2{lo, hi}, bf16x2));
}
__device__ __forceinline__ float bflo(u32 w) { return __uint_as_float(w << 16); }
__device__ __forceinline__ float bfhi(u32 w) { return __uint_as_float(w & 0xffff0000u); }
__device__ __forceinline__ float bf16r(float x) { return bflo(cvt2(x, x)); }

template <int CTRL>
__device__ __forceinline__ float dpp(float x) {
  return __builtin_bit_cast(float, __builtin_amdgcn_mov_dpp(__builtin_bit_cast(int, x), CTRL, 0xf, 0xf, true));
}
constexpr int XOR1 = 0xB1, XOR2 = 0x4E, XOR7 = 0x141, ROR8 = 0x128;
__device__ __forceinline__ float fmx(float a, float b) {
  float r;
  asm("v_max_f32 %0, %1, %2" : "=v"(r) : "v"(a), "v"(b));
  return r;
}
__device__ __forceinline__ float xrow16_max(float x) {
  auto s = __builtin_amdgcn_permlane16_swap(__float_as_uint(x), __float_as_uint(x), false, false);
  x = fmx(__uint_as_float(s[0]), __uint_as_float(s[1]));
  auto t = __builtin_amdgcn_permlane32_swap(__float_as_uint(x), __float_as_uint(x), false, false);
  return fmx(__uint_as_float(t[0]), __uint_as_float(t[1]));
}
__device__ __forceinline__ float xrow16_sum(float x) {
  auto s = __builtin_amdgcn_permlane16_swap(__float_as_uint(x), __float_as_uint(x), false, false);
  x = __uint_as_float(s[0]) + __uint_as_float(s[1]);
  auto t = __builtin_amdgcn_permlane32_swap(__float_as_uint(x), __float_as_uint(x), false, false);
  return __uint_as_float(t[0]) + __uint_as_float(t[1]);
}
__device__ __forceinline__ float sum8(float v) {
  v += dpp<XOR1>(v);
  v += dpp<XOR2>(v);
  v += dpp<XOR7>(v);
  return v;
}
__device__ __forceinline__ float max8(float v) {
  asm("s_nop 1\n\tv_max_f32_dpp %0, %0, %0 quad_perm:[1,0,3,2] row_mask:0xf bank_mask:0xf\n\t"
      "s_nop 1\n\tv_max_f32_dpp %0, %0, %0 quad_perm:[2,3,0,1] row_mask:0xf bank_mask:0xf\n\t"
      "s_nop 1\n\tv_max_f32_dpp %0, %0, %0 row_half_mirror row_mask:0xf bank_mask:0xf"
      : "+v"(v));
  return v;
}
__device__ __forceinline__ float max_ror8(float v) {
  asm("s_nop 1\n\tv_max_f32_dpp %0, %0, %0 row_ror:8 row_mask:0xf bank_mask:0xf" : "+v"(v));
  return v;
}
__device__ __forceinline__ float wave_sum(float v) {
  v = sum8(v);
  v += dpp<ROR8>(v);
  return xrow16_sum(v);
}
__device__ __forceinline__ float tsum8(f32x4 a) {
  auto s0 = __builtin_amdgcn_permlane32_swap(__float_as_uint(a[0]), __float_as_uint(a[1]), false, false);
  auto s1 = __builtin_amdgcn_permlane32_swap(__float_as_uint(a[2]), __float_as_uint(a[3]), false, false);
  const float r0 = __uint_as_float(s0[0]) + __uint_as_float(s0[1]);
  const float r1 = __uint_as_float(s1[0]) + __uint_as_float(s1[1]);
  auto t = __builtin_amdgcn_permlane16_swap(__float_as_uint(r0), __float_as_uint(r1), false, false);
  float r = __uint_as_float(t[0]) + __uint_as_float(t[1]);
  r += dpp<ROR8>(r);
  return r;
}

__device__ __forceinline__ u32 now() { return (u32)__builtin_amdgcn_s_memtime(); }
__device__ __forceinline__ void wait_until(u32 t) {
#pragma unroll 1
  while ((int)(now() - t) < -160) __builtin_amdgcn_s_sleep(1);
#pragma unroll 1
  while ((int)(now() - t) < 0) {
  }
}
__device__ __forceinline__ u32 rtnow() { return (u32)__builtin_amdgcn_s_memrealtime(); }
__device__ __forceinline__ void wait_tt(u32 a) {
  asm volatile("s_mov_b32 s80, %0\n\ts_call_b64 s[78:79], .Lmk_wait"
               :
               : "is"(a)
               : "s78", "s79", "s80", "s81", "s82", "s83", "s84", "s85", "s86", "s87", "s88", "s89", "s90", "s91", "s92", "s93", "s94",
                 "s95", "s96", "s97", "s98", "s99", "v62", "v63", "scc", "memory");
}
__device__ __forceinline__ void lds_wait(volatile LAS u32* p, u32 want) {
  u32 spins = 0;
#pragma unroll 1
  while (__builtin_amdgcn_readfirstlane(*p) != want) {
    if (++spins > (SPIN_CAP << 6)) break;
    __builtin_amdgcn_s_sleep(1);
  }
  asm volatile("" ::: "memory");
}
__device__ __forceinline__ void stamp_poll(unsigned char* smem, u32x4 rq, u32 so) {
  u32 sv, budget = SPIN_CAP;
#pragma unroll 1
  for (;;) {
    asm volatile("buffer_load_dword %0, %1, %2, 0 offen sc1\n\ts_waitcnt vmcnt(0)" : "=&v"(sv) : "v"(so), "s"(rq) : "memory");
    if (__builtin_amdgcn_ballot_w64((sv & 1u) == 0u) == 0ull || budget == 0u) break;
    --budget;
    __builtin_amdgcn_s_sleep(1);
  }
  const u32 nw = rtnow() * 8u + 32u;
  const float age = (float)((nw - (sv >> 1)) & 0xffffffu);
  *(volatile LAS u32x2*)(smem + L_ANC) = u32x2{nw - (u32)-max8(-age), (u32)max8((float)(sv >> 25))};
}
__device__ __forceinline__ void lds_add(unsigned char* smem, int off, u32 v) {
  __hip_atomic_fetch_add((LAS u32*)(smem + off), v, __ATOMIC_RELAXED, __HIP_MEMORY_SCOPE_WORKGROUP);
}

__device__ __forceinline__ int fresh(int t) {
  asm volatile("" : "+v"(t));
  return t;
}
__device__ __forceinline__ int fresh6(int t) {
  t = fresh(t);
  __builtin_assume((unsigned)t < 64u);
  return t;
}
__device__ __forceinline__ f32x4 zero4() {
  f32x2 a, b;
  asm volatile("v_mov_b64 %0, 0\n\tv_mov_b64 %1, 0" : "=v"(a), "=v"(b));
  return f32x4{a[0], a[1], b[0], b[1]};
}
__device__ __forceinline__ void bar() { asm volatile("s_waitcnt lgkmcnt(0)\n\ts_barrier" ::: "memory"); }

__device__ __forceinline__ u32x2 trread(unsigned char* smem, int off) {
  return __builtin_bit_cast(u32x2, __builtin_amdgcn_ds_read_tr16_b64_v4i16((LAS s16x4*)(smem + off)));
}
__device__ __forceinline__ u32x4 wfrag(unsigned char* smem, int lb, int off) {
  const u32x2 a = trread(smem, lb + off), b = trread(smem, lb + off + 256);
  return u32x4{a[0], a[1], b[0], b[1]};
}
__device__ __forceinline__ int wlane(int lane, int cb) {
  const int i = lane & 15, G = lane >> 4;
  return (8 * G + (i >> 2)) * 64 + (i & 3) * 8 + ((cb ^ (G & 1)) * 32);
}
__device__ __forceinline__ u32x4 afrag(unsigned char* smem, int a) { return *reinterpret_cast<const u32x4*>(smem + a); }
__device__ __forceinline__ f32x4 mfma32(u32x4 w, u32x4 a, f32x4 c) {
  return __builtin_amdgcn_mfma_f32_16x16x32_bf16(__builtin_bit_cast(bf16x8, w), __builtin_bit_cast(bf16x8, a), c, 0, 0, 0);
}

__device__ __forceinline__ void dma_piece(const u16* src, int ld, unsigned char* smem, int ldsoff, int p, int lane) {
  const int row = 16 * p + (lane >> 2), part = lane & 3;
  const int cb = (part >> 1) ^ (lane >> 5);
  const u32 voff = (u32)(row * ld + cb * 16 + (part & 1) * 8) * 2u;
  const u32 dst = (u32)(uintptr_t)(LAS unsigned char*)smem + (u32)(ldsoff + p * 1024);
  u32 keep;
  asm volatile("s_mov_b32 %0, m0\n\ts_mov_b32 m0, %2\n\ts_nop 2\n\tglobal_load_lds_dwordx4 %1, %3 nt\n\ts_mov_b32 m0, %0"
               : "=&s"(keep)
               : "v"(voff), "s"(dst), "s"(src)
               : "memory");
}

__device__ __forceinline__ float sum4(f32x4 d) { return (d[0] + d[1]) + (d[2] + d[3]); }
template <int AUX = 16>
__device__ __forceinline__ void store16(__amdgpu_buffer_rsrc_t rs, u32 off, u32x4 v) {
  __builtin_amdgcn_raw_buffer_store_b128(v, rs, off, 0, AUX);
}

struct G80 {
  u32x4 w[6];
  int ra, rb, rc;
};
__device__ __forceinline__ void g80_load(G80& a, unsigned char* smem, int lane, int wb, int xs, int land, int one) {
  const int m = lane & 15, G = lane >> 4;
  const int l0 = wb + wlane(lane, 0), l1 = wb + wlane(lane, 1), bk = 4096 - (G >> 1) * 1024;
  a.w[0] = wfrag(smem, l0, 0);
  a.w[1] = wfrag(smem, l1, 0);
  a.w[2] = wfrag(smem, l0, 2048);
  a.w[3] = wfrag(smem, l1, 2048);
  a.w[4] = wfrag(smem, l0 + bk, 0);
  a.w[5] = wfrag(smem, l1 + bk, 0);
  const bool vec = m <= 8;
  const int st = vec ? 64 : 0;
  const int sa = vec ? (m == 8 ? xs : land + 256 * m) + 16 * G : (m == 15 ? one : L_ZERO);
  const int sw = m < 8 ? (m & 3) * 64 : 0;
  a.ra = fresh(sa ^ sw);
  a.rb = fresh((sa + st) ^ sw);
  a.rc = fresh(G < 2 ? (sa + 2 * st) ^ sw : L_TAG);
}
template <bool X4, bool NT = false>
__device__ __forceinline__ void ctl_load(u32x4 rq, u32 voff, u32 lds) {
  u32 keep;
  if (NT)
    asm volatile("s_mov_b32 %0, m0\n\ts_mov_b32 m0, %3\n\ts_nop 2\n\tbuffer_load_dwordx4 %1, %2, 0 offen sc1 nt lds\n\ts_mov_b32 m0, %0"
                 : "=&s"(keep) : "v"(voff), "s"(rq), "s"(lds) : "memory");
  else if (X4)
    asm volatile("s_mov_b32 %0, m0\n\ts_mov_b32 m0, %3\n\ts_nop 2\n\tbuffer_load_dwordx4 %1, %2, 0 offen sc1 lds\n\ts_mov_b32 m0, %0"
                 : "=&s"(keep) : "v"(voff), "s"(rq), "s"(lds) : "memory");
  else
    asm volatile("s_mov_b32 %0, m0\n\ts_mov_b32 m0, %3\n\ts_nop 2\n\tbuffer_load_dword %1, %2, 0 offen sc1 lds\n\ts_mov_b32 m0, %0"
                 : "=&s"(keep) : "v"(voff), "s"(rq), "s"(lds) : "memory");
}
#define MF "v_mfma_f32_16x16x32_bf16 "
#define F0 "v[52:55]"
#define F1 "v[56:59]"
#define F2 "v[60:63]"
#define V52 "v52", "v53", "v54", "v55", "v56", "v57", "v58", "v59", "v60", "v61", "v62", "v63"
#define V40 "v40", "v41", "v42", "v43", "v44", "v45", "v46", "v47", "v48", "v49", "v50", "v51"
#define V36 "v36", "v37", "v38", "v39"
#define V32 "v32", "v33", "v34", "v35"
#define S80 "s80", "s81", "s82", "s83", "s84", "s85", "s86", "s87", "s88", "s89", "s90", "s91", "vcc", "scc", "memory"
#define AND3(a, b, c) "v_bitop3_b32 %[t], " a ", " b ", " c " bitop3:0x80\n\t"
#define ALL " row_mask:0xf bank_mask:0xf\n\t"
#define Q1 " quad_perm:[1,0,3,2]" ALL
#define Q2 " quad_perm:[2,3,0,1]" ALL
#define HM " row_half_mirror" ALL
#define R8 " row_ror:8" ALL
#define PK(a, b) "v_pk_add_f32 v[" #a ":" #a "+1], v[" #a ":" #a "+1], v[" #b ":" #b "+1]\n\t"
#define CV "v_cvt_pk_bf16_f32 "
constexpr u32 OOB = 0x80000000u;
#define POLL_HEAD                                                                                                     \
  "s_mov_b32 m0, %[land]\n\ts_mov_b32 s88, 0x10000\n\ts_mov_b32 s80, 0xff00ff\n\ts_mov_b32 s81, s80\n\t"               \
  "s_mov_b64 s[84:85], s[82:83]\n\ts_mov_b64 s[86:87], s[82:83]\n\t"
#define POLL_AGAIN                                                                                                    \
  "s_sub_u32 s88, s88, 1\n\ts_cbranch_scc1 3f\n\ts_or_b32 s84, vcc_lo, vcc_hi\n\ts_lshr_b32 s85, s84, 16\n\t"          \
  "s_or_b32 s84, s84, s85\n\ts_lshr_b32 s85, s84, 8\n\ts_or_b32 s84, s84, s85\n\t"                                     \
  "s_bitreplicate_b64_b32 s[84:85], s84\n\ts_bitreplicate_b64_b32 s[84:85], s84\n\t"                                   \
  "s_bitreplicate_b64_b32 s[84:85], s84\n\ts_bitreplicate_b64_b32 s[86:87], s85\n\t"                                   \
  "s_bitreplicate_b64_b32 s[84:85], s84\n\ts_and_b64 s[84:85], s[84:85], s[82:83]\n\t"                                 \
  "s_and_b64 s[86:87], s[86:87], s[82:83]\n\ts_branch 4f\n1:\n\ts_waitcnt lgkmcnt(0)\n\ts_barrier\n4:\n\t"             \
  "s_mov_b64 exec, s[84:85]\n\tbuffer_load_dwordx4 %[vo], %[rq], 0 offen sc1 lds\n\t"                                  \
  "s_mov_b64 exec, s[86:87]\n\tbuffer_load_dwordx4 %[vo], %[rq], 0 offen offset:1024 sc1 lds\n\ts_mov_b64 exec, -1\n\t"
#define POLL_READ \
  "s_waitcnt vmcnt(0)\n\tds_read_b128 " F0 ", %[ra]\n\tds_read_b128 " F1 ", %[rb]\n\tds_read_b128 " F2 ", %[rc]\n\t"
#define USED10 "s_mov_b32 s82, 0x30ff03ff\n\ts_mov_b32 s83, 0xff30ff03\n\t"
#define WIN6(a) [w0] "v"(a[0]), [w1] "v"(a[1]), [w2] "v"(a[2]), [w3] "v"(a[3]), [w4] "v"(a[4]), [w5] "v"(a[5])
__device__ __forceinline__ f32x4 quad4(float r) { return f32x4{r, dpp<0x55>(r), dpp<0xAA>(r), dpp<0xFF>(r)}; }

__device__ __forceinline__ int q_addr(int d) {
  const int w = d >> 1;
  return L_Q + ((w & 1) * 48 + (w >> 1)) * 4 + (d & 1) * 2;
}

__device__ __forceinline__ void q1_finish(unsigned char* smem, int lane, int wave) {
  const int e = wave * 32 + (lane & 7) * 4 + ((lane >> 4) & 1) * 2 + (lane >> 5);
  asm volatile("" ::: "memory");
  const u32x4 v = afrag(smem, L_LAND + wave * 2048 + lane * 16);
  const float t = tsum8(f32x4{bflo(v[0]), bflo(v[1]), bflo(v[2]), bflo(v[3])});
  *reinterpret_cast<u16*>(smem + q_addr(e)) = (u16)cvt2(t, t);
  *reinterpret_cast<float*>(smem + L_CS + e * 4) = tsum8(f32x4{bfhi(v[0]), bfhi(v[1]), bfhi(v[2]), bfhi(v[3])}) * (-1.0f / D);
}
template <bool LDS>
__device__ __forceinline__ void x_update(unsigned char* smem, u32x4 rq, int lane, u32 src, int i) {
  const int l = lane < 40 ? lane : 0;
  const u32 off = src + 4 * l;
  u32 v[8];
  if (LDS) {
#pragma unroll
    for (int k = 0; k < 8; ++k) v[k] = *reinterpret_cast<const u32*>(smem + (off ^ ((k & 3) * 64)) + 256 * k);
  } else {
    u32 budget = SPIN_CAP;
#pragma unroll 1
    for (;;) {
#pragma unroll
      for (int k = 0; k < 8; ++k)
        asm volatile("buffer_load_dword %0, %1, %2, 0 offen offset:%3 sc1" : "=v"(v[k]) : "v"(off), "s"(rq), "n"(256 * k) : "memory");
      asm volatile("s_waitcnt vmcnt(0)"
                   : "+v"(v[0]), "+v"(v[1]), "+v"(v[2]), "+v"(v[3]), "+v"(v[4]), "+v"(v[5]), "+v"(v[6]), "+v"(v[7])::"memory");
      const u32 a = v[0] & v[1] & v[2] & v[3] & v[4] & v[5] & v[6] & v[7] & 1u;
      if (__builtin_amdgcn_ballot_w64(a == 0u) == 0ull || budget == 0u) break;
      --budget;
      __builtin_amdgcn_s_sleep(2);
    }
  }
  float lo = 0.f, hi = 0.f;
#pragma unroll
  for (int k = 0; k < 8; ++k) {
    lo += bflo(v[k]);
    hi += bfhi(v[k]);
  }
  u32* px = reinterpret_cast<u32*>(smem + L_HB + 160 * i + 4 * l);
  const u32 xo = *px, y = cvt2(lo, hi);
  if (lane < 40) *px = cvt2(bflo(xo) + bflo(y), bfhi(xo) + bfhi(y));
}
template <int N>
__device__ __forceinline__ void col_poll(u32x4 rq, const u32 (&off)[N], float (&y)[N]) {
  u32x2 v[N];
  u32 budget = SPIN_CAP;
#pragma unroll 1
  for (;;) {
    u32 a = 1u;
#pragma unroll
    for (int k = 0; k < N; ++k)
      asm volatile("buffer_load_dwordx2 %0, %1, %2, 0 offen sc1" : "=v"(v[k]) : "v"(off[k]), "s"(rq) : "memory");
    asm volatile("s_waitcnt vmcnt(0)" ::: "memory");
#pragma unroll
    for (int k = 0; k < N; ++k) {
      asm volatile("" : "+v"(v[k]));
      a &= v[k][0] & v[k][1];
    }
    if (__builtin_amdgcn_ballot_w64(a == 0u) == 0ull || budget == 0u) break;
    --budget;
  }
#pragma unroll
  for (int k = 0; k < N; ++k) y[k] = bf16r(tsum8(f32x4{bflo(v[k][0]), bfhi(v[k][0]), bflo(v[k][1]), bfhi(v[k][1])}));
}

#define SJ 31
#define DWIN 250
#define DEQ 0.9f
#define PFB 16
#define AUXQ 18
#define UQ 1
#define UAD 4
#define UB 3
#define UL 5
#define UG 6
#define UK 7
#define UC 8
#define UW 8
#define UF 10
#define UX 11
#define TSTEP 236
#define TSV 232
#define USW 4
#define TSQ 192
#define WOFS 21
#define WBK 60
#define NINO 4
#define NINF 1
#define NINS 2
#define ORDO 0x543210u
#define ORDF 0x34210u
#define NPRE 2
#define NPF 2
#define TPF 220
#define T_FW 30
#define T_FD 12
#define WU0 3
#define WSP 2
#define WPC 100
#define NUN 1
static_assert(NINS + NPF <= 5 && NINS <= NINO && NINO + NPRE == 6 && NINF + NPF <= 5 && NPF == NPRE, "counts");
static_assert(WU0 + NINO * WSP <= UX + 1, "pieces before the scores");
#define TPV 220
#define TSE TSV
constexpr int wcnt(int u) { return u < WU0 || u >= WU0 + NINO * WSP ? 0 : (20 + ((u - WU0) / WSP < NINF ? 12 : ((u - WU0) / WSP < NINS ? 2 : 0))) / WSP; }
#define PQ(u) ((((u) < USW ? TSTEP : TSV) * (9600 + wcnt(u) * WPC)) / 12800)
#define ROW0 ((wave * 8 + c) * 8)
#define GST 512
#define ROT h
#define PACE_ASM                                                                                                     \
  "1:\n\ts_memrealtime s[98:99]\n\ts_waitcnt lgkmcnt(0)\n\ts_lshl_b32 s98, s98, 3\n\ts_sub_i32 s98, s98, %[tn]\n\t"  \
  "s_cmp_lt_i32 s98, 0\n\ts_cbranch_scc0 2f\n\ts_sleep 1\n\ts_branch 1b\n2:\n\ts_add_i32 %[tn], %[tn], %[ts]\n\t"
__device__ __forceinline__ void pace(u32& tnext, u32 tstep) {
  asm volatile(PACE_ASM : [tn] "+s"(tnext) : [ts] "s"(tstep) : "scc", "s98", "s99");
}
constexpr int kreg(int i) { return i % 3 < 2 ? 64 + 8 * (i / 3) + 4 * (i % 3) : 128 + 4 * (i / 3); }
constexpr int vreg(int i) { return 160 + 24 * (i / 6) + 8 * (i % 3) + 4 * ((i / 3) & 1); }
constexpr int treg(int t) { return t < 24 ? kreg(t) : vreg(t - 24); }
template <int V>
struct Cn {
  static constexpr int v = V;
};
template <int A, int B, class Fn>
__device__ __forceinline__ void slots(Fn& f) {
  if constexpr (A < B) {
    f(Cn<A>{});
    slots<A + 1, B>(f);
  }
}
#define PRT 200
#define STR2(x) #x
#define STR(x) STR2(x)
#define PRIO_ASM                                                                                                     \
  "s_setprio 0\n\ts_cmpk_lt_i32 s98, " STR(PRT) "\n\ts_cbranch_scc1 4f\n\ts_setprio 1\n\ts_cmpk_lt_i32 s98, 3*" STR(PRT) "\n\t"  \
  "s_cbranch_scc1 4f\n\ts_setprio 2\n\ts_cmpk_lt_i32 s98, 6*" STR(PRT) "\n\ts_cbranch_scc1 4f\n\ts_setprio 3\n4:\n\t"
#define LD1(r) "global_load_dwordx4 v[%[" #r "]:%[" #r "]+3], %[p], %[sb] offset:"
#define BURST(u, voff, sbase)                                                                                       \
  asm volatile(PACE_ASM PRIO_ASM LD1(a) "0 nt\n\t" LD1(b) "128 nt\n\t" LD1(c) "256 nt\n\t"                                    \
               "s_waitcnt vmcnt(%[w]+2)\n\ts_cmp_gt_u32 %[ex], 1\n\ts_cbranch_scc1 3f\n\t"                          \
               "s_waitcnt vmcnt(%[w]+1)\n\ts_cmp_gt_u32 %[ex], 0\n\ts_cbranch_scc1 3f\n\ts_waitcnt vmcnt(%[w])\n3:"  \
               : [tn] "+s"(tnext)                                                                                   \
               : [a] "n"(treg(3 * (u))), [b] "n"(treg(3 * (u) + 1)), [c] "n"(treg(3 * (u) + 2)), [p] "v"(voff),     \
                 [sb] "s"(sbase), [ts] "s"(tstep), [ex] "s"(exa), [w] "n"(6)                                        \
               : "scc", "s98", "s99");                                                                              \
  exa = exl;                                                                                                        \
  exl = 0u
#define LOADW(t, voff, sbase)                                                                                       \
  asm volatile(PACE_ASM PRIO_ASM LD1(a) "%[im] nt\n\ts_waitcnt vmcnt(9)"                                                    \
               : [tn] "+s"(tnext)                                                                                   \
               : [a] "n"(treg(t)), [p] "v"(voff), [sb] "s"(sbase), [im] "n"(128 * ((t) % 3)), [ts] "s"(tstep)       \
               : "scc", "s98", "s99")

#define HS_WAIT(COND)                                                                                \
  _Pragma("unroll 1") for (;;) {                                                                     \
    gd = CTL[512 + lane0] - CTL[576 + lane0];                                                        \
    const u64 ch = __builtin_amdgcn_ballot_w64(gd != 0u) & 0x1ffull;                                 \
    if (ch > 0xffull || (COND) || tmo == 0u) break;                                                  \
    if ((tmo & 3u) == 0u && lane0 == 0 && cnt[1] >= 7u)                                              \
      __builtin_amdgcn_raw_buffer_store_b32(hb, rs, WS_HB + h * HBS + c * 4, 0, 16);                 \
    hb += 4u;                                                                                        \
    --tmo;                                                                                           \
    __builtin_amdgcn_s_sleep(3);                                                                     \
    ctl_load<false>(rq, hso, (u32)(smb + L_CTL + 2048));                                             \
    asm volatile("s_waitcnt vmcnt(0)" ::: "memory");                                                 \
  }

struct Att {
  int qad[6], idxq, wa, ra, idx, oa, sa, la, ma, qw;
  u32 dst;
};
template <int K>
__device__ __forceinline__ void att_table(unsigned char* smem, int lane, int wave, u32 go) {
  const int n = lane & 15, G = lane >> 4;
  const int seg = n & 1, pr = (n >> 1) & 1, tq = n >> 2, xy = G >> 1;
  const int qa = L_Q + (pr * 48 + seg * 8 + (G & 1) * 4) * 4;
  u32 t[16];
#pragma unroll
  for (int k = 0; k < 4; ++k) t[k] = tq == k ? qa + xy * 64 : L_ZERO;
#pragma unroll
  for (int k = 0; k < 2; ++k) t[4 + k] = tq == 2 * k + xy ? qa + 128 : L_ZERO;
  t[6] = 0x4444 + pr * 0xAAAA;
  t[7] = L_P + wave * P_W + ((n >> 1) & 1) * P_PAR + ((((G & 1) * 2 + (n >> 3)) * 4 + (n & 1) * 2 + (G >> 1)) * 16) + ((n >> 2) & 1) * 8;
  t[8] = L_P + wave * P_W + ((n >> 2) & 1) * P_U + (n >> 3) * P_PAR + G * 64;
  t[9] = (n & 3) * 0x5555;
  t[10] = L_OM + (wave * OM_S + (n >> 3) * OM_P + (n & 7) * 4 + (G & 1) * 32) * 4;
  t[11] = L_OM + (wave * OM_S + DH + G) * 4;
  const int q = (G & 1) * 8 + (lane & 7);
  t[12] = L_OM + (((n >> 3) * 4 + (G & 2)) * OM_S + (wave & 3) * 64 + (q >> 1) * 4 + (q & 1) * 32) * 4;
  t[13] = L_OM + ((lane & 7) * OM_S + DH) * 4;
  const int d0 = wave * 64 + q * 4 + (G & 2);
  t[14] = (go + (wave < 3 ? (d0 >= 96 ? 2048 - 192 : 0) + d0 * 2 : lane * 2048 + 192)) & 0xffffu;
  t[15] = wave < 6 ? q_addr(wave * 32 + (lane & 7) * 4 + ((lane >> 4) & 1) * 2 + (lane >> 5)) : L_LAND + wave * 2048 + lane * 4;
  *reinterpret_cast<u32x4*>(smem + L_TAB + wave * 2048 + K * 1024 + lane * 16) =
      u32x4{t[8 * K] | (t[8 * K + 4] << 16), t[8 * K + 1] | (t[8 * K + 5] << 16), t[8 * K + 2] | (t[8 * K + 6] << 16),
            t[8 * K + 3] | (t[8 * K + 7] << 16)};
}
__device__ __forceinline__ void att_setup(Att& r, unsigned char* smem, int lane, int wave) {
  const u32x4 a = afrag(smem, L_TAB + wave * 2048 + lane * 16), b = afrag(smem, L_TAB + wave * 2048 + 1024 + lane * 16);
  r.qw = fresh(b[3] >> 16);
#pragma unroll
  for (int k = 0; k < 4; ++k) r.qad[k] = fresh(a[k] & 0xffffu);
  r.qad[4] = fresh(a[0] >> 16);
  r.qad[5] = fresh(a[1] >> 16);
  r.idxq = fresh(a[2] >> 16);
  r.wa = fresh(a[3] >> 16);
  r.ra = fresh(b[0] & 0xffffu);
  r.idx = fresh(b[1] & 0xffffu);
  r.oa = fresh(b[2] & 0xffffu);
  r.la = fresh(b[3] & 0xffffu);
  r.sa = fresh(b[0] >> 16);
  r.ma = fresh(b[1] >> 16);
  r.dst = fresh(((lane | 64) & (wave < 3 ? 8 : (wave == 3 ? 62 : 64))) ? OOB : b[2] >> 16);
}
#define SM "v_smfmac_f32_16x16x64_bf16 "
#define OEARLY 0xff
#define SAFEB "0x41c00000"

using u32x16 = __attribute__((ext_vector_type(16))) unsigned;
using u32x32 = __attribute__((ext_vector_type(32))) unsigned;
template <int OFF>
__device__ __forceinline__ u16* karg() {
  u64 p;
  asm volatile("s_load_dwordx2 %0, %1, %2\n\ts_waitcnt lgkmcnt(0)" : "=s"(p) : "s"(__builtin_amdgcn_kernarg_segment_ptr()), "n"(OFF));
  return (u16*)p;
}
template <class T>
__device__ __forceinline__ T* mkptr(u32 lo, u32 hi) {
  return (T*)(((u64)hi << 32) | lo);
}

__global__ __launch_bounds__(NT, 2) void mega_kernel(Params) {
  __shared__ __attribute__((aligned(16))) unsigned char smem[L_TOTAL];
  u32x16 ka;
  u32x4 kb4;
  u32 rtt;
  u64 ts64;
  {
    static_assert(AREA == 1 << 18 && A_GY == 1 << 17 && SIDE == 1 << 13, "clear addresses");
    asm volatile("s_load_dwordx16 s[36:51], %[kp], 0x0\n\ts_load_dwordx4 s[52:55], %[kp], 0x40\n\t"
                 "s_getpc_b64 s[58:59]\n.Lmk_pc:\n\t"
                 "s_mov_b32 s62, m0\n\ts_mov_b32 m0, %[dst]\n\t"
                 "s_cmpk_lt_u32 %[b], %[pfb]\n\ts_cselect_b32 s57, 0x140, 0\n\t"
                 "v_lshlrev_b32 v1, 7, %[tid]\n\tv_min_u32 v1, .Lmk_code_end-.Lmk_pc-4, v1\n\t"
                 "v_cmp_gt_u32 vcc, s57, %[tid]\n\ts_mov_b64 exec, vcc\n\t"
                 "global_load_lds_dword v1, s[58:59]\n\ts_mov_b64 exec, -1\n\t"
                 "v_readfirstlane_b32 s56, %[tid]\n\t"
                 "v_lshrrev_b32 v1, 2, %[tid]\n\tv_cmp_eq_u32 vcc, 3, v1\n\tv_mov_b32 v3, 0x3f803f80\n\ts_lshr_b32 s57, %[b], 5\n\t"
                 "v_cndmask_b32 v2, 0, 1, vcc\n\tv_cmp_eq_u32 vcc, 2, v1\n\tv_lshlrev_b32 v1, 2, %[tid]\n\ts_bfe_u32 s60, %[b], 0x20003\n\t"
                 "v_cndmask_b32 v2, v2, v3, vcc\n\tv_cmp_eq_u32 vcc, 52, %[tid]\n\tv_mov_b32 v3, %[scl]\n\t"
                 "v_min_u32 v1, 0xfc, v1\n\tv_cndmask_b32 v2, v2, v3, vcc\n\t"
                 "ds_write_b32 v1, v2 offset:%[sm]\n\t"
                 "v_lshlrev_b32 v2, 4, %[tid]\n\tv_and_b32 v1, 0x3ff, v2\n\t"
                 "s_cmpk_ge_u32 s56, 448\n\ts_cbranch_scc1 1f\n\t"
                 "s_mul_i32 s57, s57, 0xf0000\n\ts_and_b32 s61, %[b], 7\n\ts_lshl2_add_u32 s60, s61, s60\n\t"
                 "v_bfe_u32 v3, %[tid], 2, 4\n\ts_mul_i32 s60, s60, 0x180\n\tv_mul_u32_u24 v3, 0x3000, v3\n\t"
                 "s_add_u32 s57, s57, s56\n\tv_bitop3_b32 v2, v2, %[tid], 32 bitop3:0x78\n\ts_add_u32 s57, s57, s60\n\t"
                 "v_and_or_b32 v3, v2, 48, v3\n\ts_mul_i32 s60, s56, 80\n\tv_add_u32 v3, s57, v3\n\t"
                 "s_add_u32 s60, s60, %[lwq]\n\ts_cmpk_eq_u32 s56, 384\n\ts_cselect_b64 vcc, -1, 0\n\ts_cselect_b32 m0, %[lhb], s60\n\t"
                 "v_cndmask_b32 v3, v3, v1, vcc\n\ts_mov_b32 %[rtt], 0\n\t"
                 "s_waitcnt lgkmcnt(0)\n\ts_cselect_b64 s[58:59], s[36:37], s[42:43]\n\ts_barrier\n\t"
                 "global_load_lds_dwordx4 v3, s[58:59] nt\n\t"
                 "s_memrealtime %[ts]\n\ts_branch 2f\n"
                 "1:\n\ts_lshl_b32 s57, s57, 17\n\ts_lshl_b32 s60, s60, 13\n\ts_and_b32 s61, %[b], 15\n\ts_or_b32 s57, s57, s60\n\t"
                 "s_lshl_b32 s61, s61, 9\n\t"
                 "v_mov_b64 v[4:5], 0\n\ts_or_b32 s57, s57, s61\n\tv_mov_b64 v[6:7], 0\n\t"
                 "v_or_b32 v1, s57, v1\n\tv_xor_b32 v2, 0x2000, v1\n\ts_mov_b32 exec_hi, 0\n\t"
                 "s_waitcnt lgkmcnt(0)\n\t"
                 "s_add_u32 s58, s54, 0x1fffff\n\ts_addc_u32 s59, s55, 0\n\ts_and_b32 s58, s58, 0xffe00000\n\t"
                 "global_store_dwordx4 v1, v[4:7], s[58:59] sc1\n\tglobal_store_dwordx4 v2, v[4:7], s[58:59] sc1\n\t"
                 "s_barrier\n\ts_mov_b64 exec, -1\n\ts_memrealtime %[ts]\n\t"
                 "s_waitcnt vmcnt(1)\n\ts_memtime s[58:59]\n\ts_waitcnt vmcnt(0)\n\ts_memtime s[60:61]\n\ts_waitcnt lgkmcnt(0)\n\t"
                 "s_sub_u32 %[rtt], s60, s58\n"
                 "2:\n\ts_mov_b32 m0, s62\n\ts_waitcnt lgkmcnt(0)"
                 : "=&{s[36:51]}"(ka), "=&{s[52:55]}"(kb4), [rtt] "=&s"(rtt), [ts] "=&s"(ts64)
                 : [kp] "s"(__builtin_amdgcn_kernarg_segment_ptr()), [tid] "v"(threadIdx.x), [b] "s"(blockIdx.x), [dst] "n"(L_PF),
                   [scl] "n"(__builtin_bit_cast(u32, (float)D)), [sm] "n"(0), [lwq] "n"(L_WQ), [lhb] "n"(L_HB), [pfb] "n"(PFB)
                 : "memory", "vcc", "scc", "v1", "v2", "v3", "v4", "v5", "v6", "v7", "s56", "s57", "s58", "s59", "s60", "s61", "s62");
  }
  const u32 tstart = (u32)ts64;
  const int tid = threadIdx.x, lane0 = tid & 63;
  const int wave = __builtin_amdgcn_readfirstlane(tid >> 6);
  const int b = blockIdx.x;
  const int g = b & 7, j = b >> 3, hsub = j & 3, c = j >> 2, h = g * 4 + hsub;
  const bool isO = j < 20;
  const bool leader = b == 0 && wave == 6;
  const u32 fslot = (u32)((j & 1) * 16384 + ((j - 20) >> 1) * 128);
  u32 tmo = SPIN_CAP;
  volatile LAS u32* cnt = (volatile LAS u32*)(smem + L_CNT);
  Params P;
  P.x0 = mkptr<const u16>(ka[0], ka[1]);
  P.kc = mkptr<const u16>(ka[2], ka[3]);
  P.vc = mkptr<const u16>(ka[4], ka[5]);
  P.wq = mkptr<const u16>(ka[6], ka[7]);
  P.wo = mkptr<const u16>(ka[8], ka[9]);
  P.w1 = mkptr<const u16>(ka[10], ka[11]);
  P.w3 = mkptr<const u16>(ka[12], ka[13]);
  P.ws = mkptr<u32>(kb4[2], kb4[3]);
  u32* const wsb = (u32*)(((uintptr_t)P.ws + 0x1fffffu) & ~(uintptr_t)0x1fffffu);
  const __amdgpu_buffer_rsrc_t rs = __builtin_amdgcn_make_buffer_rsrc(wsb, 0, (int)WS_END, 0x00020000);
  const u32x4 rq = {(u32)(uintptr_t)wsb, (u32)((uintptr_t)wsb >> 32) & 0xffffu, WS_END, 0x00020000u};

  u32 to2;
  u32 tref = 0u, ar = 0u;
  u32x32 kv0, kv1, kv2, kv3, kv4, kv5;
  asm volatile(""
               : "={v[64:95]}"(kv0), "={v[96:127]}"(kv1), "={v[128:159]}"(kv2), "={v[160:191]}"(kv3), "={v[192:223]}"(kv4),
                 "={v[224:255]}"(kv5));
  const int smb = (int)(uintptr_t)(LAS unsigned char*)smem;
  const u32 gop = A_GO + go_slot(hsub * 2) + c * 256;
  u32 tstep = 4 * PQ(0);
  u32 tnext = tstart * 8u + 5 * TSQ + (u32)(hsub * PQ(0) + wave * WOFS);
  const size_t rowbase = (size_t)h * SK + (SK - WIN) + ROW0;
  const u16* vb = P.vc + rowbase * DH;
  const int rot = ROT;
  const u16* ws0 = isO ? P.wo + (size_t)(768 * g) * D + 32 * j : ((j & 1) ? P.w3 : P.w1) + 192 * g + 32 * ((j - 20) >> 1);
  const int nin = isO ? NINO : ((((uintptr_t)ws0 >> 7) & 7) == 3 ? NINS : NINF);
  const u32 word = (((uintptr_t)ws0 >> 6) & 1) ? (isO ? ORDO : ORDF) : 0x543210u;
  int wn = 0;
  u32 hb = 0u;
  u32 exa = 0u, exl = 0u;
  {
    constexpr int L_CTL = L_LAND + 6 * 2048;
    volatile LAS u32* const CTL = (volatile LAS u32*)(smem + L_CTL);
    u32 gd;
    const u32 lho = WS_HB + (lane0 >> 1) * HBS + (lane0 & 1) * 16;
    const u32 hso = lane0 < 8 ? WS_HB + (u32)h * HBS + lane0 * 4 : WS_GOF + g * HBS;
    if (wave < 6) {
      const u16* s0 = P.wq + (size_t)(80 * c) * (H * DH) + h * DH + wave * 32;
      u32 tq = tstart * 8u + TSQ + (u32)wave * (TSTEP / 8);
#pragma unroll 1
      for (int p = 1; p < 5; ++p) {
        pace(tq, TSQ);
        dma_piece(s0, H * DH, smem, L_WQ + wave * 5120, p, lane0);
      }
    } else if (wave == 6) {
      u32 keep;
      asm volatile("s_mov_b32 %0, m0\n\ts_mov_b32 m0, %3\n\ts_nop 2\n\tglobal_load_lds_dword %1, %2 offset:1024\n\ts_mov_b32 m0, %0"
                   : "=&s"(keep)
                   : "v"(lane0 * 4), "s"(P.x0), "s"((u32)(smb + L_HB))
                   : "memory");
      if (j == SJ) store16(rs, rp_line(0, lane0 >> 3, g) + (lane0 & 7) * 16, u32x4{tstart, tstart, tstart, tstart});
      if (leader) {
        ctl_load<true>(rq, lho, (u32)(smb + L_CTL));
        ctl_load<false>(rq, WS_GOF + (lane0 & 7) * HBS, (u32)(smb + L_CTL + 2560));
      }
      asm volatile("s_waitcnt vmcnt(0)" ::: "memory");
      const u32x4 lv = afrag(smem, L_HB + lane0 * 16);
      const u32 xb = *reinterpret_cast<const u32*>(smem + L_HB + 1024 + lane0 * 4);
      float v[10];
#pragma unroll
      for (int k = 0; k < 4; ++k) {
        v[2 * k] = bflo(lv[k]);
        v[2 * k + 1] = bfhi(lv[k]);
      }
      v[8] = bflo(xb);
      v[9] = bfhi(xb);
      float sm = 0.f;
#pragma unroll
      for (int k = 0; k < 10; ++k) sm += v[k];
      const float mean = wave_sum(sm) * (1.0f / D);
      float q = 0.f;
#pragma unroll
      for (int k = 0; k < 10; ++k) q = fmaf(v[k] - mean, v[k] - mean, q);
      const float var = wave_sum(q) * (1.0f / D);
      const float mb = bf16r(mean);
      const float ri = __builtin_amdgcn_rcpf(bf16r(__builtin_amdgcn_sqrtf(bf16r(bf16r(var) + 1e-5f))));
      u32 hp[5];
#pragma unroll
      for (int k = 0; k < 5; ++k) {
        const u32 cc = cvt2(v[2 * k] - mb, v[2 * k + 1] - mb);
        hp[k] = cvt2(bflo(cc) * ri, bfhi(cc) * ri);
      }
      *reinterpret_cast<u32x4*>(smem + L_LN + lane0 * 16) = u32x4{hp[0], hp[1], hp[2], hp[3]};
      *reinterpret_cast<u32*>(smem + L_LN + 1024 + lane0 * 4) = hp[4];
      asm volatile("s_waitcnt lgkmcnt(0)" ::: "memory");
      *(volatile LAS u32*)(smem + L_VOTE + 4) = 1u;
    } else {
      ctl_load<false>(rq, hso, (u32)(smb + L_CTL + 2304));
      hb = rtt > PTHR ? 1u << ((g >> 2) ^ (j & 1)) : 0u;
    }
    if (wave == 1) *(volatile LAS u32x2*)(smem + L_CLK) = u32x2{now(), rtnow()};
    *reinterpret_cast<u32x4*>(smem + L_P + wave * P_W + lane0 * 16) = u32x4{0u, 0u, 0u, 0u};
    *reinterpret_cast<u32x4*>(smem + (lane0 < 16 ? L_P + wave * P_W + 1024 + lane0 * 16 : L_OM + (wave * OM_S + OM_P + DH) * 4)) =
        u32x4{0u, 0u, 0u, 0u};
    const u16* kb = P.kc + rowbase * DH;
    const u32 vo = (lane0 >> 3) * 384 + 16 * (lane0 & 7);
    const u32 kw = (u32)(smb + L_W2 + wave * 1024 + lane0 * 16), kr = (u32)(smb + L_W2 + wave * 1024 + (lane0 & 15) * 64 + (lane0 >> 4) * 16);
#define KPERM(i)                                                                          \
  asm volatile("ds_write_b128 %[w], v[%[r0]:%[r1]]\n\tds_read_b128 v[%[r0]:%[r1]], %[r]" \
               :                                                                          \
               : [w] "v"(kw), [r] "v"(kr), [r0] "n"(kreg(i)), [r1] "n"(kreg(i) + 3)       \
               : "memory")
#define WNEXT                                                                                  \
  dma_piece(ws0, isO ? D : F, smem, L_WB, wave * (isO ? 6 : 5) + ((word >> (4 * wn)) & 7), fresh6(lane0)); \
  wn = wn == (isO ? 5 : 4) ? 0 : wn + 1;
    const u32 qo = WS_GQ1 + (u32)(h * 8 + (lane0 >> 3)) * Q1S + wave * 128 + (lane0 & 7) * 16;
    auto step = [&](auto uc) __attribute__((always_inline)) {
      constexpr int u = decltype(uc)::v;
      if (u == UAD + 1) tstep += __builtin_amdgcn_readfirstlane(cnt[2]);
      BURST(u, vo, (u < 8 ? kb : vb) + ((u + rot) & 7) * GST * DH);
      if (wcnt(u + 1) != wcnt(u) || u + 1 == USW) {
        tnext += hsub * (PQ(u + 1) - PQ(u));
        tstep += 4 * (PQ(u + 1) - PQ(u));
      }
      if (u >= 2 && u < 10) {
        KPERM((3 * u + 18) % 24);
        KPERM((3 * u + 19) % 24);
        KPERM((3 * u + 20) % 24);
      }
      if (u == UAD - 3 && wave == 7) {
        ctl_load<false>(rq, rp_line(0, g, lane0 & 7), (u32)(smb + L_CTL + 2816));
        ++exa;
      }
      if (u == 0) {
        att_table<0>(smem, fresh6(lane0), wave, gop);
        att_table<1>(smem, fresh6(lane0), wave, gop);
      }
      if (u == UQ) {
        if (wave < 6) {
          const int lane = fresh6(lane0), m = lane & 15, G = lane >> 4;
          const int q_l0 = L_WQ + wave * 5120 + wlane(lane, 0), q_l1 = L_WQ + wave * 5120 + wlane(lane, 1);
          const int q_bk = 4096 - (G >> 1) * 1024;
          const int st = m == 8 ? 64 : 0;
          const int sa = m == 8 ? L_LN + 160 * c + 16 * G : (m == 15 ? L_ONE : L_ZERO);
          const u32 q_dst = WS_GQ1 + (u32)(h * 8 + c) * Q1S + wave * 128 + (lane >> 5) * 64 + m * 4;
          asm volatile("" ::: "memory");
          lds_wait((volatile LAS u32*)(smem + L_VOTE + 4), 1u);
          const u32x4 f0 = afrag(smem, sa), f1 = afrag(smem, sa + st), f2 = afrag(smem, G < 2 ? sa + 2 * st : L_TAG);
          f32x4 q_d0 = {0.f, 0.f, 0.f, 0.f}, q_d1 = q_d0;
          q_d0 = mfma32(f0, wfrag(smem, q_l0, 0), q_d0);
          q_d1 = mfma32(f0, wfrag(smem, q_l1, 0), q_d1);
          q_d0 = mfma32(f1, wfrag(smem, q_l0, 2048), q_d0);
          q_d1 = mfma32(f1, wfrag(smem, q_l1, 2048), q_d1);
          q_d0 = mfma32(f2, wfrag(smem, q_l0 + q_bk, 0), q_d0);
          q_d1 = mfma32(f2, wfrag(smem, q_l1 + q_bk, 0), q_d1);
          auto sw = __builtin_amdgcn_permlane32_swap(__float_as_uint(sum4(q_d0)), __float_as_uint(sum4(q_d1)), false, false);
          auto sx = __builtin_amdgcn_permlane16_swap(sw[1], sw[1], false, false);
          const f32x4 qd = quad4(__uint_as_float(cvt2(__uint_as_float(sx[0]), __uint_as_float(sx[1]))));
          if ((lane0 & 19) == 0) store16<AUXQ>(rs, q_dst, __builtin_bit_cast(u32x4, qd));
          ++exa;
        }
      }
      if (u == UAD - 1 && wave == 7) {
        asm volatile("" ::: "memory");
        const int d = (int)(CTL[704 + lane0] - tstart);
        const float sd = sum8((u32)(d + DWIN) < 2u * DWIN ? (float)d : 0.f);
        cnt[2] = (u32)(int)__builtin_rintf(sd * (DEQ / (15 - NUN - UAD)));
      }
      if ((u == UB && wave < 6) || (u == UB - 1 && wave == 7)) {
        asm volatile("" ::: "memory");
        if (u == UB - 1) {
          hb += (__builtin_amdgcn_readfirstlane(CTL[576 + c]) & ~3u) + 4u;
          *(volatile LAS u32*)(smem + L_VOTE + 8) = hb;
          hb += 4u;
        }
        u32 t0, t1, last;
        asm volatile("s_mov_b64 exec, 1\n\tv_mov_b32 %0, 1\n\tv_mov_b32 %1, %[sm]\n\tds_add_rtn_u32 %0, %1, %0 offset:%[c]\n\t"
                     "ds_read_b32 %1, %1 offset:%[v]\n\ts_waitcnt lgkmcnt(0)\n\tv_readfirstlane_b32 %2, %0\n\t"
                     "s_cmp_eq_u32 %2, 6\n\ts_cbranch_scc0 1f\n\tbuffer_store_dword %1, off, %[rq], %[o] sc1\n1:\n\t"
                     "s_cselect_b32 %2, 1, 0\n\ts_mov_b64 exec, -1"
                     : "=&v"(t0), "=&v"(t1), "=&s"(last)
                     : [sm] "s"(smb), [c] "n"(L_CNT + 4), [v] "n"(L_VOTE + 8), [rq] "s"(rq), [o] "s"(WS_HB + h * HBS + c * 4)
                     : "memory", "scc");
        exa += last;
        exl += last;
      }
      if (u == UL && wave == 7) {
        ctl_load<false>(rq, hso, (u32)(smb + L_CTL + 2048));
        ++exa;
        if (UL + 2 < UK) ++exl;
      }
      if (u == UG && leader) {
        ctl_load<true>(rq, lho, (u32)(smb + L_CTL + 1024));
        ++exa;
      }
      if (u == UK && wave == 7) {
        HS_WAIT((u32)ch == 0xffu)
        if (lane0 == 0) cnt[1] = 15u;
      }
      if (u == UC && wave < 6) {
        lds_wait(cnt + 1, 15u);
        ctl_load<true, true>(rq, qo, (u32)(smb + L_LAND + wave * 2048));
        ++exa;
      }
      if (u == UW && leader) {
        const u32x4 xa = afrag(smem, L_CTL + lane0 * 16);
        u32x4 lv;
#pragma unroll 1
        for (;;) {
          asm volatile("" ::: "memory");
          lv = afrag(smem, L_CTL + 1024 + lane0 * 16);
          const bool same = lv[0] == xa[0] || lv[1] == xa[1] || lv[2] == xa[2] || lv[3] == xa[3];
          if (__builtin_amdgcn_ballot_w64(same) == 0ull || tmo == 0u) break;
          --tmo;
          __builtin_amdgcn_s_sleep(4);
          ctl_load<true>(rq, lho, (u32)(smb + L_CTL + 1024));
          asm volatile("s_waitcnt vmcnt(0)" ::: "memory");
        }
        u32 nv = 256u;
#pragma unroll
        for (int e = 0; e < 4; ++e)
          nv += (u32)__builtin_popcountll(__builtin_amdgcn_ballot_w64((lv[e] & 2u) != 0u)) -
                (u32)__builtin_popcountll(__builtin_amdgcn_ballot_w64((lv[e] & 1u) != 0u));
        if (lane0 < 8) __builtin_amdgcn_raw_buffer_store_b32(CTL[640 + lane0] + (nv > 256u ? 2u : 1u), rs, WS_GOF + lane0 * HBS, 0, 16);
        ++exa;
        ++exl;
      }
      if (u == UX && wave == 7) {
        ctl_load<false>(rq, hso, (u32)(smb + L_CTL + 2048));
        ++exa;
      }
      if (u == UX && wave < 6) {
        const int lane = fresh6(lane0);
        const int ca = L_CS + wave * 128 + (((lane >> 1) ^ (lane >> 5)) & 1) * 64 + (lane & 1) * 32;
        const f32x4 c0 = *reinterpret_cast<const f32x4*>(smem + ca), c1 = *reinterpret_cast<const f32x4*>(smem + ca + 16);
#pragma unroll 1
        for (int p = 0; p < 5; ++p) {
          u32x4* const pw = reinterpret_cast<u32x4*>(smem + L_WQ + wave * 5120 + p * 1024 + lane * 16);
          const u32x4 w = *pw;
          *pw = u32x4{cvt2(bflo(w[0]) + c0[0], bfhi(w[0]) + c0[1]), cvt2(bflo(w[1]) + c0[2], bfhi(w[1]) + c0[3]),
                      cvt2(bflo(w[2]) + c1[0], bfhi(w[2]) + c1[1]), cvt2(bflo(w[3]) + c1[2], bfhi(w[3]) + c1[3])};
        }
      }
      if (u == UF) {
        if (wave < 6) {
          q1_finish(smem, fresh6(lane0), wave);
          asm volatile("s_waitcnt lgkmcnt(0)" ::: "memory");
          if (lane0 == 0) lds_add(smem, L_CNT, 1u);
        }
      }
      if (wcnt(u) != 0 && (u - WU0) / WSP < nin && (wave & (WSP - 1)) == (u - WU0) % WSP) {
        u32 tw = tnext - WBK - hsub * PQ(u + 1);
        pace(tw, 0u);
        WNEXT
        ++exa;
        ++exl;
      }
    };
    static_assert(UQ >= 1 && UQ + 2 <= UB && UB < UL && UL + 2 <= UK && UK <= UC && UG + 2 <= UW && UC + 2 <= UF && UF < UX && UX >= 9 && UX + 2 < 16 - NUN && UAD >= 3 && WU0 >= UB, "order");
    slots<0, UX + 1>(step);
  }

#pragma unroll 1
  for (int half = 0;; ++half) {
    {
      float pend, pmax;
      u32x4 O0, O1, O2, A0, A1, A2, A3;
      Att at;
      const int lane = fresh6(lane0);
      {
        const u32 off = fresh(ar + A_GQ + hsub * 16384 + (lane >> 3) * 896 + wave * 128 + (wave < 6 ? (lane & 7) * 16 : 0));
        if (half == 1) {
          if (wave == 7) wait_tt(A_Q);
        } else {
          BURST(UX + 1, (u32)((lane >> 3) * 384 + 16 * (lane & 7)), vb + ((UX + 1 + rot) & 7) * GST * DH);
          lds_wait(cnt, 6u);
        }
        asm volatile("s_cmp_eq_u32 %[hf], 0\n\ts_cbranch_scc1 1f\n\ts_cmp_lt_u32 %[wv], 7\n\ts_cselect_b64 exec, -1, 0\n\ts_barrier\n\t"
                     "buffer_load_dwordx4 v[44:47], %[of], %[rq], 0 offen sc1\n\ts_mov_b64 exec, -1\n\t"
                     "s_cmp_lt_u32 %[wv], 4\n\ts_cbranch_scc0 1f\n\ts_setprio 2\n1:"
                     : "={v[44:47]}"(O1)
                     : [hf] "s"(half), [wv] "s"(wave), [of] "v"(off), [rq] "s"(rq)
                     : "scc", "memory");
        att_setup(at, smem, lane, wave);
        to2 = T_O2;
        const int sw = fresh(wave < 6 ? L_LAND + wave * 2048 + lane * 4 : L_SUM + ((lane >> 4) & 1) * 8 + (lane >> 5) * 4);
#define QK(W, a, k0, k1, X, Y) \
  "s_waitcnt lgkmcnt(" #W ")\n\t" SM "v[32:35], v[" #a ":" #a "+3], v[" #k0 ":" #k0 "+7], %[ix]\n\t" X SM "v[36:39], v[" #a ":" #a "+3], v[" #k1 ":" #k1 "+7], %[ix]\n\t" Y
#define MX(CTL) "s_nop 1\n\tv_max_f32_dpp %[mx], %[mx], %[mx]" CTL
        u32 t;
        float sk;
        asm volatile("v_mov_b64 v[32:33], 0\n\tv_mov_b64 v[34:35], 0\n\tv_mov_b64 v[36:37], 0\n\tv_mov_b64 v[38:39], 0\n\t"
                     "v_mov_b32 %[mx], 0\n\ts_cmp_eq_u32 %[hf], 0\n\ts_cbranch_scc1 5f\n\t"
                     "s_mov_b32 s88, 0x10000\n\ts_cmp_lt_u32 %[wv], 7\n\ts_cselect_b64 exec, -1, 0\n\ts_branch 2f\n"
                     "7:\n\tv_max_f32_dpp %[mx], %[sk], %[sk]" Q1 MX(Q2) MX(HM) MX(R8)
                     "v_mov_b32 %[t], %[mx]\n\ts_nop 1\n\tv_permlane16_swap_b32 %[mx], %[t]\n\tv_max_f32 %[mx], %[mx], %[t]\n\t"
                     "v_mov_b32 %[t], %[mx]\n\ts_nop 1\n\tv_permlane32_swap_b32 %[mx], %[t]\n\tv_max_f32 %[mx], %[mx], %[t]\n\t"
                     "v_sub_f32 %[l], %[sk], %[mx]\n\tv_exp_f32 %[l], %[l]\n\tv_mov_b32 %[sk], 0\n\ts_branch 6f\n"
                     "1:\n\tbuffer_load_dwordx4 v[44:47], %[of], %[rq], 0 offen sc1\n2:\n\ts_sub_u32 s88, s88, 1\n\ts_waitcnt vmcnt(0)\n\t"
                     "v_permlane32_swap_b32 v44, v45\n\tv_permlane32_swap_b32 v46, v47\n\t"
                     "v_add_f32 v48, v44, v45\n\tv_add_f32 v49, v46, v47\n\t" AND3("v44", "v45", "v46") AND3("%[t]", "1", "v47")
                     "v_permlane16_swap_b32 v48, v49\n\tv_add_f32 v48, v48, v49\n\t"
                     "v_cmp_eq_u32 vcc, 0, %[t]\n\ts_cselect_b64 vcc, 0, vcc\n\tv_add_f32_dpp v48, v48, v48" R8
                     CV "v49, v48, v48\n\tds_write_b16 %[qw], v49\n\tds_write_b32 %[sw], v48\n\t"
                     "s_cbranch_vccnz 1b\n\ts_mov_b64 exec, -1\n\ts_waitcnt lgkmcnt(0)\n\ts_barrier\n"
                     "5:\n\tds_read_b128 v[40:43], %[q0]\n\tds_read_b128 v[44:47], %[q1]\n\tds_read_b128 v[28:31], %[mx] offset:%[su]\n\t"
                     "ds_read_b128 v[48:51], %[q2]\n\tds_read_b128 v[52:55], %[q3]\n\t"
                     "ds_read_b128 v[56:59], %[q4]\n\tds_read_b128 v[60:63], %[q5]\n\t"
                     QK(6, 40, 64, 96, "s_mov_b32 vcc_lo, 0xaaaaaaaa\n\t", "s_mov_b32 vcc_hi, vcc_lo\n\t")
                     QK(4, 44, 72, 104, "v_add_f32 v28, v28, v29\n\tv_mul_f32 v31, 0x3acccccd, v31\n\t",
                        "v_add_f32 v28, v28, v30\n\tv_mul_f32 v28, 0x3acccccd, v28\n\t")
                     QK(3, 48, 80, 112, "v_fma_f32 v28, -v31, v31, v28\n\t" CV "v28, 0, |v28|\n\t",
                        "v_add_f32 v28, 0x3727c5ac, v28\n\t" CV "v28, 0, v28\n\t")
                     QK(2, 52, 88, 120, "v_sqrt_f32 v28, v28\n\t", CV "v28, 0, v28\n\tv_rcp_f32 v28, v28\n\t")
                     QK(1, 56, 128, 144, "v_mul_f32 v28, %[sc], v28\n\t", "")
                     QK(0, 60, 136, 152, "", "")
                     "v_mov_b64 v[40:41], 0\n\tv_mov_b64 v[42:43], 0\n\tv_mov_b64 v[44:45], 0\n\tv_mov_b64 v[46:47], 0\n\t"
                     "v_mov_b64 v[48:49], 0\n\tv_mov_b64 v[50:51], 0\n\ts_nop 0\n\t" PK(32, 34) PK(36, 38)
                     "s_nop 0\n\tv_add_f32_dpp v34, v33, v32" Q1 "v_add_f32_dpp v35, v36, v37" Q1
                     "v_cndmask_b32 %[sk], v34, v35, vcc\n\tv_mul_f32 %[sk], v28, %[sk]\n\tv_exp_f32 %[l], %[sk]\n"
                     "6:\n\tv_cmp_nge_f32 vcc, " SAFEB ", %[sk]\n\t" CV "%[t], %[l], 0\n\t"
                     "ds_write2_b32 %[wa], %[t], %[t] offset1:%[o1]\n\t"
                     "ds_read_b128 v[52:55], %[ra]\n\tds_read_b128 v[56:59], %[ra] offset:16\n\t"
                     "ds_read_b128 v[60:63], %[ra] offset:32\n\tds_read_b128 v[36:39], %[ra] offset:48\n\t"
                     "s_cbranch_vccnz 7b\n\tv_cmp_le_f32 vcc, 0xc1c00000, %[sk]\n\ts_cbranch_vccz 7b"
                     : "=&{v[40:43]}"(O0), "+{v[44:47]}"(O1), "=&{v[48:51]}"(O2), "=&{v[52:55]}"(A0), "=&{v[56:59]}"(A1),
                       "=&{v[60:63]}"(A2), "=&{v[36:39]}"(A3), [l] "=&v"(pend), [mx] "=&v"(pmax), [t] "=&{v29}"(t), [sk] "=&{v30}"(sk)
                     : [hf] "s"(half), [wv] "s"(wave), [of] "v"(off), [rq] "s"(rq), [qw] "v"(at.qw), [sw] "v"(sw), [q0] "v"(at.qad[0]),
                       [q1] "v"(at.qad[1]), [q2] "v"(at.qad[2]), [q3] "v"(at.qad[3]), [q4] "v"(at.qad[4]), [q5] "v"(at.qad[5]),
                       [ix] "v"(at.idxq), [wa] "v"(at.wa), [ra] "v"(at.ra), [su] "n"(L_SUM), [o1] "n"(P_U / 4 + 1),
                       [sc] "n"(__builtin_bit_cast(u32, SCL)), "{v[64:95]}"(kv0), "{v[96:127]}"(kv1), "{v[128:159]}"(kv2)
                     : "v28", "v31", V32, "s88", "vcc", "scc", "memory");
      }
      if (__builtin_expect(half == 0, 0)) {
        const int lv_ = fresh6(lane0);
        const u32 vo = (lv_ >> 3) * 384 + 16 * (lv_ & 7);
#pragma unroll
        for (int u = UX + 2; u < 16 - NUN; ++u) {
          BURST(u, vo, vb + ((u + rot) & 7) * GST * DH);
          if (u == UX + 2 && wave == 7) {
            constexpr int L_CTL = L_LAND + 6 * 2048;
            volatile LAS u32* const CTL = (volatile LAS u32*)(smem + L_CTL);
            const u32 hso = lv_ < 8 ? WS_HB + (u32)h * HBS + lv_ * 4 : WS_GOF + g * HBS;
            u32 gd, tmo = SPIN_CAP;
            HS_WAIT(false)
            if (lane0 == 8) *(volatile LAS u32*)(smem + L_AR) = (u32)(g & 3) * AREA + (((u32)(g >> 2) ^ (gd >> 1)) & 1u) * SIDE;
          }
        }
        tnext += wave * (TSE / 8 - WOFS) - hsub * PQ(15);
        tstep = TSE;
#pragma unroll
        for (int t = 48 - 3 * NUN; t < 48; ++t) {
          LOADW(t, vo, vb + ((t / 3 + rot) & 7) * GST * DH);
        }
        if (wave == 7) {
          const u32 a7 = *(volatile LAS u32*)(smem + L_AR) + (u32)(j >> 3) * 16384u + (u32)(j & 7) * 1024u;
          const u32x4 zz = __builtin_bit_cast(u32x4, zero4());
          store16(rs, lv_ * 16 + a7 + 2 * 16384, zz);
          store16(rs, lv_ * 16 + a7 + (j < 16 ? 6 : 8) * 16384, zz);
          store16(rs, lv_ * 16 + a7 + 12 * 16384, zz);
        }
#pragma unroll 1
        for (int p = 0; p < (isO ? NPRE : NPF); ++p) {
          pace(tnext, isO ? TPV : TPF);
          WNEXT
        }
      }
#define DA(CTL) "v_add_f32_dpp %[l], %[l], %[l]" CTL
#define PW(W) "s_waitcnt vmcnt(6*" #W "+%[no]) lgkmcnt(" #W ")\n\t"
#define PX(o, a, k) SM "v[40+4*" #o ":43+4*" #o "], v[" #a ":" #a "+3], v[160+24*" #k "+8*" #o ":167+24*" #k "+8*" #o "], %[ix]\n\t"
#define PM(a) "v_pk_mul_f32 v[" #a ":" #a "+1], v[" #a ":" #a "+1], v[52:53] op_sel_hi:[1,0]\n\t"
      u32 av, m_;
      asm volatile(PW(3) PX(0, 52, 0) DA(Q1) PX(1, 52, 0) "s_mov_b32 s80, 0xff00ff\n\tv_mov_b32 v32, 1\n\t" PX(2, 52, 0)
                   PW(2) PX(0, 56, 1) DA(Q2) PX(1, 56, 1) "s_not_b32 s81, s80\n\tv_mov_b32 v34, 1\n\t" PX(2, 56, 1)
                   PW(1) PX(0, 60, 2) DA(HM) PX(1, 60, 2) "v_mov_b32 v35, 1\n\t" PX(2, 60, 2)
                   PW(0) PX(0, 36, 3) DA(R8) PX(1, 36, 3) PX(2, 36, 3) "s_setprio 0\n"
                   "4:\n\tds_write2_b32 %[la], %[l], %[mx] offset1:4\n\tv_mov_b32 %[av], %[lar]\n\ts_mov_b64 exec, s[80:81]\n\t"
                   "s_cmp_lt_u32 %[wv], 3\n\ts_nop 0\n\t"
                   "ds_write_b128 %[oa], v[40:43]\n\tds_write_b128 %[oa], v[44:47] offset:256\n\t"
                   "ds_write_b128 %[oa], v[48:51] offset:512\n\t"
                   "s_cselect_b64 exec, -1, 0\n\ts_waitcnt lgkmcnt(0)\n\ts_barrier\n\t"
                   "ds_read_b128 v[52:55], %[sa]\n\tds_read_b128 v[56:59], %[sa] offset:%[op]\n\t"
                   "ds_read_b128 v[60:63], %[sa] offset:%[os]\n\tds_read_b128 v[36:39], %[sa] offset:%[os]+%[op]\n\t"
                   "s_mov_b64 exec, -1\n\tds_read_b32 %[m], %[ma] offset:16\n\tds_read_b32 %[av], %[av]\n\ts_cbranch_scc0 5f\n\t"
                   "s_waitcnt lgkmcnt(4)\n\t" PK(52, 56) PK(54, 58) "s_waitcnt lgkmcnt(3)\n\t" PK(52, 60) PK(54, 62)
                   "s_waitcnt lgkmcnt(2)\n\t" PK(52, 36) PK(54, 38) "s_waitcnt lgkmcnt(0)\n\tv_cmp_ne_u32 vcc, 0, %[m]\n\t"
                   "v_permlane32_swap_b32 v52, v54\n\tv_permlane32_swap_b32 v53, v55\n\t" PK(52, 54)
                   "v_add_u32 v56, %[ds], %[av]\n\ts_cbranch_vccnz 8f\n\t"
                   "v_add_f32_dpp v52, v52, v52" R8 "v_add_f32_dpp v53, v53, v53" R8 CV "v52, v52, v53\n\tv_or_b32 v52, 1, v52\n\t"
                   "buffer_store_dword v52, v56, %[rq], %[so] offen sc1\n\ts_branch 9f\n"
                   "8:\n\tv_max_f32_dpp v32, %[m], %[m]" Q1 "s_nop 1\n\tv_max_f32_dpp v32, v32, v32" Q2
                   "s_nop 1\n\tv_max_f32_dpp v32, v32, v32" HM "s_barrier\n\tv_sub_f32 v52, %[mx], v32\n\tv_exp_f32 v52, v52\n\t"
                   "v_mov_b32 %[mx], 0\n\tv_or_b32 v32, 1, v32\n\tv_mul_f32 %[l], %[l], v52\n\t"
                   PM(40) PM(42) PM(44) PM(46) PM(48) PM(50) "s_branch 4b\n"
                   "5:\n\tds_read_b128 v[52:55], %[ma]\n\ts_cmp_gt_u32 %[wv], 3\n\ts_waitcnt lgkmcnt(0)\n\tv_cmp_ne_u32 vcc, 0, %[m]\n\t" PK(52, 54)
                   "v_add_f32 v33, v52, v53\n\ts_cbranch_vccnz 8b\n\tv_add_u32 v56, %[ds], %[av]\n\ts_cbranch_scc1 6f\n\t"
                   "v_add_f32_dpp v33, v33, v33" Q1 "s_nop 1\n\tv_add_f32_dpp v33, v33, v33" Q2
                   "s_nop 1\n\tv_add_f32_dpp v33, v33, v33" HM "v_or_b32 v33, 1, v33\n6:\n\t"
                   "buffer_store_dwordx4 v[32:35], v56, %[rq], %[so] offen sc1\n\ts_nop 1\n9:"
                   : "+{v[40:43]}"(O0), "+{v[44:47]}"(O1), "+{v[48:51]}"(O2), "+{v[52:55]}"(A0), "+{v[56:59]}"(A1),
                     "+{v[60:63]}"(A2), "+{v[36:39]}"(A3), [l] "+v"(pend), [mx] "+v"(pmax), [av] "=&v"(av), [m] "=&v"(m_)
                   : [ix] "v"(at.idx), [no] "n"(NPRE), [la] "v"(at.la), [oa] "v"(at.oa), [sa] "v"(at.sa), [ma] "v"(at.ma),
                     [ds] "v"(at.dst), [wv] "s"(wave), [rq] "s"(rq), [so] "s"((u32)half * GO_SZ), [lar] "n"(L_AR), [op] "n"(OM_P * 4),
                     [os] "n"(OM_S * 4), "{v[160:191]}"(kv3), "{v[192:223]}"(kv4), "{v[224:255]}"(kv5)
                   : V32, "s80", "s81", "vcc", "scc", "memory");
      tref = now();
      ar = __builtin_amdgcn_readfirstlane(av);
    }
    const u32 go = ar + A_GO + (u32)half * GO_SZ;
    const u32 gyw = A_GY + (u32)half * GY_PAIR;
    const u32 gy = gyw + ar;
    const u32 gh = go + A_GH;
    const int land = L_LAND + wave * 2048;
    if (isO) {
      {
        const int lane = fresh6(lane0);
        const u32 voff = fresh(go + go_slot(wave) + ((lane * 16) ^ ((lane >> 4) * 64)));
        asm volatile("s_waitcnt vmcnt(1)" ::: "memory");
        if (wave == 7) wait_until(tref + (half ? to2 : 24 * T_O));
        asm volatile("s_mov_b32 m0, %2\n\ts_bitcmp1_b32 " STR(OEARLY) ", %3\n\ts_cbranch_scc0 1f\n\ts_barrier\n\t"
                     "buffer_load_dwordx4 %0, %1, 0 offen sc1 lds\n\tbuffer_load_dwordx4 %0, %1, 0 offen offset:1024 sc1 lds\n1:"
                     :
                     : "v"(voff), "s"(rq), "s"(land), "s"(wave)
                     : "memory", "scc");
        const int l0 = fresh(L_WB + wave * 6144 + wlane(lane, 0)), l1 = l0 ^ 32;
        u32x4 wf[6];
#pragma unroll
        for (int k = 0; k < 3; ++k) {
          wf[2 * k] = wfrag(smem, l0, 2048 * k);
          wf[2 * k + 1] = wfrag(smem, l1, 2048 * k);
        }
        asm volatile("" ::: "memory");
        const int n = lane & 15, G = lane >> 4;
        const int r0_ = land + (n & 7) * 256 + (n & 3) * 64;
        const int ra = fresh(n < 8 ? r0_ + G * 16 : r0_ ^ 192), rb = fresh((r0_ + G * 16) ^ 64), rc = fresh((r0_ + G * 16) ^ 128);
        const int rw = fresh(L_RED + (lane >> 1) * 80 + wave * 8 + (lane & 1) * 4);
        const int rr = fresh(L_RED + n * 80);
        const u32 dst = fresh(gyw + gy_rep(G) + gy_chunk(4 * j + (n >> 2)) + (n & 3) * 4 + g * 256);
        u32 t;
#define RS(r, e) "v_mul_f32_dpp v" #r ", v41, v" #r " row_newbcast:" #e ALL
        asm volatile("s_mov_b64 s[82:83], -1\n\ts_movk_i32 s89, %[sd]\n\t" POLL_HEAD
                     "s_bitcmp1_b32 " STR(OEARLY) ", %[wv]\n\ts_cbranch_scc0 1f\n\ts_branch 5f\n"
                     "7:\n\tv_max_f32_dpp v41, v52, v52" Q1 "s_nop 1\n\tv_max_f32_dpp v41, v41, v41" Q2
                     "s_nop 1\n\tv_max_f32_dpp v41, v41, v41" HM "v_sub_f32 v41, v52, v41\n\tv_exp_f32 v41, v41\n\ts_nop 0\n\t"
                     "v_mul_f32 v42, v41, v53\n\ts_nop 1\n\tv_add_f32_dpp v42, v42, v42" Q1 "s_nop 1\n\tv_add_f32_dpp v42, v42, v42" Q2
                     "s_nop 1\n\tv_add_f32_dpp v42, v42, v42" HM "v_mov_b32_dpp v41, v41 row_shl:4 row_mask:0x2 bank_mask:0xf\n\t"
                     "v_rcp_f32 v42, v42\n\ts_nop 0\n\t"
                     RS(44, 8) RS(45, 9) RS(46, 10) RS(47, 11) RS(48, 8) RS(49, 9) RS(50, 10) RS(51, 11) "s_branch 3f\n"
                     "2:\n\tv_cmp_eq_u32 vcc, 0, %[t]\n\ts_cbranch_vccz 7b\n\t" POLL_AGAIN
                     "\n5:\n\tv_mov_b32 v41, 1\n\t" POLL_READ
                     "s_waitcnt lgkmcnt(2)\n\t"
                     MF "v[44:47], " F0 ", %[w0], 0\n\tv_add_f32_dpp v42, v53, v53" Q1
                     "v_or_b32_dpp v41, v52, v41 quad_perm:[0,1,2,3] row_mask:0xf bank_mask:0xc\n\t"
                     MF "v[48:51], " F0 ", %[w1], 0\n\tv_add_f32_dpp v42, v42, v42" Q2 AND3("v52", "v53", "v54")
                     "s_waitcnt lgkmcnt(1)\n\t"
                     MF "v[44:47], " F1 ", %[w2], v[44:47]\n\tv_add_f32_dpp v42, v42, v42" HM AND3("%[t]", "v55", "v56")
                     MF "v[48:51], " F1 ", %[w3], v[48:51]\n\t" AND3("%[t]", "v57", "v58") "v_rcp_f32 v42, v42\n\t"
                     "s_waitcnt lgkmcnt(0)\n\t"
                     MF "v[44:47], " F2 ", %[w4], v[44:47]\n\t" MF "v[48:51], " F2 ", %[w5], v[48:51]\n\t"
                     AND3("%[t]", "v59", "v60") AND3("%[t]", "v61", "v62") AND3("%[t]", "1", "v63")
                     "v_cmp_gt_u32 vcc, v41, %[t]\n\ts_cbranch_vccnz 2b\n"
                     "3:\n\ts_mov_b32 exec_hi, 0\n\ts_cmp_eq_u32 %[wv], 0\n\t" PK(44, 46) PK(48, 50)
                     "v_add_f32 v44, v44, v45\n\tv_add_f32 v48, v48, v49\n\ts_nop 1\n\t"
                     "v_permlane16_swap_b32 v44, v48\n\tv_add_f32 v44, v44, v48\n\tv_mul_f32_dpp v44, v42, v44 row_newbcast:8" ALL
                     "ds_write_b32 %[rw], v44\n\ts_cselect_b64 exec, -1, 0\n\ts_waitcnt lgkmcnt(0)\n\ts_barrier\n\t"
                     "ds_read_b128 " F0 ", %[rr]\n\tds_read_b128 " F1 ", %[rr] offset:16\n\tds_read_b128 " F2 ", %[rr] offset:32\n\t"
                     "ds_read_b128 v[44:47], %[rr] offset:48\n\ts_cbranch_execz 9f\n\t"
                     "s_waitcnt lgkmcnt(2)\n\t" PK(52, 54) PK(56, 58) PK(52, 56) "s_waitcnt lgkmcnt(1)\n\t" PK(52, 60) PK(52, 62)
                     "s_waitcnt lgkmcnt(0)\n\t" PK(52, 44) PK(52, 46) CV "v52, v52, v53\n\tv_or_b32 v52, 1, v52\n\t"
                     "buffer_store_dword v52, %[ds], %[rq], 0 offen sc1\n\tbuffer_store_dword v52, %[ds], %[rq], s89 offen sc1\n"
                     "9:\n\ts_mov_b64 exec, -1"
                     : [t] "=&v"(t)
                     : [ra] "v"(ra), [rb] "v"(rb), [rc] "v"(rc), WIN6(wf), [vo] "v"(voff), [rq] "s"(rq), [land] "s"(land), [rw] "v"(rw),
                       [rr] "v"(rr), [ds] "v"(dst), [wv] "s"(wave), [sd] "n"(SIDE)
                     : V40, V52, S80);
      }
      tref = now();
      if (half == 0) {
        if (wave >= 1 && wave < 7) {
          const u16* s0 = karg<56>() + (size_t)(192 * g) * D + 32 * j;
#pragma unroll
          for (int p = 0; p < 2; ++p) dma_piece(s0, D, smem, L_W2, (wave - 1) * 2 + p, fresh6(lane0));
          asm volatile("s_waitcnt vmcnt(0)" ::: "memory");
        }
        bar();
        if (wave == 5) {
          wait_until(tref + 24 * T_LAZY);
          x_update<false>(smem, rq, fresh6(lane0), gy + gy_row(c), c);
        }
      } else if (g == 0 && wave == 2) {
        const int lane = fresh6(lane0);
        const int col = j * 32 + (lane & 7) * 4 + ((lane >> 4) & 1) * 2 + (lane >> 5);
        u32 x0v, dummy;
        u16* const po = karg<64>() + col;
        asm volatile("global_load_ushort %0, %2, off\n\tglobal_load_ushort %1, %3, off\n\ts_waitcnt vmcnt(0)"
                     : "=&v"(x0v), "=&v"(dummy)
                     : "v"(karg<0>() + col), "v"(po)
                     : "memory");
        wait_until(tref + 24 * T_LAZY);
        const u32 o = gy + gy_chunk(4 * j + ((lane >> 1) & 3)) + (lane >> 3) * 256 + (lane & 1) * 8;
        const u32 off[3] = {o - GY_PAIR, o - GY_PAIR + GY_INST, o};
        float y[3];
        col_poll<3>(rq, off, y);
        float x3 = bf16r(bf16r(bf16r(bflo(x0v) + y[0]) + y[1]) + y[2]);
        const u32 off2 = fresh(o + GY_INST);
        asm volatile("" : "+v"(x3));
        wait_tt(A_F);
        u32 t;
        asm volatile("s_mov_b32 s88, 0x10000\n"
                     "1:\n\tbuffer_load_dwordx2 v[40:41], %[of], %[rq], 0 offen sc1\n\ts_sub_u32 s88, s88, 1\n\ts_waitcnt vmcnt(0)\n\t"
                     "v_lshlrev_b32 v42, 16, v40\n\tv_and_b32 v43, 0xffff0000, v40\n\t"
                     "v_lshlrev_b32 v44, 16, v41\n\tv_and_b32 v45, 0xffff0000, v41\n\t"
                     "v_permlane32_swap_b32 v42, v43\n\t" AND3("v40", "1", "v41") "v_permlane32_swap_b32 v44, v45\n\t"
                     "v_add_f32 v42, v42, v43\n\tv_add_f32 v44, v44, v45\n\tv_cmp_eq_u32 vcc, 0, %[t]\n\ts_cselect_b64 vcc, 0, vcc\n\t"
                     "v_permlane16_swap_b32 v42, v44\n\tv_add_f32 v42, v42, v44\n\ts_cbranch_vccnz 1b\n\ts_nop 0\n\t"
                     "v_add_f32_dpp v42, v42, v42" R8 CV "v42, 0, v42\n\tv_add_f32 v42, %[x], v42\n\t" CV "v42, v42, v42\n\t"
                     "global_store_short %[po], v42, off"
                     : [t] "=&v"(t)
                     : [of] "v"(off2), [rq] "s"(rq), [x] "v"(x3), [po] "v"(po)
                     : V40, "s88", "vcc", "scc", "memory");
      }
      if (wave >= 6) {
        const int lane = fresh6(lane0), wv = wave - 6;
        const int n = lane & 15, G = lane >> 4;
        const int l0 = fresh(L_W2 + wlane(lane, wv));
        u32x4 wf[6];
#pragma unroll
        for (int k = 0; k < 6; ++k) wf[k] = wfrag(smem, l0, 2048 * k);
        const int l48 = lane < 48 ? lane : 0;
        const u32 off = fresh(gh + (u32)l48 * 16);
        const int aw = fresh(L_ACT + wv * 384 + l48 * 8);
        const int ab = fresh(L_ACT + wv * 384 + 16 * G);
        const u32 dst = fresh((lane & (half ? 49 : 1)) ? OOB : gyw + GY_INST + gy_rep(G) + gy_chunk(4 * j + wv * 2 + (n >> 3)) + (n & 7) * 2 + g * 256);
        if (half)
          wait_tt(A_H);
        else
          wait_until(tref + 24 * T_H);
        u32 t;
#define HK(W, a, w, c) "s_waitcnt lgkmcnt(" #W ")\n\t" MF "v[36:39], v[" #a ":" #a "+3], %[" #w "], " c "\n\t"
        asm volatile("s_mov_b32 s88, 0x10000\n\ts_movk_i32 s89, %[sd]\n\ts_movk_i32 s90, 0x4000\n"
                     "1:\n\ts_waitcnt lgkmcnt(0)\n\tbuffer_load_dwordx4 v[40:43], %[of], %[rq], 0 offen sc1\n\t"
                     "buffer_load_dwordx4 v[44:47], %[of], %[rq], s90 offen sc1\n\ts_sub_u32 s88, s88, 1\n\ts_waitcnt vmcnt(0)\n\t"
                     "v_pk_mul_f32 v[48:49], v[40:41], v[44:45]\n\tv_pk_mul_f32 v[50:51], v[42:43], v[46:47]\n\t"
                     CV "v48, v48, v49\n\t" CV "v49, v50, v51\n\tds_write_b64 %[aw], v[48:49]\n\t"
                     "ds_read_b128 v[48:51], %[ab]\n\tds_read_b128 v[52:55], %[ab] offset:64\n\t"
                     "ds_read_b128 v[56:59], %[ab] offset:128\n\tds_read_b128 v[60:63], %[ab] offset:192\n\t"
                     AND3("v40", "v41", "v42") AND3("%[t]", "v43", "v44") AND3("%[t]", "v45", "v46") AND3("%[t]", "1", "v47")
                     "ds_read_b128 v[40:43], %[ab] offset:256\n\tds_read_b128 v[44:47], %[ab] offset:320\n\t"
                     "v_cmp_eq_u32 vcc, 0, %[t]\n\ts_cselect_b64 vcc, 0, vcc\n\ts_cbranch_vccnz 1b\n\t"
                     HK(5, 48, w0, "0") HK(4, 52, w1, "v[36:39]") HK(3, 56, w2, "v[36:39]") HK(2, 60, w3, "v[36:39]")
                     HK(1, 40, w4, "v[36:39]") HK(0, 44, w5, "v[36:39]")
                     "s_nop 7\n\tv_mov_b32_dpp v37, v36" Q1 CV "v36, v36, v37\n\tv_or_b32 v36, 1, v36\n\t"
                     "buffer_store_dword v36, %[ds], %[rq], 0 offen sc1\n\tbuffer_store_dword v36, %[ds], %[rq], s89 offen sc1"
                     : [t] "=&v"(t)
                     : WIN6(wf), [of] "v"(off), [rq] "s"(rq), [aw] "v"(aw), [ab] "v"(ab), [ds] "v"(dst), [sd] "n"(SIDE)
                     : V36, V40, V52, "s88", "s89", "s90", "vcc", "scc", "memory");
      }
    } else {
      const int lane = fresh6(lane0);
      const int n = lane & 15, G = lane >> 4;
      if (half == 1) {
        x_update<false>(smem, rq, lane, gy - GY_PAIR + GY_INST + gy_row(wave), wave);
      } else {
        if (wave == 1) {
          const u32x2 c0 = *(volatile LAS u32x2*)(smem + L_CLK);
          const float r = (float)(rtnow() - c0[1]) * (24.2f * 512.f) * __builtin_amdgcn_rcpf((float)(now() - c0[0])) - 511.5f;
          *(volatile LAS u32*)(smem + L_CLK) = ((u32)__builtin_fminf(__builtin_fmaxf(r, 0.f), 127.f) << 25) | 1u;
        }
#pragma unroll 1
        for (u32 tw = tref + 24 * T_FW; wn != 0; tw += 24 * T_FD) {
          wait_until(tw);
          WNEXT
        }
      }
      asm volatile("s_waitcnt vmcnt(0) lgkmcnt(0)" ::: "memory");
      G80 a;
      g80_load(a, smem, lane, L_WB + wave * 5120, L_HB + 160 * wave, land, L_ONE);
      const u32 voff = fresh(gy + gy_row(wave) + ((lane * 16) ^ ((lane >> 4) * 64)));
      const int rw = fresh(L_RED + n * 144 + wave * 16 + G * 4);
      const int sw_ = fresh(n == 8 ? L_ST + wave * 16 + G * 4 : L_ACT + lane * 4);
      const int rr = fresh(L_RED + (lane & 31) * 144 + (lane >> 5) * 64), rs_ = fresh(L_ST + 16 * (lane & 7));
      const u32 dst = fresh(lane < 32 ? gh + fslot + lane * 4 : OOB);
      if (wave == 7) {
        if (half)
          wait_tt(A_Y);
        else
          wait_until(tref + 24 * T_YF);
      }
      u32 t;
#define SH4 " row_shr:4" ALL
      asm volatile(USED10 "s_mov_b32 s89, 0x810000\n\t" POLL_HEAD "s_branch 1f\n2:\n\t" POLL_AGAIN POLL_READ
                   "s_waitcnt lgkmcnt(2)\n\t"
                   MF "v[40:43], " F0 ", " F0 ", 0\n\t" AND3("v52", "v53", "v54")
                   MF "v[44:47], " F0 ", %[w0], 0\n\t" MF "v[48:51], " F0 ", %[w1], 0\n\t"
                   "s_waitcnt lgkmcnt(1)\n\t"
                   MF "v[40:43], " F1 ", " F1 ", v[40:43]\n\t" AND3("%[t]", "v55", "v56") AND3("%[t]", "v57", "v58")
                   "s_waitcnt lgkmcnt(0)\n\t"
                   MF "v[40:43], " F2 ", " F2 ", v[40:43]\n\t" AND3("%[t]", "v59", "v60") AND3("%[t]", "v61", "v62")
                   MF "v[44:47], " F1 ", %[w2], v[44:47]\n\t" AND3("%[t]", "1", "v63") "v_cmp_eq_u32 vcc, 0, %[t]\n\t"
                   MF "v[48:51], " F1 ", %[w3], v[48:51]\n\ts_and_b64 vcc, vcc, s[80:81]\n\ts_cbranch_scc1 2b\n"
                   "3:\n\t" MF "v[44:47], " F2 ", %[w4], v[44:47]\n\t" PK(40, 42) "v_add_f32 v40, v40, v41\n\t"
                   MF "v[48:51], " F2 ", %[w5], v[48:51]\n\ts_cmp_eq_u32 %[wv], 0\n\t"
                   "v_add_f32_dpp v40, v40, v40" Q1 "s_nop 1\n\tv_add_f32_dpp v40, v40, v40" Q2 PK(44, 46)
                   "v_add_f32 v44, v44, v45\n\tv_add_f32_dpp v41, v40, v40" SH4 PK(48, 50) "v_add_f32 v48, v48, v49\n\t"
                   "v_add_f32_dpp v40, v41, v40" SH4
                   "ds_write2st64_b32 %[rw], v44, v48 offset1:9\n\tds_write_b32 %[sw], v40\n\t"
                   "s_cselect_b64 exec, -1, 0\n\ts_bitcmp1_b32 %[j], 0\n\ts_waitcnt lgkmcnt(0)\n\ts_barrier\n\t"
                   "ds_read_b128 v[40:43], %[rs]\n\tds_read_b128 " F0 ", %[rr]\n\tds_read_b128 " F1 ", %[rr] offset:16\n\t"
                   "ds_read_b128 " F2 ", %[rr] offset:32\n\tds_read_b128 v[44:47], %[rr] offset:48\n\ts_cbranch_execz 9f\n\t"
                   "s_waitcnt lgkmcnt(4)\n\tv_add_f32 v40, v40, v41\n\tv_add_f32 v42, v40, v42\n\tv_add_f32_dpp v43, v43, v43" Q1
                   "s_waitcnt lgkmcnt(2)\n\tv_add_f32_dpp v42, v42, v42" Q1 "v_add_f32_dpp v43, v43, v43" Q2 PK(52, 56)
                   "v_add_f32_dpp v42, v42, v42" Q2 "v_add_f32_dpp v43, v43, v43" HM PK(54, 58) "v_add_f32_dpp v42, v42, v42" HM
                   "v_mul_f32 v43, 0x3acccccd, v43\n\tv_mul_f32 v42, 0x3acccccd, v42\n\tv_fma_f32 v42, -v43, v43, v42\n\t"
                   "s_waitcnt lgkmcnt(0)\n\t" PK(52, 60) PK(54, 62) PK(52, 44) PK(54, 46)
                   CV "v43, 0, v43\n\tv_add_f32 v52, v52, v53\n\tv_fma_f32 v54, -v43, v55, v54\n\t"
                   "v_add_f32 v53, v52, v54\n\tv_add_f32 v52, v52, v54\n\t"
                   CV "v42, 0, |v42|\n\tv_add_f32 v42, 0x3727c5ac, v42\n\t" CV "v42, 0, v42\n\tv_sqrt_f32 v42, v42\n\t"
                   "v_permlane32_swap_b32 v52, v53\n\t" CV "v42, 0, v42\n\tv_rcp_f32 v42, v42\n\tv_add_f32 v52, v52, v53\n\t"
                   "v_mul_f32 v52, v52, v42\n\t" CV "v53, 0, v52\n\tv_mul_f32 v54, 0xbfb8aa3b, v53\n\tv_exp_f32 v54, v54\n\t"
                   "s_cbranch_scc1 8f\n\tv_add_f32 v54, 1.0, v54\n\tv_rcp_f32 v54, v54\n\ts_nop 0\n\tv_mul_f32 v52, v53, v54\n"
                   "8:\n\t" CV "v52, s89, v52\n\tbuffer_store_dword v52, %[ds], %[rq], 0 offen sc1\n"
                   "9:\n\ts_mov_b64 exec, -1"
                   : [t] "=&v"(t)
                   : [ra] "v"(a.ra), [rb] "v"(a.rb), [rc] "v"(a.rc), WIN6(a.w), [vo] "v"(voff), [rq] "s"(rq), [land] "s"(land),
                     [rw] "v"(rw), [sw] "v"(sw_), [rr] "v"(rr), [rs] "v"(rs_), [ds] "v"(dst), [wv] "s"(wave), [j] "s"(j)
                   : V40, V52, S80);
      tref = now();
      if (half == 0 && wave == 1 && j == 20 && lane0 < 8)
        __builtin_amdgcn_raw_buffer_store_b32(((rtnow() & 0x1fffffu) << 4) | *(volatile LAS u32*)(smem + L_CLK), rs, gy_rep(fresh6(lane0)) + st_line(g), 0, 16);
      if (half == 0) {
        x_update<true>(smem, rq, fresh6(lane0), land, wave);
      }
    }
    if (half == 1) break;
    {
      if (wave < 7) {
        const int lane = fresh6(lane0);
        const int n = lane & 15;
        G80 a;
        g80_load(a, smem, lane, L_WQ + wave * 5120, L_HB + 160 * c, land, wave == 6 ? L_ONE : L_ZERO);
        const u32 voff = fresh(gy + GY_INST + gy_row(c) + ((lane * 16) ^ ((lane >> 4) * 64)));
        const u32 gq = ar + A_GQ + hsub * 16384 + c * 896 + wave * 128;
        const u32 dst = fresh(wave < 6 ? (lane < 32 ? gq + lane * 4 : OOB) : (n == 8 ? gq + (lane >> 4) * 4 : OOB));
        if (wave == 4) {
          wait_until(tref + 24 * (isO ? T_AO : T_AN));
          stamp_poll(smem, rq, ar + st_line(lane & 7));
          wait_tt(A_E);
        }
        u32 t;
        asm volatile(USED10 POLL_HEAD "s_branch 1f\n"
                     "6:\n\ts_waitcnt lgkmcnt(2)\n\t" MF "v[44:47], " F0 ", " F0 ", 0\n\t" AND3("v52", "v53", "v54")
                     "s_waitcnt lgkmcnt(1)\n\t" MF "v[44:47], " F1 ", " F1 ", v[44:47]\n\t" AND3("%[t]", "v55", "v56") AND3("%[t]", "v57", "v58")
                     "s_waitcnt lgkmcnt(0)\n\t" MF "v[44:47], " F2 ", " F2 ", v[44:47]\n\t" AND3("%[t]", "v59", "v60")
                     AND3("%[t]", "v61", "v62") AND3("%[t]", "1", "v63")
                     "v_cmp_eq_u32 vcc, 0, %[t]\n\ts_and_b64 vcc, vcc, s[80:81]\n\ts_cbranch_scc1 2f\n\ts_nop 1\n\t"
                     PK(44, 46) "v_add_f32 v44, v44, v45\n\ts_nop 1\n\tv_add_f32_dpp v44, v44, v44" Q1
                     "s_nop 1\n\tv_add_f32_dpp v44, v44, v44" Q2 "s_nop 1\n\tv_add_f32_dpp v45, v44, v44" SH4
                     "s_nop 1\n\tv_add_f32_dpp v44, v45, v44" SH4 "v_or_b32 v44, 1, v44\n\t"
                     "buffer_store_dword v44, %[ds], %[rq], 0 offen sc1\n\ts_branch 9f\n"
                     "2:\n\t" POLL_AGAIN "s_cmp_eq_u32 %[wv], 6\n\t" POLL_READ "s_cbranch_scc1 6b\n\t"
                     "s_waitcnt lgkmcnt(2)\n\t"
                     MF "v[44:47], " F0 ", %[w0], 0\n\t" AND3("v52", "v53", "v54") MF "v[48:51], " F0 ", %[w1], 0\n\t"
                     "s_waitcnt lgkmcnt(1)\n\t"
                     MF "v[44:47], " F1 ", %[w2], v[44:47]\n\t" AND3("%[t]", "v55", "v56") AND3("%[t]", "v57", "v58")
                     MF "v[48:51], " F1 ", %[w3], v[48:51]\n\t"
                     "s_waitcnt lgkmcnt(0)\n\t"
                     MF "v[44:47], " F2 ", %[w4], v[44:47]\n\t" MF "v[48:51], " F2 ", %[w5], v[48:51]\n\t"
                     AND3("%[t]", "v59", "v60") AND3("%[t]", "v61", "v62") AND3("%[t]", "1", "v63")
                     "v_cmp_eq_u32 vcc, 0, %[t]\n\ts_and_b64 vcc, vcc, s[80:81]\n\ts_cbranch_scc1 2b\n"
                     "3:\n\ts_nop 0\n\t" PK(44, 46) PK(48, 50) "v_add_f32 v44, v44, v45\n\tv_add_f32 v48, v48, v49\n\ts_nop 1\n\t"
                     "v_permlane16_swap_b32 v44, v48\n\tv_add_f32 v45, v44, v48\n\tv_add_f32 v44, v44, v48\n\ts_nop 1\n\t"
                     "v_permlane32_swap_b32 v44, v45\n\tv_add_f32 v44, v44, v45\n\tv_or_b32 v44, 1, v44\n\t"
                     "buffer_store_dword v44, %[ds], %[rq], 0 offen sc1\n9:"
                     : [t] "=&v"(t)
                     : [ra] "v"(a.ra), [rb] "v"(a.rb), [rc] "v"(a.rc), WIN6(a.w), [vo] "v"(voff), [rq] "s"(rq), [land] "s"(land),
                       [ds] "v"(dst), [wv] "s"(wave)
                     : V40, V52, S80);
      } else {
        bar();
      }
    }
  }
#undef RS
#define RS(n) "s_memrealtime s[" #n ":" #n "+1]\n\ts_nop 1\n\t"
#define RD(n) "s_sub_i32 s80, s80, s" #n "\n\t"
  asm volatile("s_branch 9f\n.Lmk_wait:\n\t"
               "v_mov_b32 v62, 0\n\tds_read_b64 v[62:63], v62 offset:%[an]\n\ts_waitcnt lgkmcnt(0)\n\t"
               "v_readfirstlane_b32 s81, v63\n\tv_readfirstlane_b32 s82, v62\n\ts_mul_i32 s81, s81, 3\n\t"
               "s_add_i32 s82, s82, s80\n\ts_mul_i32 s80, s80, s81\n\ts_lshr_b32 s80, s80, 11\n\ts_add_i32 s80, s80, s82\n\t"
               "s_sub_i32 s81, 1549, s81\n"
               "1:\n\ts_memrealtime s[98:99]\n\ts_waitcnt lgkmcnt(0)\n\ts_lshl_b32 s98, s98, 3\n\ts_sub_i32 s98, s80, s98\n\t"
               "s_cmpk_gt_i32 s98, 8*20\n\ts_cbranch_scc1 1b\n\ts_cmpk_lt_i32 s98, 8*9\n\ts_cbranch_scc1 3f\n\t"
               RS(84) RS(86) RS(88) RS(90) RS(92) RS(94) RS(96) "s_memrealtime s[98:99]\n\t"
               "s_getpc_b64 s[82:83]\n2:\n\ts_add_u32 s82, s82, 3f-2b\n\ts_addc_u32 s83, s83, 0\n\ts_waitcnt lgkmcnt(0)\n\t"
               RD(84) RD(86) RD(88) RD(90) RD(92) RD(94) RD(96) RD(98)
               "s_mul_i32 s80, s80, s81\n\ts_ashr_i32 s80, s80, 10\n\ts_sub_i32 s80, s80, 100\n\t"
               "s_max_i32 s80, s80, 0\n\ts_min_i32 s80, s80, 4*26\n\ts_andn2_b32 s80, s80, 3\n\t"
               "s_sub_u32 s82, s82, s80\n\ts_subb_u32 s83, s83, 0\n\ts_setpc_b64 s[82:83]\n\t"
               ".rept 26\n\ts_nop 1\n\t.endr\n3:\n\ts_setpc_b64 s[78:79]\n9:"
               :
               : [an] "n"(L_ANC));
  asm volatile(".Lmk_code_end:");
}

}

extern "C" void kernel_launch(void* const* d_in, const int* in_sizes, int n_in, void* d_out, int out_size, void* d_ws,
                              size_t ws_size, hipStream_t stream) {
  Params P;
  P.x0 = (const u16*)d_in[0];
  P.kc = (const u16*)d_in[1];
  P.vc = (const u16*)d_in[2];
  P.wq = (const u16*)d_in[3];
  P.wo = (const u16*)d_in[4];
  P.w1 = (const u16*)d_in[6];
  P.w3 = (const u16*)d_in[7];
  P.w2 = (const u16*)d_in[8];
  P.out = (u16*)d_out;
  P.ws = (u32*)d_ws;
  mega_kernel<<<NWG, NT, 0, stream>>>(P);
}
```

```cpp
#include <hip/hip_runtime.h>
#include <hip/hip_bf16.h>
#include <stdint.h>

namespace {

typedef unsigned int u32;
typedef unsigned short u16;
typedef unsigned long long u64;
using u32x2 = __attribute__((ext_vector_type(2))) unsigned;
using u32x4 = __attribute__((ext_vector_type(4))) unsigned;
using f32x2 = __attribute__((ext_vector_type(2))) float;
using f32x4 = __attribute__((ext_vector_type(4))) float;
using s16x4 = __attribute__((ext_vector_type(4))) short;
using bf16x2 = __attribute__((ext_vector_type(2))) __bf16;
using bf16x8 = __attribute__((ext_vector_type(8))) __bf16;
#define LAS __attribute__((address_space(3)))

constexpr int D = 640, H = 32, DH = 192, WIN = 4096, SK = 32768, F = 1536;
constexpr int NWG = 256, NT = 512;
constexpr u32 SPIN_CAP = 1u << 16;
constexpr float SCL = 0.07216878364870322f * 1.4426950408889634f;

#define T_O 40
#define T_YF 126
#define T_H 128
#define T_AN 30
#define T_AO 115
#define PTHR 60
#define T_LAZY 60

#define D_E 840
#define D_Q 505
#define T_O2 100
#define D_Y 1505
#define D_H 590
#define D_F 395
#define A_E D_E
#define A_Q (A_E + D_Q)
#define A_Y (A_Q + D_Y)
#define A_H (A_Y + D_H)
#define A_F (A_H + D_F)

constexpr u32 SIDE = 8192, AREA = 16 * 16384;
constexpr u32 A_GO = 0;
constexpr u32 A_GQ = 4 * 16384;
constexpr u32 A_GH = A_GQ + 7168;
constexpr u32 A_GY = 8 * 16384;
constexpr u32 GO_SZ = 32768, GY_PAIR = 65536, GY_INST = 32768;
constexpr u32 WS_CLR = 4 * AREA;
constexpr u32 WS_PB = WS_CLR;
#define HBS 4352
constexpr u32 WS_GOF = WS_PB + (128 << 10), WS_HB = WS_GOF + 8 * HBS;
__device__ __forceinline__ u32 rp_line(int e, int r, int w) { return WS_PB + (u32)((e * 8 + r) * 8 + w) * 384u; }
#define Q1S 2048
constexpr u32 WS_GQ1 = WS_GOF + (192 << 10);
constexpr u32 WS_END = WS_GQ1 + 256 * 2048;
static_assert(WS_END <= (2u << 20) && WS_CLR == 256 * 4096, "layout");
__device__ __forceinline__ u32 go_slot(int w) { return (u32)(w >> 2) * 16384u + (u32)(w & 3) * 2048u; }
__device__ __forceinline__ u32 gy_row(int i) { return go_slot(i); }
__device__ __forceinline__ u32 gy_chunk(int u) {
  const u32 i = __umul24(u, 205) >> 11;
  return (i >> 2) * 8192u + __umul24(i, 2048 - 160) + (u32)u * 16u;
}
__device__ __forceinline__ u32 st_line(int g) { return A_GH + 768u + (u32)(g >> 1) * 16384u + (u32)(g & 1) * 128u; }
__device__ __forceinline__ u32 gy_rep(int r) { return (u32)(r & 3) * AREA + (u32)(r >> 2) * SIDE; }

constexpr int L_ZERO = 0;
constexpr int L_CNT = 16;
constexpr int L_ONE = 32;
constexpr int L_TAG = 48;
constexpr int L_ST = 64;
constexpr int L_VOTE = L_ST;
constexpr int L_ANC = 192;
constexpr int L_CLK = 200;
constexpr int L_SUM = 208;
constexpr int L_AR = 224;
constexpr int L_Q = 256;
constexpr int L_CS = 640;
constexpr int L_HB = L_CS + 768;
constexpr int L_ACT = L_HB + D * 2;
constexpr int L_RED = L_ACT + 2 * 384;
constexpr int L_P = L_RED + 32 * 144;
constexpr int P_W = 1280, P_U = 592, P_PAR = 288;
constexpr int OM_S = 416;
constexpr int L_OM = L_P + 8 * P_W;
constexpr int L_LN = L_OM;
constexpr int L_LAND = (L_OM + 24 * OM_S + 512 + 255) / 256 * 256;
constexpr int L_WQ = L_LAND + 8 * 2048;
constexpr int L_WB = L_WQ + 6 * 5120;
constexpr int L_W2 = L_WB + 768 * 64;
constexpr int L_LAND2 = L_WB + 640 * 64 - L_LAND;
constexpr int L_PF = L_W2 + 192 * 64;
constexpr int L_TAB = L_PF + 256;
constexpr int L_TOTAL = L_TAB + 8 * 2048;
static_assert(L_LAND2 % 256 == 0 && L_LAND % 256 == 0 && L_WB % 64 == 0 && L_P % 16 == 0 && L_WQ % 16 == 0 && L_HB % 16 == 0 && L_RED % 16 == 0, "alignment");


struct Params {
  const u16* x0;
  const u16* kc;
  const u16* vc;
  const u16* wq;
  const u16* wo;
  const u16* w1;
  const u16* w3;
  const u16* w2;
  u16* out;
  u32* ws;
};

__device__ __forceinline__ u32 cvt2(float lo, float hi) {
  return __builtin_bit_cast(u32, __builtin_convertvector(f32x2{lo, hi}, bf16x2));
}
__device__ __forceinline__ float bflo(u32 w) { return __uint_as_float(w << 16); }
__device__ __forceinline__ float bfhi(u32 w) { return __uint_as_float(w & 0xffff0000u); }
__device__ __forceinline__ float bf16r(float x) { return bflo(cvt2(x, x)); }

template <int CTRL>
__device__ __forceinline__ float dpp(float x) {
  return __builtin_bit_cast(float, __builtin_amdgcn_mov_dpp(__builtin_bit_cast(int, x), CTRL, 0xf, 0xf, true));
}
constexpr int XOR1 = 0xB1, XOR2 = 0x4E, XOR7 = 0x141, ROR8 = 0x128;
__device__ __forceinline__ float fmx(float a, float b) {
  float r;
  asm("v_max_f32 %0, %1, %2" : "=v"(r) : "v"(a), "v"(b));
  return r;
}
__device__ __forceinline__ float xrow16_max(float x) {
  auto s = __builtin_amdgcn_permlane16_swap(__float_as_uint(x), __float_as_uint(x), false, false);
  x = fmx(__uint_as_float(s[0]), __uint_as_float(s[1]));
  auto t = __builtin_amdgcn_permlane32_swap(__float_as_uint(x), __float_as_uint(x), false, false);
  return fmx(__uint_as_float(t[0]), __uint_as_float(t[1]));
}
__device__ __forceinline__ float xrow16_sum(float x) {
  auto s = __builtin_amdgcn_permlane16_swap(__float_as_uint(x), __float_as_uint(x), false, false);
  x = __uint_as_float(s[0]) + __uint_as_float(s[1]);
  auto t = __builtin_amdgcn_permlane32_swap(__float_as_uint(x), __float_as_uint(x), false, false);
  return __uint_as_float(t[0]) + __uint_as_float(t[1]);
}
__device__ __forceinline__ float sum8(float v) {
  v += dpp<XOR1>(v);
  v += dpp<XOR2>(v);
  v += dpp<XOR7>(v);
  return v;
}
__device__ __forceinline__ float max8(float v) {
  asm("s_nop 1\n\tv_max_f32_dpp %0, %0, %0 quad_perm:[1,0,3,2] row_mask:0xf bank_mask:0xf\n\t"
      "s_nop 1\n\tv_max_f32_dpp %0, %0, %0 quad_perm:[2,3,0,1] row_mask:0xf bank_mask:0xf\n\t"
      "s_nop 1\n\tv_max_f32_dpp %0, %0, %0 row_half_mirror row_mask:0xf bank_mask:0xf"
      : "+v"(v));
  return v;
}
__device__ __forceinline__ float max_ror8(float v) {
  asm("s_nop 1\n\tv_max_f32_dpp %0, %0, %0 row_ror:8 row_mask:0xf bank_mask:0xf" : "+v"(v));
  return v;
}
__device__ __forceinline__ float wave_sum(float v) {
  v = sum8(v);
  v += dpp<ROR8>(v);
  return xrow16_sum(v);
}
__device__ __forceinline__ float tsum8(f32x4 a) {
  auto s0 = __builtin_amdgcn_permlane32_swap(__float_as_uint(a[0]), __float_as_uint(a[1]), false, false);
  auto s1 = __builtin_amdgcn_permlane32_swap(__float_as_uint(a[2]), __float_as_uint(a[3]), false, false);
  const float r0 = __uint_as_float(s0[0]) + __uint_as_float(s0[1]);
  const float r1 = __uint_as_float(s1[0]) + __uint_as_float(s1[1]);
  auto t = __builtin_amdgcn_permlane16_swap(__float_as_uint(r0), __float_as_uint(r1), false, false);
  float r = __uint_as_float(t[0]) + __uint_as_float(t[1]);
  r += dpp<ROR8>(r);
  return r;
}

__device__ __forceinline__ u32 now() { return (u32)__builtin_amdgcn_s_memtime(); }
__device__ __forceinline__ void wait_until(u32 t) {
#pragma unroll 1
  while ((int)(now() - t) < -160) __builtin_amdgcn_s_sleep(1);
#pragma unroll 1
  while ((int)(now() - t) < 0) {
  }
}
__device__ __forceinline__ u32 rtnow() { return (u32)__builtin_amdgcn_s_memrealtime(); }
__device__ __forceinline__ void wait_tt(u32 a) {
  asm volatile("s_mov_b32 s80, %0\n\ts_call_b64 s[78:79], .Lmk_wait"
               :
               : "is"(a)
               : "s78", "s79", "s80", "s81", "s82", "s83", "s84", "s85", "s86", "s87", "s88", "s89", "s90", "s91", "s92", "s93", "s94",
                 "s95", "s96", "s97", "s98", "s99", "v62", "v63", "scc", "memory");
}
__device__ __forceinline__ void lds_wait(volatile LAS u32* p, u32 want) {
  u32 spins = 0;
#pragma unroll 1
  while (__builtin_amdgcn_readfirstlane(*p) != want) {
    if (++spins > (SPIN_CAP << 6)) break;
    __builtin_amdgcn_s_sleep(1);
  }
  asm volatile("" ::: "memory");
}
__device__ __forceinline__ void stamp_poll(unsigned char* smem, u32x4 rq, u32 so) {
  u32 sv, budget = SPIN_CAP;
#pragma unroll 1
  for (;;) {
    asm volatile("buffer_load_dword %0, %1, %2, 0 offen sc1\n\ts_waitcnt vmcnt(0)" : "=&v"(sv) : "v"(so), "s"(rq) : "memory");
    if (__builtin_amdgcn_ballot_w64((sv & 1u) == 0u) == 0ull || budget == 0u) break;
    --budget;
    __builtin_amdgcn_s_sleep(1);
  }
  const u32 nw = rtnow() * 8u + 32u;
  const float age = (float)((nw - (sv >> 1)) & 0xffffffu);
  *(volatile LAS u32x2*)(smem + L_ANC) = u32x2{nw - (u32)-max8(-age), (u32)max8((float)(sv >> 25))};
}
__device__ __forceinline__ void lds_add(unsigned char* smem, int off, u32 v) {
  __hip_atomic_fetch_add((LAS u32*)(smem + off), v, __ATOMIC_RELAXED, __HIP_MEMORY_SCOPE_WORKGROUP);
}

__device__ __forceinline__ int fresh(int t) {
  asm volatile("" : "+v"(t));
  return t;
}
__device__ __forceinline__ int fresh6(int t) {
  t = fresh(t);
  __builtin_assume((unsigned)t < 64u);
  return t;
}
__device__ __forceinline__ f32x4 zero4() {
  f32x2 a, b;
  asm volatile("v_mov_b64 %0, 0\n\tv_mov_b64 %1, 0" : "=v"(a), "=v"(b));
  return f32x4{a[0], a[1], b[0], b[1]};
}
__device__ __forceinline__ void bar() { asm volatile("s_waitcnt lgkmcnt(0)\n\ts_barrier" ::: "memory"); }

__device__ __forceinline__ u32x2 trread(unsigned char* smem, int off) {
  return __builtin_bit_cast(u32x2, __builtin_amdgcn_ds_read_tr16_b64_v4i16((LAS s16x4*)(smem + off)));
}
__device__ __forceinline__ u32x4 wfrag(unsigned char* smem, int lb, int off) {
  const u32x2 a = trread(smem, lb + off), b = trread(smem, lb + off + 256);
  return u32x4{a[0], a[1], b[0], b[1]};
}
__device__ __forceinline__ int wlane(int lane, int cb) {
  const int i = lane & 15, G = lane >> 4;
  return (8 * G + (i >> 2)) * 64 + (i & 3) * 8 + ((cb ^ (G & 1)) * 32);
}
__device__ __forceinline__ u32x4 afrag(unsigned char* smem, int a) { return *reinterpret_cast<const u32x4*>(smem + a); }
__device__ __forceinline__ f32x4 mfma32(u32x4 w, u32x4 a, f32x4 c) {
  return __builtin_amdgcn_mfma_f32_16x16x32_bf16(__builtin_bit_cast(bf16x8, w), __builtin_bit_cast(bf16x8, a), c, 0, 0, 0);
}

__device__ __forceinline__ void dma_piece(const u16* src, int ld, unsigned char* smem, int ldsoff, int p, int lane) {
  const int row = 16 * p + (lane >> 2), part = lane & 3;
  const int cb = (part >> 1) ^ (lane >> 5);
  const u32 voff = (u32)(row * ld + cb * 16 + (part & 1) * 8) * 2u;
  const u32 dst = (u32)(uintptr_t)(LAS unsigned char*)smem + (u32)(ldsoff + p * 1024);
  u32 keep;
  asm volatile("s_mov_b32 %0, m0\n\ts_mov_b32 m0, %2\n\ts_nop 2\n\tglobal_load_lds_dwordx4 %1, %3 nt\n\ts_mov_b32 m0, %0"
               : "=&s"(keep)
               : "v"(voff), "s"(dst), "s"(src)
               : "memory");
}

__device__ __forceinline__ float sum4(f32x4 d) { return (d[0] + d[1]) + (d[2] + d[3]); }
template <int AUX = 16>
__device__ __forceinline__ void store16(__amdgpu_buffer_rsrc_t rs, u32 off, u32x4 v) {
  __builtin_amdgcn_raw_buffer_store_b128(v, rs, off, 0, AUX);
}

struct G80 {
  u32x4 w[6];
  int ra, rb, rc;
};
__device__ __forceinline__ void g80_load(G80& a, unsigned char* smem, int lane, int wb, int xs, int land, int one) {
  const int m = lane & 15, G = lane >> 4;
  const int l0 = wb + wlane(lane, 0), l1 = wb + wlane(lane, 1), bk = 4096 - (G >> 1) * 1024;
  a.w[0] = wfrag(smem, l0, 0);
  a.w[1] = wfrag(smem, l1, 0);
  a.w[2] = wfrag(smem, l0, 2048);
  a.w[3] = wfrag(smem, l1, 2048);
  a.w[4] = wfrag(smem, l0 + bk, 0);
  a.w[5] = wfrag(smem, l1 + bk, 0);
  const bool vec = m <= 8;
  const int st = vec ? 64 : 0;
  const int sa = vec ? (m == 8 ? xs : land + 256 * m) + 16 * G : (m == 15 ? one : L_ZERO);
  const int sw = m < 8 ? (m & 3) * 64 : 0;
  a.ra = fresh(sa ^ sw);
  a.rb = fresh((sa + st) ^ sw);
  a.rc = fresh(G < 2 ? (sa + 2 * st) ^ sw : L_TAG);
}
template <bool X4, bool NT = false>
__device__ __forceinline__ void ctl_load(u32x4 rq, u32 voff, u32 lds) {
  u32 keep;
  if (NT)
    asm volatile("s_mov_b32 %0, m0\n\ts_mov_b32 m0, %3\n\ts_nop 2\n\tbuffer_load_dwordx4 %1, %2, 0 offen sc1 nt lds\n\ts_mov_b32 m0, %0"
                 : "=&s"(keep) : "v"(voff), "s"(rq), "s"(lds) : "memory");
  else if (X4)
    asm volatile("s_mov_b32 %0, m0\n\ts_mov_b32 m0, %3\n\ts_nop 2\n\tbuffer_load_dwordx4 %1, %2, 0 offen sc1 lds\n\ts_mov_b32 m0, %0"
                 : "=&s"(keep) : "v"(voff), "s"(rq), "s"(lds) : "memory");
  else
    asm volatile("s_mov_b32 %0, m0\n\ts_mov_b32 m0, %3\n\ts_nop 2\n\tbuffer_load_dword %1, %2, 0 offen sc1 lds\n\ts_mov_b32 m0, %0"
                 : "=&s"(keep) : "v"(voff), "s"(rq), "s"(lds) : "memory");
}
#define MF "v_mfma_f32_16x16x32_bf16 "
#define F0 "v[52:55]"
#define F1 "v[56:59]"
#define F2 "v[60:63]"
#define V52 "v52", "v53", "v54", "v55", "v56", "v57", "v58", "v59", "v60", "v61", "v62", "v63"
#define V40 "v40", "v41", "v42", "v43", "v44", "v45", "v46", "v47", "v48", "v49", "v50", "v51"
#define V36 "v36", "v37", "v38", "v39"
#define V32 "v32", "v33", "v34", "v35"
#define S80 "s80", "s81", "s82", "s83", "s84", "s85", "s86", "s87", "s88", "s89", "s90", "s91", "vcc", "scc", "memory"
#define AND3(a, b, c) "v_bitop3_b32 %[t], " a ", " b ", " c " bitop3:0x80\n\t"
#define ALL " row_mask:0xf bank_mask:0xf\n\t"
#define Q1 " quad_perm:[1,0,3,2]" ALL
#define Q2 " quad_perm:[2,3,0,1]" ALL
#define HM " row_half_mirror" ALL
#define R8 " row_ror:8" ALL
#define PK(a, b) "v_pk_add_f32 v[" #a ":" #a "+1], v[" #a ":" #a "+1], v[" #b ":" #b "+1]\n\t"
#define CV "v_cvt_pk_bf16_f32 "
constexpr u32 OOB = 0x80000000u;
#define POLL_HEAD                                                                                                     \
  "s_mov_b32 m0, %[land]\n\ts_mov_b32 s88, 0x10000\n\ts_mov_b32 s80, 0xff00ff\n\ts_mov_b32 s81, s80\n\t"               \
  "s_mov_b64 s[84:85], s[82:83]\n\ts_mov_b64 s[86:87], s[82:83]\n\t"
#define POLL_AGAIN                                                                                                    \
  "s_sub_u32 s88, s88, 1\n\ts_cbranch_scc1 3f\n\ts_or_b32 s84, vcc_lo, vcc_hi\n\ts_lshr_b32 s85, s84, 16\n\t"          \
  "s_or_b32 s84, s84, s85\n\ts_lshr_b32 s85, s84, 8\n\ts_or_b32 s84, s84, s85\n\t"                                     \
  "s_bitreplicate_b64_b32 s[84:85], s84\n\ts_bitreplicate_b64_b32 s[84:85], s84\n\t"                                   \
  "s_bitreplicate_b64_b32 s[84:85], s84\n\ts_bitreplicate_b64_b32 s[86:87], s85\n\t"                                   \
  "s_bitreplicate_b64_b32 s[84:85], s84\n\ts_and_b64 s[84:85], s[84:85], s[82:83]\n\t"                                 \
  "s_and_b64 s[86:87], s[86:87], s[82:83]\n\ts_branch 4f\n1:\n\ts_waitcnt lgkmcnt(0)\n\ts_barrier\n4:\n\t"             \
  "s_mov_b64 exec, s[84:85]\n\tbuffer_load_dwordx4 %[vo], %[rq], 0 offen sc1 lds\n\t"                                  \
  "s_mov_b64 exec, s[86:87]\n\tbuffer_load_dwordx4 %[vo], %[rq], 0 offen offset:1024 sc1 lds\n\ts_mov_b64 exec, -1\n\t"
#define POLL_READ \
  "s_waitcnt vmcnt(0)\n\tds_read_b128 " F0 ", %[ra]\n\tds_read_b128 " F1 ", %[rb]\n\tds_read_b128 " F2 ", %[rc]\n\t"
#define USED10 "s_mov_b32 s82, 0x30ff03ff\n\ts_mov_b32 s83, 0xff30ff03\n\t"
#define WIN6(a) [w0] "v"(a[0]), [w1] "v"(a[1]), [w2] "v"(a[2]), [w3] "v"(a[3]), [w4] "v"(a[4]), [w5] "v"(a[5])
__device__ __forceinline__ f32x4 quad4(float r) { return f32x4{r, dpp<0x55>(r), dpp<0xAA>(r), dpp<0xFF>(r)}; }

__device__ __forceinline__ int q_addr(int d) {
  const int w = d >> 1;
  return L_Q + ((w & 1) * 48 + (w >> 1)) * 4 + (d & 1) * 2;
}

__device__ __forceinline__ void q1_finish(unsigned char* smem, int lane, int wave) {
  const int e = wave * 32 + (lane & 7) * 4 + ((lane >> 4) & 1) * 2 + (lane >> 5);
  asm volatile("" ::: "memory");
  const u32x4 v = afrag(smem, L_LAND + wave * 2048 + lane * 16);
  const float t = tsum8(f32x4{bflo(v[0]), bflo(v[1]), bflo(v[2]), bflo(v[3])});
  *reinterpret_cast<u16*>(smem + q_addr(e)) = (u16)cvt2(t, t);
  *reinterpret_cast<float*>(smem + L_CS + e * 4) = tsum8(f32x4{bfhi(v[0]), bfhi(v[1]), bfhi(v[2]), bfhi(v[3])}) * (-SCL / D);
}
__device__ __forceinline__ void x_request(u32x4 rq, int lane, u32 src, int land) {
  asm volatile("s_mov_b32 m0, %2\n\ts_nop 0\n\tbuffer_load_dwordx4 %0, %1, 0 offen sc1 lds\n\t"
               "buffer_load_dwordx4 %0, %1, 0 offen offset:1024 sc1 lds"
               :
               : "v"(src + ((lane * 16) ^ ((lane >> 4) * 64))), "s"(rq), "s"(land)
               : "memory");
}
template <bool POLL>
__device__ __forceinline__ void x_update(unsigned char* smem, u32x4 rq, int lane, u32 src, int land, int i) {
  const int l = lane < 40 ? lane : 0;
  const u32 off = land + 4 * l;
  u32 v[8];
  u32 budget = SPIN_CAP;
#pragma unroll 1
  for (;;) {
    if (POLL) asm volatile("s_waitcnt vmcnt(0)" ::: "memory");
#pragma unroll
    for (int k = 0; k < 8; ++k) v[k] = *reinterpret_cast<const u32*>(smem + (off ^ ((k & 3) * 64)) + 256 * k);
    const u32 a = v[0] & v[1] & v[2] & v[3] & v[4] & v[5] & v[6] & v[7] & 1u;
    if (!POLL || __builtin_amdgcn_ballot_w64(a == 0u) == 0ull || budget == 0u) break;
    --budget;
    x_request(rq, lane, src, land);
  }
  float lo = 0.f, hi = 0.f;
#pragma unroll
  for (int k = 0; k < 8; ++k) {
    lo += bflo(v[k]);
    hi += bfhi(v[k]);
  }
  u32* px = reinterpret_cast<u32*>(smem + L_HB + 160 * i + 4 * l);
  const u32 xo = *px, y = cvt2(lo, hi);
  if (lane < 40) *px = cvt2(bflo(xo) + bflo(y), bfhi(xo) + bfhi(y));
}
template <int N>
__device__ __forceinline__ void col_poll(u32x4 rq, const u32 (&off)[N], float (&y)[N]) {
  u32x2 v[N];
  u32 budget = SPIN_CAP;
#pragma unroll 1
  for (;;) {
    u32 a = 1u;
#pragma unroll
    for (int k = 0; k < N; ++k)
      asm volatile("buffer_load_dwordx2 %0, %1, %2, 0 offen sc1" : "=v"(v[k]) : "v"(off[k]), "s"(rq) : "memory");
    asm volatile("s_waitcnt vmcnt(0)" ::: "memory");
#pragma unroll
    for (int k = 0; k < N; ++k) {
      asm volatile("" : "+v"(v[k]));
      a &= v[k][0] & v[k][1];
    }
    if (__builtin_amdgcn_ballot_w64(a == 0u) == 0ull || budget == 0u) break;
    --budget;
  }
#pragma unroll
  for (int k = 0; k < N; ++k) y[k] = bf16r(tsum8(f32x4{bflo(v[k][0]), bfhi(v[k][0]), bflo(v[k][1]), bfhi(v[k][1])}));
}

#define SJ 31
#define DWIN 250
#define DEQ 0.9f
#define PFB 16
#define AUXQ 18
#define UQ 1
#define UAD 4
#define UB 3
#define UL 5
#define UG 6
#define UK 7
#define UC 8
#define UW 8
#define UF 10
#define UX 11
#define TSTEP 236
#define TSV 232
#define USW 4
#define TSQ 192
#define WOFS 21
#define WBK 60
#define NINO 4
#define NINF 1
#define NINS 2
#define ORDO 0x543210u
#define ORDF 0x34210u
#define NPRE 2
#define NPF 2
#define TPF 220
#define T_FW 30
#define T_FD 12
#define WU0 3
#define WSP 2
#define WPC 100
#define NUN 1
static_assert(NINS + NPF < 5 && NINS <= NINO && NINO + NPRE == 6 && NINF + NPF < 5 && NPF == NPRE, "counts");
static_assert(WU0 + NINO * WSP <= UX + 1, "pieces before the scores");
#define TPV 220
#define TSE TSV
constexpr int wcnt(int u) { return u < WU0 || u >= WU0 + NINO * WSP ? 0 : (20 + ((u - WU0) / WSP < NINF ? 12 : ((u - WU0) / WSP < NINS ? 2 : 0))) / WSP; }
#define PQ(u) ((((u) < USW ? TSTEP : TSV) * (9600 + wcnt(u) * WPC)) / 12800)
#define ROW0 ((wave * 8 + c) * 8)
#define GST 512
#define ROT h
#define PACE_ASM                                                                                                     \
  "1:\n\ts_memrealtime s[98:99]\n\ts_waitcnt lgkmcnt(0)\n\ts_lshl_b32 s98, s98, 3\n\ts_sub_i32 s98, s98, %[tn]\n\t"  \
  "s_cmp_lt_i32 s98, 0\n\ts_cbranch_scc0 2f\n\ts_sleep 1\n\ts_branch 1b\n2:\n\ts_add_i32 %[tn], %[tn], %[ts]\n\t"
__device__ __forceinline__ void pace(u32& tnext, u32 tstep) {
  asm volatile(PACE_ASM : [tn] "+s"(tnext) : [ts] "s"(tstep) : "scc", "s98", "s99");
}
constexpr int kreg(int i) { return i % 3 < 2 ? 64 + 8 * (i / 3) + 4 * (i % 3) : 128 + 4 * (i / 3); }
constexpr int vreg(int i) { return 160 + 24 * (i / 6) + 8 * (i % 3) + 4 * ((i / 3) & 1); }
constexpr int treg(int t) { return t < 24 ? kreg(t) : vreg(t - 24); }
template <int V>
struct Cn {
  static constexpr int v = V;
};
template <int A, int B, class Fn>
__device__ __forceinline__ void slots(Fn& f) {
  if constexpr (A < B) {
    f(Cn<A>{});
    slots<A + 1, B>(f);
  }
}
#define PRT 200
#define STR2(x) #x
#define STR(x) STR2(x)
#define PRIO_ASM                                                                                                     \
  "s_setprio 0\n\ts_cmpk_lt_i32 s98, " STR(PRT) "\n\ts_cbranch_scc1 4f\n\ts_setprio 1\n\ts_cmpk_lt_i32 s98, 3*" STR(PRT) "\n\t"  \
  "s_cbranch_scc1 4f\n\ts_setprio 2\n\ts_cmpk_lt_i32 s98, 6*" STR(PRT) "\n\ts_cbranch_scc1 4f\n\ts_setprio 3\n4:\n\t"
#define LD1(r) "global_load_dwordx4 v[%[" #r "]:%[" #r "]+3], %[p], %[sb] offset:"
#define BURST(u, voff, sbase)                                                                                       \
  asm volatile(PACE_ASM PRIO_ASM LD1(a) "0 nt\n\t" LD1(b) "128 nt\n\t" LD1(c) "256 nt\n\t"                                    \
               "s_waitcnt vmcnt(%[w]+2)\n\ts_cmp_gt_u32 %[ex], 1\n\ts_cbranch_scc1 3f\n\t"                          \
               "s_waitcnt vmcnt(%[w]+1)\n\ts_cmp_gt_u32 %[ex], 0\n\ts_cbranch_scc1 3f\n\ts_waitcnt vmcnt(%[w])\n3:"  \
               : [tn] "+s"(tnext)                                                                                   \
               : [a] "n"(treg(3 * (u))), [b] "n"(treg(3 * (u) + 1)), [c] "n"(treg(3 * (u) + 2)), [p] "v"(voff),     \
                 [sb] "s"(sbase), [ts] "s"(tstep), [ex] "s"(exa), [w] "n"(6)                                        \
               : "scc", "s98", "s99");                                                                              \
  exa = exl;                                                                                                        \
  exl = 0u
#define LOADW(t, voff, sbase)                                                                                       \
  asm volatile(PACE_ASM PRIO_ASM LD1(a) "%[im] nt\n\ts_waitcnt vmcnt(9)"                                                    \
               : [tn] "+s"(tnext)                                                                                   \
               : [a] "n"(treg(t)), [p] "v"(voff), [sb] "s"(sbase), [im] "n"(128 * ((t) % 3)), [ts] "s"(tstep)       \
               : "scc", "s98", "s99")

#define HS_WAIT(COND)                                                                                \
  _Pragma("unroll 1") for (;;) {                                                                     \
    gd = CTL[512 + lane0] - CTL[576 + lane0];                                                        \
    const u64 ch = __builtin_amdgcn_ballot_w64(gd != 0u) & 0x1ffull;                                 \
    if (ch > 0xffull || (COND) || tmo == 0u) break;                                                  \
    if ((tmo & 3u) == 0u && lane0 == 0 && cnt[1] >= 7u)                                              \
      __builtin_amdgcn_raw_buffer_store_b32(hb, rs, WS_HB + h * HBS + c * 4, 0, 16);                 \
    hb += 4u;                                                                                        \
    --tmo;                                                                                           \
    __builtin_amdgcn_s_sleep(3);                                                                     \
    ctl_load<false>(rq, hso, (u32)(smb + L_CTL + 2048));                                             \
    asm volatile("s_waitcnt vmcnt(0)" ::: "memory");                                                 \
  }

struct Att {
  int qad[6], idxq, wa, ra, idx, oa, sa, la, ma, qw;
  u32 dst;
};
template <int K>
__device__ __forceinline__ void att_table(unsigned char* smem, int lane, int wave, u32 go) {
  const int n = lane & 15, G = lane >> 4;
  const int seg = n & 1, pr = (n >> 1) & 1, tq = n >> 2, xy = G >> 1;
  const int qa = L_Q + (pr * 48 + seg * 8 + (G & 1) * 4) * 4;
  u32 t[16];
#pragma unroll
  for (int k = 0; k < 4; ++k) t[k] = tq == k ? qa + xy * 64 : L_ZERO;
#pragma unroll
  for (int k = 0; k < 2; ++k) t[4 + k] = tq == 2 * k + xy ? qa + 128 : L_ZERO;
  t[6] = 0x4444 + pr * 0xAAAA;
  t[7] = L_P + wave * P_W + (n & 1) * P_PAR + ((((G & 1) * 2 + ((n >> 2) & 1)) * 4 + (n >> 3) * 2 + (G >> 1)) * 16) + (n & 2) * 4;
  t[8] = L_P + wave * P_W + ((n >> 2) & 1) * P_U + (n >> 3) * P_PAR + G * 64;
  t[9] = (n & 3) * 0x5555;
  t[10] = L_OM + (wave * 2 + (n >> 3)) * OM_S + (n & 7) * 16 + (G & 1) * 8;
  t[11] = L_OM + (16 + (wave >> 2) * 8) * OM_S + (wave & 3) * 128 + lane * 2;
  t[12] = wave == 3 ? L_OM + 16 * OM_S + lane * 8 : L_OM + ((G & 1) * 4 + tq) * OM_S + wave * 128 + xy * 32 + (n & 3) * 8;
  t[13] = wave == 3 || n == xy ? L_ONE : L_ZERO;
  const int d0 = wave * 64 + n * 16 + G * 4;
  t[14] = (go + (wave < 3 ? (d0 >= 96 ? 2048 - 192 : 0) + d0 * 2 : (lane & 1) * 2048 + 192 + xy * 8)) & 0xffffu;
  t[15] = wave < 6 ? q_addr(wave * 32 + (lane & 7) * 4 + ((lane >> 4) & 1) * 2 + (lane >> 5)) : L_SUM;
  *reinterpret_cast<u32x4*>(smem + L_TAB + wave * 2048 + K * 1024 + lane * 16) =
      u32x4{t[8 * K] | (t[8 * K + 4] << 16), t[8 * K + 1] | (t[8 * K + 5] << 16), t[8 * K + 2] | (t[8 * K + 6] << 16),
            t[8 * K + 3] | (t[8 * K + 7] << 16)};
}
__device__ __forceinline__ void att_setup(Att& r, unsigned char* smem, int lane, int wave) {
  const u32x4 a = afrag(smem, L_TAB + wave * 2048 + lane * 16), b = afrag(smem, L_TAB + wave * 2048 + 1024 + lane * 16);
  r.qw = fresh(b[3] >> 16);
#pragma unroll
  for (int k = 0; k < 4; ++k) r.qad[k] = fresh(a[k] & 0xffffu);
  r.qad[4] = fresh(a[0] >> 16);
  r.qad[5] = fresh(a[1] >> 16);
  r.idxq = fresh(a[2] >> 16);
  r.wa = fresh(a[3] >> 16);
  r.ra = fresh(b[0] & 0xffffu);
  r.idx = fresh(b[1] & 0xffffu);
  r.oa = fresh(b[2] & 0xffffu);
  r.la = fresh(b[3] & 0xffffu);
  r.sa = fresh(b[0] >> 16);
  r.ma = fresh(b[1] >> 16);
  r.dst = fresh((wave < 3 ? lane & 12 : ~lane & 30) ? OOB : b[2] >> 16);
}
#define SM "v_smfmac_f32_16x16x64_bf16 "
#define QNOP "s_nop 0\n\t"
#define OEARLY 0xff
#define SAFEB "0x42200000"

using u32x16 = __attribute__((ext_vector_type(16))) unsigned;
using u32x32 = __attribute__((ext_vector_type(32))) unsigned;
template <int OFF>
__device__ __forceinline__ u16* karg() {
  u64 p;
  asm volatile("s_load_dwordx2 %0, %1, %2\n\ts_waitcnt lgkmcnt(0)" : "=s"(p) : "s"(__builtin_amdgcn_kernarg_segment_ptr()), "n"(OFF));
  return (u16*)p;
}
template <class T>
__device__ __forceinline__ T* mkptr(u32 lo, u32 hi) {
  return (T*)(((u64)hi << 32) | lo);
}

__global__ __launch_bounds__(NT, 2) void mega_kernel(Params) {
  __shared__ __attribute__((aligned(16))) unsigned char smem[L_TOTAL];
  u32x16 ka;
  u32x4 kb4;
  u32 rtt;
  u64 ts64;
  {
    static_assert(AREA == 1 << 18 && A_GY == 1 << 17 && SIDE == 1 << 13, "clear addresses");
    asm volatile("s_load_dwordx16 s[36:51], %[kp], 0x0\n\ts_load_dwordx4 s[52:55], %[kp], 0x40\n\t"
                 "s_getpc_b64 s[58:59]\n.Lmk_pc:\n\t"
                 "s_mov_b32 s62, m0\n\ts_mov_b32 m0, %[dst]\n\t"
                 "s_cmpk_lt_u32 %[b], %[pfb]\n\ts_cselect_b32 s57, 0x140, 0\n\t"
                 "v_lshlrev_b32 v1, 7, %[tid]\n\tv_min_u32 v1, .Lmk_code_end-.Lmk_pc-4, v1\n\t"
                 "v_cmp_gt_u32 vcc, s57, %[tid]\n\ts_mov_b64 exec, vcc\n\t"
                 "global_load_lds_dword v1, s[58:59]\n\ts_mov_b64 exec, -1\n\t"
                 "v_readfirstlane_b32 s56, %[tid]\n\t"
                 "v_lshrrev_b32 v1, 2, %[tid]\n\tv_cmp_eq_u32 vcc, 3, v1\n\tv_mov_b32 v3, 0x3f803f80\n\ts_lshr_b32 s57, %[b], 5\n\t"
                 "v_cndmask_b32 v2, 0, 1, vcc\n\tv_cmp_eq_u32 vcc, 2, v1\n\tv_lshlrev_b32 v1, 2, %[tid]\n\ts_bfe_u32 s60, %[b], 0x20003\n\t"
                 "v_cndmask_b32 v2, v2, v3, vcc\n\tv_cmp_eq_u32 vcc, 52, %[tid]\n\tv_mov_b32 v3, %[scl]\n\t"
                 "v_min_u32 v1, 0xfc, v1\n\tv_cndmask_b32 v2, v2, v3, vcc\n\t"
                 "ds_write_b32 v1, v2 offset:%[sm]\n\t"
                 "v_lshlrev_b32 v2, 4, %[tid]\n\tv_and_b32 v1, 0x3ff, v2\n\t"
                 "s_cmpk_ge_u32 s56, 448\n\ts_cbranch_scc1 1f\n\t"
                 "s_mul_i32 s57, s57, 0xf0000\n\ts_and_b32 s61, %[b], 7\n\ts_lshl2_add_u32 s60, s61, s60\n\t"
                 "v_bfe_u32 v3, %[tid], 2, 4\n\ts_mul_i32 s60, s60, 0x180\n\tv_mul_u32_u24 v3, 0x3000, v3\n\t"
                 "s_add_u32 s57, s57, s56\n\tv_bitop3_b32 v2, v2, %[tid], 32 bitop3:0x78\n\ts_add_u32 s57, s57, s60\n\t"
                 "v_and_or_b32 v3, v2, 48, v3\n\ts_mul_i32 s60, s56, 80\n\tv_add_u32 v3, s57, v3\n\t"
                 "s_add_u32 s60, s60, %[lwq]\n\ts_cmpk_eq_u32 s56, 384\n\ts_cselect_b64 vcc, -1, 0\n\ts_cselect_b32 m0, %[lhb], s60\n\t"
                 "v_cndmask_b32 v3, v3, v1, vcc\n\ts_mov_b32 %[rtt], 0\n\t"
                 "s_waitcnt lgkmcnt(0)\n\ts_cselect_b64 s[58:59], s[36:37], s[42:43]\n\ts_barrier\n\t"
                 "global_load_lds_dwordx4 v3, s[58:59] nt\n\t"
                 "s_memrealtime %[ts]\n\ts_branch 2f\n"
                 "1:\n\ts_lshl_b32 s57, s57, 17\n\ts_lshl_b32 s60, s60, 13\n\ts_and_b32 s61, %[b], 15\n\ts_or_b32 s57, s57, s60\n\t"
                 "s_lshl_b32 s61, s61, 9\n\t"
                 "v_mov_b64 v[4:5], 0\n\ts_or_b32 s57, s57, s61\n\tv_mov_b64 v[6:7], 0\n\t"
                 "v_or_b32 v1, s57, v1\n\tv_xor_b32 v2, 0x2000, v1\n\ts_mov_b32 exec_hi, 0\n\t"
                 "s_waitcnt lgkmcnt(0)\n\t"
                 "s_add_u32 s58, s54, 0x1fffff\n\ts_addc_u32 s59, s55, 0\n\ts_and_b32 s58, s58, 0xffe00000\n\t"
                 "global_store_dwordx4 v1, v[4:7], s[58:59] sc1\n\tglobal_store_dwordx4 v2, v[4:7], s[58:59] sc1\n\t"
                 "s_barrier\n\ts_mov_b64 exec, -1\n\ts_memrealtime %[ts]\n\t"
                 "s_waitcnt vmcnt(1)\n\ts_memtime s[58:59]\n\ts_waitcnt vmcnt(0)\n\ts_memtime s[60:61]\n\ts_waitcnt lgkmcnt(0)\n\t"
                 "s_sub_u32 %[rtt], s60, s58\n"
                 "2:\n\ts_mov_b32 m0, s62\n\ts_waitcnt lgkmcnt(0)"
                 : "=&{s[36:51]}"(ka), "=&{s[52:55]}"(kb4), [rtt] "=&s"(rtt), [ts] "=&s"(ts64)
                 : [kp] "s"(__builtin_amdgcn_kernarg_segment_ptr()), [tid] "v"(threadIdx.x), [b] "s"(blockIdx.x), [dst] "n"(L_PF),
                   [scl] "n"(__builtin_bit_cast(u32, SCL)), [sm] "n"(0), [lwq] "n"(L_WQ), [lhb] "n"(L_HB), [pfb] "n"(PFB)
                 : "memory", "vcc", "scc", "v1", "v2", "v3", "v4", "v5", "v6", "v7", "s56", "s57", "s58", "s59", "s60", "s61", "s62");
  }
  const u32 tstart = (u32)ts64;
  const int tid = threadIdx.x, lane0 = tid & 63;
  const int wave = __builtin_amdgcn_readfirstlane(tid >> 6);
  const int b = blockIdx.x;
  const int g = b & 7, j = b >> 3, hsub = j & 3, c = j >> 2, h = g * 4 + hsub;
  const bool isO = j < 20;
  const bool leader = b == 0 && wave == 6;
  const u32 fslot = (u32)((j & 1) * 16384 + ((j - 20) >> 1) * 128);
  u32 tmo = SPIN_CAP;
  volatile LAS u32* cnt = (volatile LAS u32*)(smem + L_CNT);
  Params P;
  P.x0 = mkptr<const u16>(ka[0], ka[1]);
  P.kc = mkptr<const u16>(ka[2], ka[3]);
  P.vc = mkptr<const u16>(ka[4], ka[5]);
  P.wq = mkptr<const u16>(ka[6], ka[7]);
  P.wo = mkptr<const u16>(ka[8], ka[9]);
  P.w1 = mkptr<const u16>(ka[10], ka[11]);
  P.w3 = mkptr<const u16>(ka[12], ka[13]);
  P.ws = mkptr<u32>(kb4[2], kb4[3]);
  u32* const wsb = (u32*)(((uintptr_t)P.ws + 0x1fffffu) & ~(uintptr_t)0x1fffffu);
  const __amdgpu_buffer_rsrc_t rs = __builtin_amdgcn_make_buffer_rsrc(wsb, 0, (int)WS_END, 0x00020000);
  const u32x4 rq = {(u32)(uintptr_t)wsb, (u32)((uintptr_t)wsb >> 32) & 0xffffu, WS_END, 0x00020000u};

  u32 to2;
  u32 tref = 0u, ar = 0u;
  u32x32 kv0, kv1, kv2, kv3, kv4, kv5;
  asm volatile(""
               : "={v[64:95]}"(kv0), "={v[96:127]}"(kv1), "={v[128:159]}"(kv2), "={v[160:191]}"(kv3), "={v[192:223]}"(kv4),
                 "={v[224:255]}"(kv5));
  const int smb = (int)(uintptr_t)(LAS unsigned char*)smem;
  const u32 gop = A_GO + go_slot(hsub * 2) + c * 256;
  u32 tstep = 4 * PQ(0);
  u32 tnext = tstart * 8u + 5 * TSQ + (u32)(hsub * PQ(0) + wave * WOFS);
  const size_t rowbase = (size_t)h * SK + (SK - WIN) + ROW0;
  const u16* vb = P.vc + rowbase * DH;
  const int rot = ROT;
  const u16* ws0 = isO ? P.wo + (size_t)(768 * g) * D + 32 * j : ((j & 1) ? P.w3 : P.w1) + 192 * g + 32 * ((j - 20) >> 1);
  const int nin = isO ? NINO : ((((uintptr_t)ws0 >> 7) & 7) == 3 ? NINS : NINF);
  const u32 word = (((uintptr_t)ws0 >> 6) & 1) ? (isO ? ORDO : ORDF) : 0x543210u;
  int wn = 0;
  u32 hb = 0u;
  u32 exa = 0u, exl = 0u;
  {
    constexpr int L_CTL = L_LAND + 6 * 2048;
    volatile LAS u32* const CTL = (volatile LAS u32*)(smem + L_CTL);
    u32 gd;
    const u32 lho = WS_HB + (lane0 >> 1) * HBS + (lane0 & 1) * 16;
    const u32 hso = lane0 < 8 ? WS_HB + (u32)h * HBS + lane0 * 4 : WS_GOF + g * HBS;
    if (wave < 6) {
      const u16* s0 = P.wq + (size_t)(80 * c) * (H * DH) + h * DH + wave * 32;
      u32 tq = tstart * 8u + TSQ + (u32)wave * (TSTEP / 8);
#pragma unroll 1
      for (int p = 1; p < 5; ++p) {
        pace(tq, TSQ);
        dma_piece(s0, H * DH, smem, L_WQ + wave * 5120, p, lane0);
      }
    } else if (wave == 6) {
      u32 keep;
      asm volatile("s_mov_b32 %0, m0\n\ts_mov_b32 m0, %3\n\ts_nop 2\n\tglobal_load_lds_dword %1, %2 offset:1024\n\ts_mov_b32 m0, %0"
                   : "=&s"(keep)
                   : "v"(lane0 * 4), "s"(P.x0), "s"((u32)(smb + L_HB))
                   : "memory");
      if (j == SJ) store16(rs, rp_line(0, lane0 >> 3, g) + (lane0 & 7) * 16, u32x4{tstart, tstart, tstart, tstart});
      if (leader) {
        ctl_load<true>(rq, lho, (u32)(smb + L_CTL));
        ctl_load<false>(rq, WS_GOF + (lane0 & 7) * HBS, (u32)(smb + L_CTL + 2560));
      }
      asm volatile("s_waitcnt vmcnt(0)" ::: "memory");
      const u32x4 lv = afrag(smem, L_HB + lane0 * 16);
      const u32 xb = *reinterpret_cast<const u32*>(smem + L_HB + 1024 + lane0 * 4);
      float v[10];
#pragma unroll
      for (int k = 0; k < 4; ++k) {
        v[2 * k] = bflo(lv[k]);
        v[2 * k + 1] = bfhi(lv[k]);
      }
      v[8] = bflo(xb);
      v[9] = bfhi(xb);
      float sm = 0.f;
#pragma unroll
      for (int k = 0; k < 10; ++k) sm += v[k];
      const float mean = wave_sum(sm) * (1.0f / D);
      float q = 0.f;
#pragma unroll
      for (int k = 0; k < 10; ++k) q = fmaf(v[k] - mean, v[k] - mean, q);
      const float var = wave_sum(q) * (1.0f / D);
      const float mb = bf16r(mean);
      const float ri = __builtin_amdgcn_rcpf(bf16r(__builtin_amdgcn_sqrtf(bf16r(bf16r(var) + 1e-5f))));
      u32 hp[5];
#pragma unroll
      for (int k = 0; k < 5; ++k) {
        const u32 cc = cvt2(v[2 * k] - mb, v[2 * k + 1] - mb);
        hp[k] = cvt2(bflo(cc) * ri, bfhi(cc) * ri);
      }
      *reinterpret_cast<u32x4*>(smem + L_LN + lane0 * 16) = u32x4{hp[0], hp[1], hp[2], hp[3]};
      *reinterpret_cast<u32*>(smem + L_LN + 1024 + lane0 * 4) = hp[4];
      asm volatile("s_waitcnt lgkmcnt(0)" ::: "memory");
      *(volatile LAS u32*)(smem + L_VOTE + 4) = 1u;
    } else {
      ctl_load<false>(rq, hso, (u32)(smb + L_CTL + 2304));
      hb = rtt > PTHR ? 1u << ((g >> 2) ^ (j & 1)) : 0u;
    }
    if (wave == 1) *(volatile LAS u32x2*)(smem + L_CLK) = u32x2{now(), rtnow()};
    *reinterpret_cast<u32x4*>(smem + L_P + wave * P_W + lane0 * 16) = u32x4{0u, 0u, 0u, 0u};
    *reinterpret_cast<u32x4*>(smem + L_P + wave * P_W + 256 + lane0 * 16) = u32x4{0u, 0u, 0u, 0u};
    const u16* kb = P.kc + rowbase * DH;
    const u32 vo = (lane0 >> 3) * 384 + 16 * (lane0 & 7);
    const u32 kw = (u32)(smb + L_W2 + wave * 1024 + lane0 * 16), kr = (u32)(smb + L_W2 + wave * 1024 + (lane0 & 7) * 128 + (lane0 & 8) * 8 + (lane0 >> 4) * 16);
#define KPERM(i)                                                                          \
  asm volatile("ds_write_b128 %[w], v[%[r0]:%[r1]]\n\tds_read_b128 v[%[r0]:%[r1]], %[r]" \
               :                                                                          \
               : [w] "v"(kw), [r] "v"(kr), [r0] "n"(kreg(i)), [r1] "n"(kreg(i) + 3)       \
               : "memory")
#define WNEXT                                                                                  \
  dma_piece(ws0, isO ? D : F, smem, L_WB, wave * (isO ? 6 : 5) + ((word >> (4 * wn)) & 7), fresh6(lane0)); \
  wn = wn == (isO ? 5 : 4) ? 0 : wn + 1;
    const u32 qo = WS_GQ1 + (u32)(h * 8 + (lane0 >> 3)) * Q1S + wave * 128 + (lane0 & 7) * 16;
    auto step = [&](auto uc) __attribute__((always_inline)) {
      constexpr int u = decltype(uc)::v;
      if (u == UAD + 1) tstep += __builtin_amdgcn_readfirstlane(cnt[2]);
      BURST(u, vo, (u < 8 ? kb : vb) + ((u + rot) & 7) * GST * DH);
      if (wcnt(u + 1) != wcnt(u) || u + 1 == USW) {
        tnext += hsub * (PQ(u + 1) - PQ(u));
        tstep += 4 * (PQ(u + 1) - PQ(u));
      }
      if (u >= 2 && u < 10) {
        KPERM((3 * u + 18) % 24);
        KPERM((3 * u + 19) % 24);
        KPERM((3 * u + 20) % 24);
      }
      if (u == UAD - 3 && wave == 7) {
        ctl_load<false>(rq, rp_line(0, g, lane0 & 7), (u32)(smb + L_CTL + 2816));
        ++exa;
      }
      if (u == 0) {
        att_table<0>(smem, fresh6(lane0), wave, gop);
        att_table<1>(smem, fresh6(lane0), wave, gop);
      }
      if (u == UQ) {
        if (wave < 6) {
          const int lane = fresh6(lane0), m = lane & 15, G = lane >> 4;
          const int q_l0 = L_WQ + wave * 5120 + wlane(lane, 0), q_l1 = L_WQ + wave * 5120 + wlane(lane, 1);
          const int q_bk = 4096 - (G >> 1) * 1024;
          const int st = m == 8 ? 64 : 0;
          const int sa = m == 8 ? L_LN + 160 * c + 16 * G : (m == 15 ? L_ONE : L_ZERO);
          const u32 q_dst = WS_GQ1 + (u32)(h * 8 + c) * Q1S + wave * 128 + (lane >> 5) * 64 + m * 4;
          asm volatile("" ::: "memory");
          lds_wait((volatile LAS u32*)(smem + L_VOTE + 4), 1u);
          const u32x4 f0 = afrag(smem, sa), f1 = afrag(smem, sa + st), f2 = afrag(smem, G < 2 ? sa + 2 * st : L_TAG);
          f32x4 q_d0 = {0.f, 0.f, 0.f, 0.f}, q_d1 = q_d0;
          q_d0 = mfma32(f0, wfrag(smem, q_l0, 0), q_d0);
          q_d1 = mfma32(f0, wfrag(smem, q_l1, 0), q_d1);
          q_d0 = mfma32(f1, wfrag(smem, q_l0, 2048), q_d0);
          q_d1 = mfma32(f1, wfrag(smem, q_l1, 2048), q_d1);
          q_d0 = mfma32(f2, wfrag(smem, q_l0 + q_bk, 0), q_d0);
          q_d1 = mfma32(f2, wfrag(smem, q_l1 + q_bk, 0), q_d1);
          auto sw = __builtin_amdgcn_permlane32_swap(__float_as_uint(sum4(q_d0)), __float_as_uint(sum4(q_d1)), false, false);
          auto sx = __builtin_amdgcn_permlane16_swap(sw[1], sw[1], false, false);
          const f32x4 qd = quad4(__uint_as_float(cvt2(__uint_as_float(sx[0]), __uint_as_float(sx[1]))));
          if ((lane0 & 19) == 0) store16<AUXQ>(rs, q_dst, __builtin_bit_cast(u32x4, qd));
          ++exa;
        }
      }
      if (u == UAD - 1 && wave == 7) {
        asm volatile("" ::: "memory");
        const int d = (int)(CTL[704 + lane0] - tstart);
        const float sd = sum8((u32)(d + DWIN) < 2u * DWIN ? (float)d : 0.f);
        cnt[2] = (u32)(int)__builtin_rintf(sd * (DEQ / (15 - NUN - UAD)));
      }
      if ((u == UB && wave < 6) || (u == UB - 1 && wave == 7)) {
        asm volatile("" ::: "memory");
        if (u == UB - 1) {
          hb += (__builtin_amdgcn_readfirstlane(CTL[576 + c]) & ~3u) + 4u;
          *(volatile LAS u32*)(smem + L_VOTE + 8) = hb;
          hb += 4u;
        }
        u32 t0, t1, last;
        asm volatile("s_mov_b64 exec, 1\n\tv_mov_b32 %0, 1\n\tv_mov_b32 %1, %[sm]\n\tds_add_rtn_u32 %0, %1, %0 offset:%[c]\n\t"
                     "ds_read_b32 %1, %1 offset:%[v]\n\ts_waitcnt lgkmcnt(0)\n\tv_readfirstlane_b32 %2, %0\n\t"
                     "s_cmp_eq_u32 %2, 6\n\ts_cbranch_scc0 1f\n\tbuffer_store_dword %1, off, %[rq], %[o] sc1\n1:\n\t"
                     "s_cselect_b32 %2, 1, 0\n\ts_mov_b64 exec, -1"
                     : "=&v"(t0), "=&v"(t1), "=&s"(last)
                     : [sm] "s"(smb), [c] "n"(L_CNT + 4), [v] "n"(L_VOTE + 8), [rq] "s"(rq), [o] "s"(WS_HB + h * HBS + c * 4)
                     : "memory", "scc");
        exa += last;
        exl += last;
      }
      if (u == UL && wave == 7) {
        ctl_load<false>(rq, hso, (u32)(smb + L_CTL + 2048));
        ++exa;
        if (UL + 2 < UK) ++exl;
      }
      if (u == UG && leader) {
        ctl_load<true>(rq, lho, (u32)(smb + L_CTL + 1024));
        ++exa;
      }
      if (u == UK && wave == 7) {
        HS_WAIT((u32)ch == 0xffu)
        if (lane0 == 0) cnt[1] = 15u;
      }
      if (u == UC && wave < 6) {
        lds_wait(cnt + 1, 15u);
        ctl_load<true, true>(rq, qo, (u32)(smb + L_LAND + wave * 2048));
        ++exa;
      }
      if (u == UW && leader) {
        const u32x4 xa = afrag(smem, L_CTL + lane0 * 16);
        u32x4 lv;
#pragma unroll 1
        for (;;) {
          asm volatile("" ::: "memory");
          lv = afrag(smem, L_CTL + 1024 + lane0 * 16);
          const bool same = lv[0] == xa[0] || lv[1] == xa[1] || lv[2] == xa[2] || lv[3] == xa[3];
          if (__builtin_amdgcn_ballot_w64(same) == 0ull || tmo == 0u) break;
          --tmo;
          __builtin_amdgcn_s_sleep(4);
          ctl_load<true>(rq, lho, (u32)(smb + L_CTL + 1024));
          asm volatile("s_waitcnt vmcnt(0)" ::: "memory");
        }
        u32 nv = 256u;
#pragma unroll
        for (int e = 0; e < 4; ++e)
          nv += (u32)__builtin_popcountll(__builtin_amdgcn_ballot_w64((lv[e] & 2u) != 0u)) -
                (u32)__builtin_popcountll(__builtin_amdgcn_ballot_w64((lv[e] & 1u) != 0u));
        if (lane0 < 8) __builtin_amdgcn_raw_buffer_store_b32(CTL[640 + lane0] + (nv > 256u ? 2u : 1u), rs, WS_GOF + lane0 * HBS, 0, 16);
        ++exa;
        ++exl;
      }
      if (u == UX && wave == 7) {
        ctl_load<false>(rq, hso, (u32)(smb + L_CTL + 2048));
        ++exa;
      }
      if (u == UX && wave < 6) {
        const int lane = fresh6(lane0);
        const int ca = L_CS + wave * 128 + (((lane >> 1) ^ (lane >> 5)) & 1) * 64 + (lane & 1) * 32;
        const f32x4 c0 = *reinterpret_cast<const f32x4*>(smem + ca), c1 = *reinterpret_cast<const f32x4*>(smem + ca + 16);
#pragma unroll 1
        for (int p = 0; p < 5; ++p) {
          u32x4* const pw = reinterpret_cast<u32x4*>(smem + L_WQ + wave * 5120 + p * 1024 + lane * 16);
          const u32x4 w = *pw;
#define CW(k, c, e) cvt2(fmaf(bflo(w[k]), SCL, c[e]), fmaf(bfhi(w[k]), SCL, c[e + 1]))
          *pw = u32x4{CW(0, c0, 0), CW(1, c0, 2), CW(2, c1, 0), CW(3, c1, 2)};
        }
      }
      if (u == UF) {
        if (wave < 6) {
          q1_finish(smem, fresh6(lane0), wave);
          asm volatile("s_waitcnt lgkmcnt(0)" ::: "memory");
          if (lane0 == 0) lds_add(smem, L_CNT, 1u);
        }
      }
      if (wcnt(u) != 0 && (u - WU0) / WSP < nin && (wave & (WSP - 1)) == (u - WU0) % WSP) {
        u32 tw = tnext - WBK - hsub * PQ(u + 1);
        pace(tw, 0u);
        WNEXT
        ++exa;
        ++exl;
      }
    };
    static_assert(UQ >= 1 && UQ + 2 <= UB && UB < UL && UL + 2 <= UK && UK <= UC && UG + 2 <= UW && UC + 2 <= UF && UF < UX && UX >= 9 && UX + 2 < 16 - NUN && UAD >= 3 && WU0 >= UB, "order");
    slots<0, UX + 1>(step);
  }

#pragma unroll 1
  for (int half = 0;; ++half) {
    {
      u32x4 O0, O1, O2, S0, S1;
      float sk;
      Att at;
      const int lane = fresh6(lane0);
      const u32 gso = (u32)half * GO_SZ;
      {
        const u32 off = fresh(ar + A_GQ + hsub * 16384 + wave * 128 + (wave < 6 ? (lane >> 3) * 896 + (lane & 7) * 16 : (lane & 7) * 896));
        if (half == 1) {
          if (wave == 7) wait_tt(A_Q);
        } else {
          BURST(UX + 1, (u32)((lane >> 3) * 384 + 16 * (lane & 7)), vb + ((UX + 1 + rot) & 7) * GST * DH);
          lds_wait(cnt, 6u);
        }
        asm volatile("s_cmp_eq_u32 %[hf], 0\n\ts_cbranch_scc1 1f\n\ts_cmp_lt_u32 %[wv], 7\n\ts_cselect_b64 exec, -1, 0\n\ts_barrier\n\t"
                     "buffer_load_dwordx4 v[44:47], %[of], %[rq], 0 offen sc1\n\ts_mov_b64 exec, -1\n\ts_setprio 2\n1:"
                     : "={v[44:47]}"(O1)
                     : [hf] "s"(gso), [wv] "s"(wave), [of] "v"(off), [rq] "s"(rq)
                     : "scc", "memory");
        att_setup(at, smem, lane, wave);
        to2 = T_O2;
#define QK(W, a, k0, k1) \
  "s_waitcnt lgkmcnt(" #W ")\n\t" SM "v[32:35], v[" #a ":" #a "+3], v[" #k0 ":" #k0 "+7], %[ix]\n\t" SM "v[36:39], v[" #a ":" #a "+3], v[" #k1 ":" #k1 "+7], %[ix]\n\t"
#define QLD "buffer_load_dwordx4 v[44:47], %[of], %[rq], 0 offen sc1\n"
#define QWT "s_sub_u32 s88, s88, 1\n\ts_waitcnt vmcnt(0)\n\t"
        u32 t, z;
        asm volatile("v_mov_b64 v[32:33], 0\n\tv_mov_b64 v[34:35], 0\n\tv_mov_b64 v[36:37], 0\n\tv_mov_b64 v[38:39], 0\n\t"
                     "v_mov_b32 %[z], 0\n\ts_cmp_eq_u32 %[hf], 0\n\ts_cbranch_scc1 5f\n\t"
                     "s_mov_b32 s88, 0x10000\n\ts_cmp_lt_u32 %[wv], 6\n\ts_cbranch_scc1 2f\n\t"
                     "s_bitcmp0_b32 %[wv], 0\n\ts_cselect_b64 exec, -1, 0\n\ts_branch 3f\n"
                     "6:\n\t" QLD "3:\n\t" QWT
                     "v_add_f32 v48, v44, v45\n\tv_mul_f32 v49, 0x3acccccd, v47\n\tv_add_f32 v48, v48, v46\n\t" AND3("v44", "v45", "v46")
                     "v_add_f32_dpp v49, v49, v49" Q1 "v_add_f32_dpp v48, v48, v48" Q1 AND3("%[t]", "1", "v47")
                     "v_add_f32_dpp v49, v49, v49" Q2 "v_add_f32_dpp v48, v48, v48" Q2 "v_cmp_eq_u32 vcc, 0, %[t]\n\t"
                     "v_add_f32_dpp v49, v49, v49" HM "v_add_f32_dpp v48, v48, v48" HM
                     "v_mul_f32 v49, -v49, v49\n\tv_fmac_f32 v49, 0x3acccccd, v48\n\t" CV "v48, 0, |v49|\n\tv_add_f32 v48, 0x3727c5ac, v48\n\t"
                     CV "v48, 0, v48\n\tv_sqrt_f32 v48, v48\n\ts_cselect_b64 vcc, 0, vcc\n\t" CV "v48, 0, v48\n\tv_rcp_f32 v48, v48\n\t"
                     "s_cbranch_vccnz 6b\n\tds_write_b32 %[qw], v48\n\ts_cbranch_execnz 4f\n\ts_branch 2f\n"
                     "1:\n\t" QLD "2:\n\t" QWT
                     "v_permlane32_swap_b32 v44, v45\n\tv_permlane32_swap_b32 v46, v47\n\t"
                     "v_add_f32 v48, v44, v45\n\tv_add_f32 v49, v46, v47\n\t" AND3("v44", "v45", "v46") AND3("%[t]", "1", "v47")
                     "v_permlane16_swap_b32 v48, v49\n\tv_add_f32 v48, v48, v49\n\t"
                     "v_cmp_eq_u32 vcc, 0, %[t]\n\ts_nop 0\n\tv_add_f32_dpp v48, v48, v48" R8
                     CV "v49, v48, v48\n\tds_write_b16 %[qw], v49\n\ts_cselect_b64 vcc, 0, vcc\n\t"
                     "s_cbranch_vccnz 1b\n\ts_mov_b64 exec, -1\n4:\n\ts_waitcnt lgkmcnt(0)\n\ts_barrier\n"
                     "5:\n\tds_read_b128 v[40:43], %[q0]\n\tds_read_b128 v[44:47], %[q1]\n\t"
                     "ds_read_b128 v[48:51], %[q2]\n\tds_read_b128 v[52:55], %[q3]\n\t"
                     "ds_read_b128 v[56:59], %[q4]\n\tds_read_b128 v[60:63], %[q5]\n\tds_read_b32 %[sk], %[z] offset:%[su]\n\t"
                     QK(6, 40, 64, 96) QK(5, 44, 72, 104) "ds_read_b128 v[40:43], %[z]\n\t"
                     QK(5, 48, 80, 112) "ds_read_b128 v[44:47], %[z]\n\t" QK(5, 52, 88, 120) "ds_read_b128 v[48:51], %[z]\n\t"
                     QK(5, 56, 128, 144) QK(3, 60, 136, 152)
                     : "=&{v[40:43]}"(O0), "+{v[44:47]}"(O1), "=&{v[48:51]}"(O2), "=&{v[32:35]}"(S0), "=&{v[36:39]}"(S1),
                       [t] "=&v"(t), [z] "=&v"(z), [sk] "=&{v31}"(sk)
                     : [hf] "s"(gso), [wv] "s"(wave), [of] "v"(off), [rq] "s"(rq), [qw] "v"(at.qw), [q0] "v"(at.qad[0]),
                       [q1] "v"(at.qad[1]), [q2] "v"(at.qad[2]), [q3] "v"(at.qad[3]), [q4] "v"(at.qad[4]), [q5] "v"(at.qad[5]),
                       [ix] "v"(at.idxq), [su] "n"(L_SUM), "{v[64:95]}"(kv0), "{v[96:127]}"(kv1), "{v[128:159]}"(kv2)
                     : V52, "s88", "vcc", "scc", "memory");
      }
      u32 hq = gso;
      asm volatile("" : "+s"(hq));
      if (__builtin_expect(hq == 0u, 0)) {
        const u32 tn_ = tnext, ts_ = tstep, ea_ = exa;
        u32 tnext = tn_, tstep = ts_, exa = ea_, exl = 0u;
        int wn = nin;
        const int lv_ = fresh6(lane0);
        const u32 vo = (lv_ >> 3) * 384 + 16 * (lv_ & 7);
#pragma unroll
        for (int u = UX + 2; u < 16 - NUN; ++u) {
          BURST(u, vo, vb + ((u + rot) & 7) * GST * DH);
          if (u == UX + 2 && wave == 7) {
            constexpr int L_CTL = L_LAND + 6 * 2048;
            volatile LAS u32* const CTL = (volatile LAS u32*)(smem + L_CTL);
            const u32 hso = lv_ < 8 ? WS_HB + (u32)h * HBS + lv_ * 4 : WS_GOF + g * HBS;
            u32 gd, tmo = SPIN_CAP;
            HS_WAIT(false)
            if (lane0 == 8) *(volatile LAS u32*)(smem + L_AR) = (u32)(g & 3) * AREA + (((u32)(g >> 2) ^ (gd >> 1)) & 1u) * SIDE;
          }
        }
        tnext += wave * (TSE / 8 - WOFS) - hsub * PQ(15);
        tstep = TSE;
#pragma unroll
        for (int t = 48 - 3 * NUN; t < 48; ++t) {
          LOADW(t, vo, vb + ((t / 3 + rot) & 7) * GST * DH);
        }
        if (wave == 7) {
          const u32 a7 = *(volatile LAS u32*)(smem + L_AR) + (u32)(j >> 3) * 16384u + (u32)(j & 7) * 1024u;
          const u32x4 zz = __builtin_bit_cast(u32x4, zero4());
          store16(rs, lv_ * 16 + a7 + 2 * 16384, zz);
          store16(rs, lv_ * 16 + a7 + (j < 16 ? 6 : 8) * 16384, zz);
          store16(rs, lv_ * 16 + a7 + 12 * 16384, zz);
        }
#pragma unroll 1
        for (int p = 0; p < (isO ? NPRE : NPF); ++p) {
          pace(tnext, isO ? TPV : TPF);
          WNEXT
        }
      }
#define PW(V, L) "s_waitcnt vmcnt(" #V "+%[no]) lgkmcnt(" #L ")\n\t"
#define PX(o, a, k) SM "v[40+4*" #o ":43+4*" #o "], v[" #a ":" #a "+3], v[160+24*" #k "+8*" #o ":167+24*" #k "+8*" #o "], %[ix]\n\t"
#define ID " quad_perm:[0,1,2,3] row_mask:0xf bank_mask:"
#define OW(o) CV "v[40+2*" #o "], v[40+4*" #o "], v[41+4*" #o "]\n\t" CV "v[41+2*" #o "], v[42+4*" #o "], v[43+4*" #o "]\n\t" \
  "ds_write_b64 %[oa], v[40+2*" #o ":41+2*" #o "] offset:128*" #o "\n\t"
#define TR "ds_read_b64_tr_b16 "
#define AREAD "ds_read_b64 v[46:47], v50 offset:%[lar]\n\t"
#define ACHK "s_waitcnt lgkmcnt(0)\n\tv_cmp_ne_u32 vcc, 0, v47\n\t"
      asm volatile("s_mov_b32 s80, 0xff00ff\n\ts_mov_b32 s81, 0xff00ff00\n\ts_mov_b32 s82, " SAFEB "\n\ts_mov_b32 s83, 1\n\t" QNOP
                   "v_add_f32 v32, v32, v34\n\tv_add_f32 v34, v33, v35\n\t"
                   "v_add_f32_dpp v34, v36, v38" ID "0x3\n\tv_add_f32_dpp v32, v37, v39" ID "0xc\n\t"
                   "s_nop 0\n\tv_add_f32_dpp v32, v34, v32" R8 "v_mul_f32 %[sk], v32, %[sk]\n"
                   "1:\n\tv_exp_f32 v33, %[sk]\n\ts_cmp_lt_u32 %[wv], 4\n\t" CV "v33, v33, 0\n\t"
                   "ds_write2_b32 %[wa], v33, v33 offset1:%[o1]\n\t"
                   "ds_read_b128 v[52:55], %[ra]\n\tds_read_b128 v[56:59], %[ra] offset:16\n\t"
                   "ds_read_b128 v[60:63], %[ra] offset:32\n\tds_read_b128 v[36:39], %[ra] offset:48\n\ts_setprio 0\n\t"
                   "v_cmp_nge_f32 vcc, s82, |%[sk]|\n\tds_write_b16 %[la], v33\n\t"
                   PW(18, 4) PX(0, 52, 0) PX(1, 52, 0) PX(2, 52, 0) PW(12, 1) PX(0, 56, 1) PX(1, 56, 1) PX(2, 56, 1)
                   PW(6, 1) PX(0, 60, 2) PW(2, 1) PX(0, 36, 3) PX(1, 60, 2) PW(1, 1) PX(1, 36, 3) PX(2, 60, 2) PW(0, 1) PX(2, 36, 3)
                   "s_cbranch_vccnz 7f\n2:\n\ts_mov_b64 exec, s[80:81]\n\t" OW(0) OW(1) OW(2)
                   "s_mov_b64 exec, -1\n\tv_mov_b32 v50, 0\n\ts_cbranch_scc0 3f\n\t"
                   "v_mov_b32_dpp v51, %[ma] row_shr:2 row_mask:0xf bank_mask:0xf bound_ctrl:0\n\t"
                   "ds_read_b128 v[32:35], %[ma]\n\tds_read_b128 v[36:39], v51\n\t"
                   "s_cmp_lt_u32 %[wv], 3\n\ts_waitcnt lgkmcnt(0)\n\ts_barrier\n\t"
                   TR "v[52:53], %[sa]\n\t" TR "v[54:55], %[sa] offset:%[os]\n\ts_cbranch_scc0 4f\n\t"
                   TR "v[56:57], %[sa] offset:64\n\t" TR "v[58:59], %[sa] offset:%[os]+64\n\t" AREAD
                   "s_waitcnt lgkmcnt(3)\n\t" MF F2 ", " F0 ", v[32:35], 0\n\ts_waitcnt lgkmcnt(1)\n\t" MF F2 ", " F1 ", v[36:39], " F2 "\n\t"
                   ACHK "v_add_u32 v56, %[ds], v46\n\ts_nop 3\n\ts_cbranch_vccnz 8f\n\t"
                   CV "v52, v60, v61\n\t" CV "v53, v62, v63\n\tv_or_b32 v52, 1, v52\n\tv_or_b32 v53, 1, v53\n\t"
                   "buffer_store_dwordx2 v[52:53], v56, %[rq], %[so] offen sc1\n\ts_branch 9f\n"
                   "7:\n\tv_mov_b32 v52, 0\n\tv_mov_b32 v53, 0xff800000\n\tv_cmp_ne_u32 vcc, s82, v52\n\ts_and_b64 exec, vcc, 1\n\t"
                   "ds_write2_b32 v52, v53, v53 offset0:%[lar]/4+1 offset1:%[lar]/4+2\n\t"
                   "s_cmp_lt_u32 %[wv], 4\n\ts_branch 2b\n"
                   "8:\n\tds_max_f32 v50, %[sk] offset:%[lar]+8\n\ts_mov_b32 s82, 0\n\ts_waitcnt lgkmcnt(0)\n\ts_barrier\n\t"
                   "ds_read_b32 v52, v50 offset:%[lar]+8\n\tds_write_b32 v50, v50 offset:%[lar]+4\n\t"
                   "ds_read_b128 v[40:43], v50\n\tds_read_b128 v[44:47], v50\n\tds_read_b128 v[48:51], v50\n\ts_waitcnt lgkmcnt(0)\n\t"
                   "v_readfirstlane_b32 s83, v52\n\tv_sub_f32 %[sk], %[sk], v52\n\ts_bitset1_b32 s83, 0\n\ts_branch 1b\n"
                   "4:\n\t" AREAD "s_waitcnt lgkmcnt(1)\n\t" MF F2 ", " F0 ", v[32:35], 0\n\t"
                   ACHK "v_add_u32 v56, %[ds], v46\n\tv_mov_b32 v58, s83\n\ts_nop 3\n\t"
                   "v_add_f32 v60, v60, v61\n\tv_add_f32 v62, v62, v63\n\tv_add_f32 v59, v60, v62\n\ts_cbranch_vccnz 8b\n\ts_nop 0\n\t"
                   "v_add_f32_dpp v59, v59, v59 row_bcast:15 row_mask:0xa bank_mask:0xf\n\tv_or_b32 v59, 1, v59\n\t"
                   "buffer_store_dwordx2 v[58:59], v56, %[rq], %[so] offen sc1\n\ts_branch 9f\n"
                   "3:\n\ts_waitcnt lgkmcnt(0)\n\ts_barrier\n\t" AREAD "v_bfrev_b32 v56, 1\n\t" ACHK "s_cbranch_vccnz 8b\n\t"
                   "buffer_store_dword v56, v56, %[rq], 0 offen\n9:"
                   : "+{v[40:43]}"(O0), "+{v[44:47]}"(O1), "+{v[48:51]}"(O2), "+{v[32:35]}"(S0), "+{v[36:39]}"(S1), [sk] "+{v31}"(sk)
                   : [ix] "v"(at.idx), [no] "n"(NPRE), [la] "v"(at.la), [oa] "v"(at.oa), [sa] "v"(at.sa), [ma] "v"(at.ma),
                     [wa] "v"(at.wa), [ra] "v"(at.ra), [o1] "n"(P_U / 4 + 1), [ds] "v"(at.dst), [wv] "s"(wave), [rq] "s"(rq),
                     [so] "s"(gso), [lar] "n"(L_AR), [os] "n"(OM_S * 8), "{v[160:191]}"(kv3), "{v[192:223]}"(kv4), "{v[224:255]}"(kv5)
                   : V52, "s80", "s81", "s82", "s83", "vcc", "scc", "memory");
      tref = now();
      ar = __builtin_amdgcn_readfirstlane(O1[2]);
    }
    const u32 go = ar + A_GO + (u32)half * GO_SZ;
    const u32 gyw = A_GY + (u32)half * GY_PAIR;
    const u32 gy = gyw + ar;
    const u32 gh = go + A_GH;
    const int land = L_LAND + wave * 2048;
    if (isO) {
      {
        const int lane = fresh6(lane0);
        const u32 voff = fresh(go + go_slot(wave) + ((lane * 16) ^ ((lane >> 4) * 64)));
        asm volatile("s_waitcnt vmcnt(1)" ::: "memory");
        if (wave == 7) wait_until(tref + (half ? to2 : 24 * T_O));
        asm volatile("s_mov_b32 m0, %2\n\ts_bitcmp1_b32 " STR(OEARLY) ", %3\n\ts_cbranch_scc0 1f\n\ts_barrier\n\t"
                     "buffer_load_dwordx4 %0, %1, 0 offen sc1 lds\n\tbuffer_load_dwordx4 %0, %1, 0 offen offset:1024 sc1 lds\n1:"
                     :
                     : "v"(voff), "s"(rq), "s"(land), "s"(wave)
                     : "memory", "scc");
        const int l0 = fresh(L_WB + wave * 6144 + wlane(lane, 0)), l1 = l0 ^ 32;
        u32x4 wf[6];
#pragma unroll
        for (int k = 0; k < 3; ++k) {
          wf[2 * k] = wfrag(smem, l0, 2048 * k);
          wf[2 * k + 1] = wfrag(smem, l1, 2048 * k);
        }
        asm volatile("" ::: "memory");
        const int n = lane & 15, G = lane >> 4;
        const int r0_ = land + (n & 7) * 256 + (n & 3) * 64;
        const int ra = fresh(n < 8 ? r0_ + G * 16 : r0_ ^ 192), rb = fresh((r0_ + G * 16) ^ 64), rc = fresh((r0_ + G * 16) ^ 128);
        const int rw = fresh(L_RED + (lane >> 1) * 80 + wave * 8 + (lane & 1) * 4);
        const int rr = fresh(L_RED + n * 80);
        const u32 dst = fresh(gyw + gy_rep(G) + gy_chunk(4 * j + (n >> 2)) + (n & 3) * 4 + g * 256);
        u32 t;
#define RS(r, e) "v_mul_f32_dpp v" #r ", v41, v" #r " row_newbcast:" #e ALL
        asm volatile("s_mov_b64 s[82:83], -1\n\ts_movk_i32 s89, %[sd]\n\t" POLL_HEAD
                     "s_bitcmp1_b32 " STR(OEARLY) ", %[wv]\n\ts_cbranch_scc0 1f\n\ts_branch 5f\n"
                     "7:\n\tv_max_f32_dpp v41, v52, v52" Q1 "s_nop 1\n\tv_max_f32_dpp v41, v41, v41" Q2
                     "s_nop 1\n\tv_max_f32_dpp v41, v41, v41" HM "v_sub_f32 v41, v52, v41\n\tv_exp_f32 v41, v41\n\ts_nop 0\n\t"
                     "v_mul_f32 v42, v41, v43\n\ts_nop 1\n\tv_add_f32_dpp v42, v42, v42" Q1 "s_nop 1\n\tv_add_f32_dpp v42, v42, v42" Q2
                     "s_nop 1\n\tv_add_f32_dpp v42, v42, v42" HM "v_mov_b32_dpp v41, v41 row_shl:4 row_mask:0x2 bank_mask:0xf\n\t"
                     "v_rcp_f32 v42, v42\n\ts_nop 0\n\t"
                     RS(44, 8) RS(45, 9) RS(46, 10) RS(47, 11) RS(48, 8) RS(49, 9) RS(50, 10) RS(51, 11) "s_branch 3f\n"
                     "2:\n\tv_cmp_eq_u32 vcc, 0, %[t]\n\ts_cbranch_vccz 7b\n\t" POLL_AGAIN
                     "\n5:\n\tv_mov_b32 v41, 1\n\t" POLL_READ
                     "s_waitcnt lgkmcnt(2)\n\t"
                     MF "v[44:47], " F0 ", %[w0], 0\n\tv_add_f32 v43, v53, v55\n\t"
                     "v_or_b32_dpp v41, v52, v41 quad_perm:[0,1,2,3] row_mask:0xf bank_mask:0xc\n\t"
                     MF "v[48:51], " F0 ", %[w1], 0\n\tv_add_f32_dpp v42, v43, v43" Q1 AND3("v52", "v53", "v54")
                     "s_waitcnt lgkmcnt(1)\n\t"
                     MF "v[44:47], " F1 ", %[w2], v[44:47]\n\tv_add_f32_dpp v42, v42, v42" Q2 AND3("%[t]", "v55", "v56")
                     MF "v[48:51], " F1 ", %[w3], v[48:51]\n\tv_add_f32_dpp v42, v42, v42" HM AND3("%[t]", "v57", "v58")
                     "s_waitcnt lgkmcnt(0)\n\t"
                     MF "v[44:47], " F2 ", %[w4], v[44:47]\n\t" MF "v[48:51], " F2 ", %[w5], v[48:51]\n\tv_rcp_f32 v42, v42\n\t"
                     AND3("%[t]", "v59", "v60") AND3("%[t]", "v61", "v62") AND3("%[t]", "1", "v63")
                     "v_cmp_gt_u32 vcc, v41, %[t]\n\ts_cbranch_vccnz 2b\n"
                     "3:\n\ts_mov_b32 exec_hi, 0\n\ts_cmp_eq_u32 %[wv], 0\n\t" PK(44, 46) PK(48, 50)
                     "v_add_f32 v44, v44, v45\n\tv_add_f32 v48, v48, v49\n\ts_nop 1\n\t"
                     "v_permlane16_swap_b32 v44, v48\n\tv_add_f32 v44, v44, v48\n\tv_mul_f32_dpp v44, v42, v44 row_newbcast:8" ALL
                     "ds_write_b32 %[rw], v44\n\ts_cselect_b64 exec, -1, 0\n\ts_waitcnt lgkmcnt(0)\n\ts_barrier\n\t"
                     "ds_read_b128 " F0 ", %[rr]\n\tds_read_b128 " F1 ", %[rr] offset:16\n\tds_read_b128 " F2 ", %[rr] offset:32\n\t"
                     "ds_read_b128 v[44:47], %[rr] offset:48\n\ts_cbranch_execz 9f\n\t"
                     "s_waitcnt lgkmcnt(2)\n\t" PK(52, 54) PK(56, 58) PK(52, 56) "s_waitcnt lgkmcnt(1)\n\t" PK(52, 60) PK(52, 62)
                     "s_waitcnt lgkmcnt(0)\n\t" PK(52, 44) PK(52, 46) CV "v52, v52, v53\n\tv_or_b32 v52, 1, v52\n\t"
                     "buffer_store_dword v52, %[ds], %[rq], 0 offen sc1\n\tbuffer_store_dword v52, %[ds], %[rq], s89 offen sc1\n"
                     "9:\n\ts_mov_b64 exec, -1"
                     : [t] "=&v"(t)
                     : [ra] "v"(ra), [rb] "v"(rb), [rc] "v"(rc), WIN6(wf), [vo] "v"(voff), [rq] "s"(rq), [land] "s"(land), [rw] "v"(rw),
                       [rr] "v"(rr), [ds] "v"(dst), [wv] "s"(wave), [sd] "n"(SIDE)
                     : V40, V52, S80);
      }
      tref = now();
      if (half == 0) {
        if (wave >= 1 && wave < 7) {
          const u16* s0 = karg<56>() + (size_t)(192 * g) * D + 32 * j;
#pragma unroll
          for (int p = 0; p < 2; ++p) dma_piece(s0, D, smem, L_W2, (wave - 1) * 2 + p, fresh6(lane0));
          asm volatile("s_waitcnt vmcnt(0)" ::: "memory");
        }
        bar();
        if (wave == 5) {
          wait_until(tref + 24 * T_LAZY);
          x_request(rq, fresh6(lane0), gy + gy_row(c), land);
          x_update<true>(smem, rq, fresh6(lane0), gy + gy_row(c), land, c);
        }
      } else if (g == 0 && wave == 2) {
        const int lane = fresh6(lane0);
        const int col = j * 32 + (lane & 7) * 4 + ((lane >> 4) & 1) * 2 + (lane >> 5);
        u32 x0v, dummy;
        u16* const po = karg<64>() + col;
        asm volatile("global_load_ushort %0, %2, off\n\tglobal_load_ushort %1, %3, off\n\ts_waitcnt vmcnt(0)"
                     : "=&v"(x0v), "=&v"(dummy)
                     : "v"(karg<0>() + col), "v"(po)
                     : "memory");
        wait_until(tref + 24 * T_LAZY);
        const u32 o = gy + gy_chunk(4 * j + ((lane >> 1) & 3)) + (lane >> 3) * 256 + (lane & 1) * 8;
        const u32 off[3] = {o - GY_PAIR, o - GY_PAIR + GY_INST, o};
        float y[3];
        col_poll<3>(rq, off, y);
        float x3 = bf16r(bf16r(bf16r(bflo(x0v) + y[0]) + y[1]) + y[2]);
        const u32 off2 = fresh(o + GY_INST);
        asm volatile("" : "+v"(x3));
        wait_tt(A_F);
        u32 t;
        asm volatile("s_mov_b32 s88, 0x10000\n"
                     "1:\n\tbuffer_load_dwordx2 v[40:41], %[of], %[rq], 0 offen sc1\n\ts_sub_u32 s88, s88, 1\n\ts_waitcnt vmcnt(0)\n\t"
                     "v_lshlrev_b32 v42, 16, v40\n\tv_and_b32 v43, 0xffff0000, v40\n\t"
                     "v_lshlrev_b32 v44, 16, v41\n\tv_and_b32 v45, 0xffff0000, v41\n\t"
                     "v_permlane32_swap_b32 v42, v43\n\t" AND3("v40", "1", "v41") "v_permlane32_swap_b32 v44, v45\n\t"
                     "v_add_f32 v42, v42, v43\n\tv_add_f32 v44, v44, v45\n\tv_cmp_eq_u32 vcc, 0, %[t]\n\ts_cselect_b64 vcc, 0, vcc\n\t"
                     "v_permlane16_swap_b32 v42, v44\n\tv_add_f32 v42, v42, v44\n\ts_cbranch_vccnz 1b\n\ts_nop 0\n\t"
                     "v_add_f32_dpp v42, v42, v42" R8 CV "v42, 0, v42\n\tv_add_f32 v42, %[x], v42\n\t" CV "v42, v42, v42\n\t"
                     "global_store_short %[po], v42, off"
                     : [t] "=&v"(t)
                     : [of] "v"(off2), [rq] "s"(rq), [x] "v"(x3), [po] "v"(po)
                     : V40, "s88", "vcc", "scc", "memory");
      }
      if (wave >= 6) {
        const int lane = fresh6(lane0), wv = wave - 6;
        const int n = lane & 15, G = lane >> 4;
        const int l0 = fresh(L_W2 + wlane(lane, wv));
        u32x4 wf[6];
#pragma unroll
        for (int k = 0; k < 6; ++k) wf[k] = wfrag(smem, l0, 2048 * k);
        const int l48 = lane < 48 ? lane : 0;
        const u32 off = fresh(gh + (u32)l48 * 16);
        const int aw = fresh(L_ACT + wv * 384 + l48 * 8);
        const int ab = fresh(L_ACT + wv * 384 + 16 * G);
        const u32 dst = fresh((lane & (half ? 49 : 1)) ? OOB : gyw + GY_INST + gy_rep(G) + gy_chunk(4 * j + wv * 2 + (n >> 3)) + (n & 7) * 2 + g * 256);
        if (half)
          wait_tt(A_H);
        else
          wait_until(tref + 24 * T_H);
        u32 t;
#define HK(W, a, w, c) "s_waitcnt lgkmcnt(" #W ")\n\t" MF "v[36:39], v[" #a ":" #a "+3], %[" #w "], " c "\n\t"
        asm volatile("s_mov_b32 s88, 0x10000\n\ts_movk_i32 s89, %[sd]\n\ts_movk_i32 s90, 0x4000\n"
                     "1:\n\ts_waitcnt lgkmcnt(0)\n\tbuffer_load_dwordx4 v[40:43], %[of], %[rq], 0 offen sc1\n\t"
                     "buffer_load_dwordx4 v[44:47], %[of], %[rq], s90 offen sc1\n\ts_sub_u32 s88, s88, 1\n\ts_waitcnt vmcnt(0)\n\t"
                     "v_pk_mul_f32 v[48:49], v[40:41], v[44:45]\n\tv_pk_mul_f32 v[50:51], v[42:43], v[46:47]\n\t"
                     CV "v48, v48, v49\n\t" CV "v49, v50, v51\n\tds_write_b64 %[aw], v[48:49]\n\t"
                     "ds_read_b128 v[48:51], %[ab]\n\tds_read_b128 v[52:55], %[ab] offset:64\n\t"
                     "ds_read_b128 v[56:59], %[ab] offset:128\n\tds_read_b128 v[60:63], %[ab] offset:192\n\t"
                     AND3("v40", "v41", "v42") AND3("%[t]", "v43", "v44") AND3("%[t]", "v45", "v46") AND3("%[t]", "1", "v47")
                     "ds_read_b128 v[40:43], %[ab] offset:256\n\tds_read_b128 v[44:47], %[ab] offset:320\n\t"
                     "v_cmp_eq_u32 vcc, 0, %[t]\n\ts_cselect_b64 vcc, 0, vcc\n\ts_cbranch_vccnz 1b\n\t"
                     HK(5, 48, w0, "0") HK(4, 52, w1, "v[36:39]") HK(3, 56, w2, "v[36:39]") HK(2, 60, w3, "v[36:39]")
                     HK(1, 40, w4, "v[36:39]") HK(0, 44, w5, "v[36:39]")
                     "s_nop 7\n\tv_mov_b32_dpp v37, v36" Q1 CV "v36, v36, v37\n\tv_or_b32 v36, 1, v36\n\t"
                     "buffer_store_dword v36, %[ds], %[rq], 0 offen sc1\n\tbuffer_store_dword v36, %[ds], %[rq], s89 offen sc1"
                     : [t] "=&v"(t)
                     : WIN6(wf), [of] "v"(off), [rq] "s"(rq), [aw] "v"(aw), [ab] "v"(ab), [ds] "v"(dst), [sd] "n"(SIDE)
                     : V36, V40, V52, "s88", "s89", "s90", "vcc", "scc", "memory");
      }
    } else {
      const int lane = fresh6(lane0);
      const int n = lane & 15, G = lane >> 4;
      if (half == 1) {
        x_update<true>(smem, rq, lane, gy - GY_PAIR + GY_INST + gy_row(wave), land + L_LAND2, wave);
      } else {
        if (wave == 1) {
          const u32x2 c0 = *(volatile LAS u32x2*)(smem + L_CLK);
          const float r = (float)(rtnow() - c0[1]) * (24.2f * 512.f) * __builtin_amdgcn_rcpf((float)(now() - c0[0])) - 511.5f;
          *(volatile LAS u32*)(smem + L_CLK) = ((u32)__builtin_fminf(__builtin_fmaxf(r, 0.f), 127.f) << 25) | 1u;
        }
        int wn = nin + NPF;
#pragma unroll 1
        for (u32 tw = tref + 24 * T_FW; wn != 0; tw += 24 * T_FD) {
          wait_until(tw);
          WNEXT
        }
      }
      asm volatile("s_waitcnt vmcnt(0) lgkmcnt(0)" ::: "memory");
      G80 a;
      g80_load(a, smem, lane, L_WB + wave * 5120, L_HB + 160 * wave, land, L_ONE);
      const u32 voff = fresh(gy + gy_row(wave) + ((lane * 16) ^ ((lane >> 4) * 64)));
      const int rw = fresh(L_RED + n * 144 + wave * 16 + G * 4);
      const int sw_ = fresh(n == 8 ? L_ST + wave * 16 + G * 4 : L_ACT + lane * 4);
      const int rr = fresh(L_RED + (lane & 31) * 144 + (lane >> 5) * 64), rs_ = fresh(L_ST + 16 * (lane & 7));
      const u32 dst = fresh(lane < 32 ? gh + fslot + lane * 4 : OOB);
      if (wave == 7) {
        if (half)
          wait_tt(A_Y);
        else
          wait_until(tref + 24 * T_YF);
      }
      u32 t;
#define SH4 " row_shr:4" ALL
      asm volatile(USED10 "s_mov_b32 s89, 0x810000\n\t" POLL_HEAD "s_branch 1f\n2:\n\t" POLL_AGAIN POLL_READ
                   "s_waitcnt lgkmcnt(2)\n\t"
                   MF "v[40:43], " F0 ", " F0 ", 0\n\t" AND3("v52", "v53", "v54")
                   MF "v[44:47], " F0 ", %[w0], 0\n\t" MF "v[48:51], " F0 ", %[w1], 0\n\t"
                   "s_waitcnt lgkmcnt(1)\n\t"
                   MF "v[40:43], " F1 ", " F1 ", v[40:43]\n\t" AND3("%[t]", "v55", "v56") AND3("%[t]", "v57", "v58")
                   "s_waitcnt lgkmcnt(0)\n\t"
                   MF "v[40:43], " F2 ", " F2 ", v[40:43]\n\t" AND3("%[t]", "v59", "v60") AND3("%[t]", "v61", "v62")
                   MF "v[44:47], " F1 ", %[w2], v[44:47]\n\t" AND3("%[t]", "1", "v63") "v_cmp_eq_u32 vcc, 0, %[t]\n\t"
                   MF "v[48:51], " F1 ", %[w3], v[48:51]\n\ts_and_b64 vcc, vcc, s[80:81]\n\ts_cbranch_scc1 2b\n"
                   "3:\n\t" MF "v[44:47], " F2 ", %[w4], v[44:47]\n\t" PK(40, 42) "v_add_f32 v40, v40, v41\n\t"
                   MF "v[48:51], " F2 ", %[w5], v[48:51]\n\ts_cmp_eq_u32 %[wv], 0\n\t"
                   "v_add_f32_dpp v40, v40, v40" Q1 "s_nop 1\n\tv_add_f32_dpp v40, v40, v40" Q2 PK(44, 46)
                   "v_add_f32 v44, v44, v45\n\tv_add_f32_dpp v41, v40, v40" SH4 PK(48, 50) "v_add_f32 v48, v48, v49\n\t"
                   "v_add_f32_dpp v40, v41, v40" SH4
                   "ds_write2st64_b32 %[rw], v44, v48 offset1:9\n\tds_write_b32 %[sw], v40\n\t"
                   "s_cselect_b64 exec, -1, 0\n\ts_bitcmp1_b32 %[j], 0\n\ts_waitcnt lgkmcnt(0)\n\ts_barrier\n\t"
                   "ds_read_b128 v[40:43], %[rs]\n\tds_read_b128 " F0 ", %[rr]\n\tds_read_b128 " F1 ", %[rr] offset:16\n\t"
                   "ds_read_b128 " F2 ", %[rr] offset:32\n\tds_read_b128 v[44:47], %[rr] offset:48\n\ts_cbranch_execz 9f\n\t"
                   "s_waitcnt lgkmcnt(4)\n\tv_add_f32 v40, v40, v41\n\tv_add_f32 v42, v40, v42\n\tv_add_f32_dpp v43, v43, v43" Q1
                   "s_waitcnt lgkmcnt(2)\n\tv_add_f32_dpp v42, v42, v42" Q1 "v_add_f32_dpp v43, v43, v43" Q2 PK(52, 56)
                   "v_add_f32_dpp v42, v42, v42" Q2 "v_add_f32_dpp v43, v43, v43" HM PK(54, 58) "v_add_f32_dpp v42, v42, v42" HM
                   "v_mul_f32 v43, 0x3acccccd, v43\n\tv_mul_f32 v42, 0x3acccccd, v42\n\tv_fma_f32 v42, -v43, v43, v42\n\t"
                   "s_waitcnt lgkmcnt(0)\n\t" PK(52, 60) PK(54, 62) PK(52, 44) PK(54, 46)
                   CV "v43, 0, v43\n\tv_add_f32 v52, v52, v53\n\tv_fma_f32 v54, -v43, v55, v54\n\t"
                   "v_add_f32 v53, v52, v54\n\tv_add_f32 v52, v52, v54\n\t"
                   CV "v42, 0, |v42|\n\tv_add_f32 v42, 0x3727c5ac, v42\n\t" CV "v42, 0, v42\n\tv_sqrt_f32 v42, v42\n\t"
                   "v_permlane32_swap_b32 v52, v53\n\t" CV "v42, 0, v42\n\tv_rcp_f32 v42, v42\n\tv_add_f32 v52, v52, v53\n\t"
                   "v_mul_f32 v52, v52, v42\n\t" CV "v53, 0, v52\n\tv_mul_f32 v54, 0xbfb8aa3b, v53\n\tv_exp_f32 v54, v54\n\t"
                   "s_cbranch_scc1 8f\n\tv_add_f32 v54, 1.0, v54\n\tv_rcp_f32 v54, v54\n\ts_nop 0\n\tv_mul_f32 v52, v53, v54\n"
                   "8:\n\t" CV "v52, s89, v52\n\tbuffer_store_dword v52, %[ds], %[rq], 0 offen sc1\n"
                   "9:\n\ts_mov_b64 exec, -1"
                   : [t] "=&v"(t)
                   : [ra] "v"(a.ra), [rb] "v"(a.rb), [rc] "v"(a.rc), WIN6(a.w), [vo] "v"(voff), [rq] "s"(rq), [land] "s"(land),
                     [rw] "v"(rw), [sw] "v"(sw_), [rr] "v"(rr), [rs] "v"(rs_), [ds] "v"(dst), [wv] "s"(wave), [j] "s"(j)
                   : V40, V52, S80);
      tref = now();
      if (half == 0 && wave == 1 && j == 20 && lane0 < 8)
        __builtin_amdgcn_raw_buffer_store_b32(((rtnow() & 0x1fffffu) << 4) | *(volatile LAS u32*)(smem + L_CLK), rs, gy_rep(fresh6(lane0)) + st_line(g), 0, 16);
      if (half == 0) {
        x_update<false>(smem, rq, fresh6(lane0), 0u, land, wave);
      }
    }
    if (half == 1) break;
    {
      if (wave < 7) {
        const int lane = fresh6(lane0);
        const int n = lane & 15;
        G80 a;
        g80_load(a, smem, lane, L_WQ + wave * 5120, L_HB + 160 * c, land, wave == 6 ? L_ONE : L_ZERO);
        const u32 voff = fresh(gy + GY_INST + gy_row(c) + ((lane * 16) ^ ((lane >> 4) * 64)));
        const u32 gq = ar + A_GQ + hsub * 16384 + c * 896 + wave * 128;
        const u32 dst = fresh(wave < 6 ? (lane < 32 ? gq + lane * 4 : OOB) : (n == 8 ? gq + (lane >> 4) * 4 : OOB));
        if (wave == 4) {
          wait_until(tref + 24 * (isO ? T_AO : T_AN));
          stamp_poll(smem, rq, ar + st_line(lane & 7));
          wait_tt(A_E);
        }
        u32 t;
        asm volatile(USED10 POLL_HEAD "s_branch 1f\n"
                     "6:\n\ts_waitcnt lgkmcnt(2)\n\t" MF "v[44:47], " F0 ", " F0 ", 0\n\t" AND3("v52", "v53", "v54")
                     "s_waitcnt lgkmcnt(1)\n\t" MF "v[44:47], " F1 ", " F1 ", v[44:47]\n\t" AND3("%[t]", "v55", "v56") AND3("%[t]", "v57", "v58")
                     "s_waitcnt lgkmcnt(0)\n\t" MF "v[44:47], " F2 ", " F2 ", v[44:47]\n\t" AND3("%[t]", "v59", "v60")
                     AND3("%[t]", "v61", "v62") AND3("%[t]", "1", "v63")
                     "v_cmp_eq_u32 vcc, 0, %[t]\n\ts_and_b64 vcc, vcc, s[80:81]\n\ts_cbranch_scc1 2f\n\ts_nop 1\n\t"
                     PK(44, 46) "v_add_f32 v44, v44, v45\n\ts_nop 1\n\tv_add_f32_dpp v44, v44, v44" Q1
                     "s_nop 1\n\tv_add_f32_dpp v44, v44, v44" Q2 "s_nop 1\n\tv_add_f32_dpp v45, v44, v44" SH4
                     "s_nop 1\n\tv_add_f32_dpp v44, v45, v44" SH4 "v_or_b32 v44, 1, v44\n\t"
                     "buffer_store_dword v44, %[ds], %[rq], 0 offen sc1\n\ts_branch 9f\n"
                     "2:\n\t" POLL_AGAIN "s_cmp_eq_u32 %[wv], 6\n\t" POLL_READ "s_cbranch_scc1 6b\n\t"
                     "s_waitcnt lgkmcnt(2)\n\t"
                     MF "v[44:47], " F0 ", %[w0], 0\n\t" AND3("v52", "v53", "v54") MF "v[48:51], " F0 ", %[w1], 0\n\t"
                     "s_waitcnt lgkmcnt(1)\n\t"
                     MF "v[44:47], " F1 ", %[w2], v[44:47]\n\t" AND3("%[t]", "v55", "v56") AND3("%[t]", "v57", "v58")
                     MF "v[48:51], " F1 ", %[w3], v[48:51]\n\t"
                     "s_waitcnt lgkmcnt(0)\n\t"
                     MF "v[44:47], " F2 ", %[w4], v[44:47]\n\t" MF "v[48:51], " F2 ", %[w5], v[48:51]\n\t"
                     AND3("%[t]", "v59", "v60") AND3("%[t]", "v61", "v62") AND3("%[t]", "1", "v63")
                     "v_cmp_eq_u32 vcc, 0, %[t]\n\ts_and_b64 vcc, vcc, s[80:81]\n\ts_cbranch_scc1 2b\n"
                     "3:\n\ts_nop 0\n\t" PK(44, 46) PK(48, 50) "v_add_f32 v44, v44, v45\n\tv_add_f32 v48, v48, v49\n\ts_nop 1\n\t"
                     "v_permlane16_swap_b32 v44, v48\n\tv_add_f32 v45, v44, v48\n\tv_add_f32 v44, v44, v48\n\ts_nop 1\n\t"
                     "v_permlane32_swap_b32 v44, v45\n\tv_add_f32 v44, v44, v45\n\tv_or_b32 v44, 1, v44\n\t"
                     "buffer_store_dword v44, %[ds], %[rq], 0 offen sc1\n9:"
                     : [t] "=&v"(t)
                     : [ra] "v"(a.ra), [rb] "v"(a.rb), [rc] "v"(a.rc), WIN6(a.w), [vo] "v"(voff), [rq] "s"(rq), [land] "s"(land),
                       [ds] "v"(dst), [wv] "s"(wave)
                     : V40, V52, S80);
      } else {
        bar();
      }
      if (!isO) x_request(rq, fresh6(lane0), gy + GY_INST + gy_row(wave), land + L_LAND2);
    }
  }
#undef RS
#define RS(n) "s_memrealtime s[" #n ":" #n "+1]\n\ts_nop 1\n\t"
#define RD(n) "s_sub_i32 s80, s80, s" #n "\n\t"
  asm volatile("s_branch 9f\n.Lmk_wait:\n\t"
               "v_mov_b32 v62, 0\n\tds_read_b64 v[62:63], v62 offset:%[an]\n\ts_waitcnt lgkmcnt(0)\n\t"
               "v_readfirstlane_b32 s81, v63\n\tv_readfirstlane_b32 s82, v62\n\ts_mul_i32 s81, s81, 3\n\t"
               "s_add_i32 s82, s82, s80\n\ts_mul_i32 s80, s80, s81\n\ts_lshr_b32 s80, s80, 11\n\ts_add_i32 s80, s80, s82\n\t"
               "s_sub_i32 s81, 1549, s81\n"
               "1:\n\ts_memrealtime s[98:99]\n\ts_waitcnt lgkmcnt(0)\n\ts_lshl_b32 s98, s98, 3\n\ts_sub_i32 s98, s80, s98\n\t"
               "s_cmpk_gt_i32 s98, 8*20\n\ts_cbranch_scc1 1b\n\ts_cmpk_lt_i32 s98, 8*9\n\ts_cbranch_scc1 3f\n\t"
               RS(84) RS(86) RS(88) RS(90) RS(92) RS(94) RS(96) "s_memrealtime s[98:99]\n\t"
               "s_getpc_b64 s[82:83]\n2:\n\ts_add_u32 s82, s82, 3f-2b\n\ts_addc_u32 s83, s83, 0\n\ts_waitcnt lgkmcnt(0)\n\t"
               RD(84) RD(86) RD(88) RD(90) RD(92) RD(94) RD(96) RD(98)
               "s_mul_i32 s80, s80, s81\n\ts_ashr_i32 s80, s80, 10\n\ts_sub_i32 s80, s80, 100\n\t"
               "s_max_i32 s80, s80, 0\n\ts_min_i32 s80, s80, 4*26\n\ts_andn2_b32 s80, s80, 3\n\t"
               "s_sub_u32 s82, s82, s80\n\ts_subb_u32 s83, s83, 0\n\ts_setpc_b64 s[82:83]\n\t"
               ".rept 26\n\ts_nop 1\n\t.endr\n3:\n\ts_setpc_b64 s[78:79]\n9:"
               :
               : [an] "n"(L_ANC));
  asm volatile(".Lmk_code_end:");
}

}

extern "C" void kernel_launch(void* const* d_in, const int* in_sizes, int n_in, void* d_out, int out_size, void* d_ws,
                              size_t ws_size, hipStream_t stream) {
  Params P;
  P.x0 = (const u16*)d_in[0];
  P.kc = (const u16*)d_in[1];
  P.vc = (const u16*)d_in[2];
  P.wq = (const u16*)d_in[3];
  P.wo = (const u16*)d_in[4];
  P.w1 = (const u16*)d_in[6];
  P.w3 = (const u16*)d_in[7];
  P.w2 = (const u16*)d_in[8];
  P.out = (u16*)d_out;
  P.ws = (u32*)d_ws;
  mega_kernel<<<NWG, NT, 0, stream>>>(P);
}
```

```cpp
#include <hip/hip_runtime.h>
#include <hip/hip_bf16.h>
#include <stdint.h>

namespace {

typedef unsigned int u32;
typedef unsigned short u16;
typedef unsigned long long u64;
using u32x2 = __attribute__((ext_vector_type(2))) unsigned;
using u32x4 = __attribute__((ext_vector_type(4))) unsigned;
using f32x2 = __attribute__((ext_vector_type(2))) float;
using f32x4 = __attribute__((ext_vector_type(4))) float;
using s16x4 = __attribute__((ext_vector_type(4))) short;
using bf16x2 = __attribute__((ext_vector_type(2))) __bf16;
using bf16x8 = __attribute__((ext_vector_type(8))) __bf16;
#define LAS __attribute__((address_space(3)))

constexpr int D = 640, H = 32, DH = 192, WIN = 4096, SK = 32768, F = 1536;
constexpr int NWG = 256, NT = 512;
constexpr u32 SPIN_CAP = 1u << 16;
constexpr float SCL = 0.07216878364870322f * 1.4426950408889634f;

#define T_AN 6
#define T_AO 70
#define T_LAZ0 40
#define B_O 975
#define B_Y 1625
#define B_H 2265
#define CLKO 510.5f
#define T_W2 0
#define W2R 1
#define T_OL 20
#define T_YL 100
#define T_HL 90
#define B_HC 30
#define B_A -160
#define B_E 2775
#define B_T 20
#define PTHR 60
#define T_LAZY 60

#define D_E 790
#define D_Q 445
#define T_O2 100
#define D_Y 1482
#define D_H 575
#define D_F 366
#define A_Q D_Q
#define A_Y (A_Q + D_Y)
#define A_H (A_Y + D_H)
#define A_F (A_H + D_F)

constexpr u32 SIDE = 8192, AREA = 16 * 16384;
constexpr u32 A_GO = 0;
constexpr u32 A_GQ = 4 * 16384;
constexpr u32 A_GH = A_GQ + 7168;
constexpr u32 A_GY = 8 * 16384;
constexpr u32 GO_SZ = 32768, GY_PAIR = 65536, GY_INST = 32768;
constexpr u32 WS_CLR = 4 * AREA;
constexpr u32 WS_PB = WS_CLR;
#define HBS 4352
constexpr u32 WS_GOF = WS_PB + (128 << 10), WS_HB = WS_GOF + 8 * HBS;
__device__ __forceinline__ u32 rp_line(int e, int r, int w) { return WS_PB + (u32)((e * 8 + r) * 8 + w) * 384u; }
#define Q1S 2048
constexpr u32 WS_GQ1 = WS_GOF + (192 << 10);
constexpr u32 WS_END = WS_GQ1 + 256 * 2048;
static_assert(WS_END <= (2u << 20) && WS_CLR == 256 * 4096, "layout");
__device__ __forceinline__ u32 go_slot(int w) { return (u32)(w >> 2) * 16384u + (u32)(w & 3) * 2048u; }
__device__ __forceinline__ u32 gy_row(int i) { return go_slot(i); }
__device__ __forceinline__ u32 gy_chunk(int u) {
  const u32 i = __umul24(u, 205) >> 11;
  return (i >> 2) * 8192u + __umul24(i, 2048 - 160) + (u32)u * 16u;
}
__device__ __forceinline__ u32 st_line(int g) { return A_GH + 768u + (u32)(g >> 1) * 16384u + (u32)(g & 1) * 128u; }
__device__ __forceinline__ u32 gy_rep(int r) { return (u32)(r & 3) * AREA + (u32)(r >> 2) * SIDE; }

constexpr int L_ZERO = 0;
constexpr int L_CNT = 16;
constexpr int L_ONE = 32;
constexpr int L_TAG = 48;
constexpr int L_ST = 64;
constexpr int L_VOTE = L_ST;
constexpr int L_ANC = 192;
constexpr int L_CLK = 200;
constexpr int L_SUM = 208;
constexpr int L_AN0 = 216;
constexpr int L_AR = 224;
constexpr int L_Q = 256;
constexpr int L_CS = 640;
constexpr int L_HB = L_CS + 768;
constexpr int L_ACT = L_HB + D * 2;
constexpr int L_RED = L_ACT + 2 * 384;
constexpr int L_P = L_RED + 32 * 144;
constexpr int P_W = 1280, P_U = 592, P_PAR = 288;
constexpr int OM_S = 416;
constexpr int L_OM = L_P + 8 * P_W;
constexpr int L_LN = L_OM;
constexpr int L_LAND = (L_OM + 24 * OM_S + 512 + 255) / 256 * 256;
constexpr int L_WQ = L_LAND + 8 * 2048;
constexpr int L_WB = L_WQ + 6 * 5120;
constexpr int L_W2 = L_WB + 768 * 64;
constexpr int L_LAND2 = L_WB + 640 * 64 - L_LAND;
constexpr int L_PF = L_W2 + 192 * 64;
constexpr int L_TAB = L_PF + 256;
constexpr int L_TOTAL = L_TAB + 8 * 2048;
static_assert(L_LAND2 % 256 == 0 && L_LAND % 256 == 0 && L_WB % 64 == 0 && L_P % 16 == 0 && L_WQ % 16 == 0 && L_HB % 16 == 0 && L_RED % 16 == 0, "alignment");

struct Params {
  const u16* x0;
  const u16* kc;
  const u16* vc;
  const u16* wq;
  const u16* wo;
  const u16* w1;
  const u16* w3;
  const u16* w2;
  u16* out;
  u32* ws;
};

__device__ __forceinline__ u32 cvt2(float lo, float hi) {
  return __builtin_bit_cast(u32, __builtin_convertvector(f32x2{lo, hi}, bf16x2));
}
__device__ __forceinline__ float bflo(u32 w) { return __uint_as_float(w << 16); }
__device__ __forceinline__ float bfhi(u32 w) { return __uint_as_float(w & 0xffff0000u); }
__device__ __forceinline__ float bf16r(float x) { return bflo(cvt2(x, x)); }

template <int CTRL>
__device__ __forceinline__ float dpp(float x) {
  return __builtin_bit_cast(float, __builtin_amdgcn_mov_dpp(__builtin_bit_cast(int, x), CTRL, 0xf, 0xf, true));
}
constexpr int XOR1 = 0xB1, XOR2 = 0x4E, XOR7 = 0x141, ROR8 = 0x128;
__device__ __forceinline__ float fmx(float a, float b) {
  float r;
  asm("v_max_f32 %0, %1, %2" : "=v"(r) : "v"(a), "v"(b));
  return r;
}
__device__ __forceinline__ float xrow16_max(float x) {
  auto s = __builtin_amdgcn_permlane16_swap(__float_as_uint(x), __float_as_uint(x), false, false);
  x = fmx(__uint_as_float(s[0]), __uint_as_float(s[1]));
  auto t = __builtin_amdgcn_permlane32_swap(__float_as_uint(x), __float_as_uint(x), false, false);
  return fmx(__uint_as_float(t[0]), __uint_as_float(t[1]));
}
__device__ __forceinline__ float xrow16_sum(float x) {
  auto s = __builtin_amdgcn_permlane16_swap(__float_as_uint(x), __float_as_uint(x), false, false);
  x = __uint_as_float(s[0]) + __uint_as_float(s[1]);
  auto t = __builtin_amdgcn_permlane32_swap(__float_as_uint(x), __float_as_uint(x), false, false);
  return __uint_as_float(t[0]) + __uint_as_float(t[1]);
}
__device__ __forceinline__ float sum8(float v) {
  v += dpp<XOR1>(v);
  v += dpp<XOR2>(v);
  v += dpp<XOR7>(v);
  return v;
}
__device__ __forceinline__ float max8(float v) {
  asm("s_nop 1\n\tv_max_f32_dpp %0, %0, %0 quad_perm:[1,0,3,2] row_mask:0xf bank_mask:0xf\n\t"
      "s_nop 1\n\tv_max_f32_dpp %0, %0, %0 quad_perm:[2,3,0,1] row_mask:0xf bank_mask:0xf\n\t"
      "s_nop 1\n\tv_max_f32_dpp %0, %0, %0 row_half_mirror row_mask:0xf bank_mask:0xf"
      : "+v"(v));
  return v;
}
__device__ __forceinline__ float max_ror8(float v) {
  asm("s_nop 1\n\tv_max_f32_dpp %0, %0, %0 row_ror:8 row_mask:0xf bank_mask:0xf" : "+v"(v));
  return v;
}
__device__ __forceinline__ float wave_sum(float v) {
  v = sum8(v);
  v += dpp<ROR8>(v);
  return xrow16_sum(v);
}
__device__ __forceinline__ float tsum8(f32x4 a) {
  auto s0 = __builtin_amdgcn_permlane32_swap(__float_as_uint(a[0]), __float_as_uint(a[1]), false, false);
  auto s1 = __builtin_amdgcn_permlane32_swap(__float_as_uint(a[2]), __float_as_uint(a[3]), false, false);
  const float r0 = __uint_as_float(s0[0]) + __uint_as_float(s0[1]);
  const float r1 = __uint_as_float(s1[0]) + __uint_as_float(s1[1]);
  auto t = __builtin_amdgcn_permlane16_swap(__float_as_uint(r0), __float_as_uint(r1), false, false);
  float r = __uint_as_float(t[0]) + __uint_as_float(t[1]);
  r += dpp<ROR8>(r);
  return r;
}

__device__ __forceinline__ u32 now() { return (u32)__builtin_amdgcn_s_memtime(); }
__device__ __forceinline__ void wait_until(u32 t) {
#pragma unroll 1
  while ((int)(now() - t) < -160) __builtin_amdgcn_s_sleep(1);
#pragma unroll 1
  while ((int)(now() - t) < 0) {
  }
}
__device__ __forceinline__ u32 rtnow() { return (u32)__builtin_amdgcn_s_memrealtime(); }
template <int AN = L_ANC>
__device__ __forceinline__ void wait_tt(u32 a) {
  asm volatile("s_mov_b32 s80, %0\n\tv_mov_b32 v62, %1\n\ts_call_b64 s[78:79], .Lmk_wait"
               :
               : "is"(a), "n"(AN)
               : "s78", "s79", "s80", "s81", "s82", "s83", "s84", "s85", "s86", "s87", "s88", "s89", "s90", "s91", "s92", "s93", "s94",
                 "s95", "s96", "s97", "s98", "s99", "v62", "v63", "scc", "memory");
}
__device__ __forceinline__ void lds_wait(volatile LAS u32* p, u32 want) {
  u32 spins = 0;
#pragma unroll 1
  while (__builtin_amdgcn_readfirstlane(*p) != want) {
    if (++spins > (SPIN_CAP << 6)) break;
    __builtin_amdgcn_s_sleep(1);
  }
  asm volatile("" ::: "memory");
}
__device__ __forceinline__ void lds_add(unsigned char* smem, int off, u32 v) {
  __hip_atomic_fetch_add((LAS u32*)(smem + off), v, __ATOMIC_RELAXED, __HIP_MEMORY_SCOPE_WORKGROUP);
}

__device__ __forceinline__ int fresh(int t) {
  asm volatile("" : "+v"(t));
  return t;
}
__device__ __forceinline__ int fresh6(int t) {
  t = fresh(t);
  __builtin_assume((unsigned)t < 64u);
  return t;
}
__device__ __forceinline__ int lane_id() {
  int t;
  asm volatile("v_mbcnt_lo_u32_b32 %0, -1, 0\n\tv_mbcnt_hi_u32_b32 %0, -1, %0" : "=v"(t));
  __builtin_assume((unsigned)t < 64u);
  return t;
}
__device__ __forceinline__ f32x4 zero4() {
  f32x2 a, b;
  asm volatile("v_mov_b64 %0, 0\n\tv_mov_b64 %1, 0" : "=v"(a), "=v"(b));
  return f32x4{a[0], a[1], b[0], b[1]};
}
__device__ __forceinline__ void bar() { asm volatile("s_waitcnt lgkmcnt(0)\n\ts_barrier" ::: "memory"); }

__device__ __forceinline__ u32x2 trread(unsigned char* smem, int off) {
  return __builtin_bit_cast(u32x2, __builtin_amdgcn_ds_read_tr16_b64_v4i16((LAS s16x4*)(smem + off)));
}
__device__ __forceinline__ u32x4 wfrag(unsigned char* smem, int lb, int off) {
  const u32x2 a = trread(smem, lb + off), b = trread(smem, lb + off + 256);
  return u32x4{a[0], a[1], b[0], b[1]};
}
__device__ __forceinline__ int wlane(int lane, int cb) {
  const int i = lane & 15, G = lane >> 4;
  return (8 * G + (i >> 2)) * 64 + (i & 3) * 8 + ((cb ^ (G & 1)) * 32);
}
__device__ __forceinline__ u32x4 afrag(unsigned char* smem, int a) { return *reinterpret_cast<const u32x4*>(smem + a); }
__device__ __forceinline__ f32x4 mfma32(u32x4 w, u32x4 a, f32x4 c) {
  return __builtin_amdgcn_mfma_f32_16x16x32_bf16(__builtin_bit_cast(bf16x8, w), __builtin_bit_cast(bf16x8, a), c, 0, 0, 0);
}

__device__ __forceinline__ void dma_piece(const u16* src, int ld, unsigned char* smem, int ldsoff, int p, int lane) {
  const int row = 16 * p + (lane >> 2), part = lane & 3;
  const int cb = (part >> 1) ^ (lane >> 5);
  const u32 voff = (u32)(row * ld + cb * 16 + (part & 1) * 8) * 2u;
  const u32 dst = (u32)(uintptr_t)(LAS unsigned char*)smem + (u32)(ldsoff + p * 1024);
  u32 keep;
  asm volatile("s_mov_b32 %0, m0\n\ts_mov_b32 m0, %2\n\ts_nop 2\n\tglobal_load_lds_dwordx4 %1, %3 nt\n\ts_mov_b32 m0, %0"
               : "=&s"(keep)
               : "v"(voff), "s"(dst), "s"(src)
               : "memory");
}

__device__ __forceinline__ float sum4(f32x4 d) { return (d[0] + d[1]) + (d[2] + d[3]); }
template <int AUX = 16>
__device__ __forceinline__ void store16(__amdgpu_buffer_rsrc_t rs, u32 off, u32x4 v) {
  __builtin_amdgcn_raw_buffer_store_b128(v, rs, off, 0, AUX);
}

struct G80 {
  u32x4 w[6];
  int ra, rb, rc, l0, l2;
};
__device__ __forceinline__ void g80_load(G80& a, unsigned char* smem) {
  a.w[0] = wfrag(smem, a.l0, 0);
  a.w[1] = wfrag(smem, a.l0 ^ 32, 0);
  a.w[2] = wfrag(smem, a.l0, 2048);
  a.w[3] = wfrag(smem, a.l0 ^ 32, 2048);
  a.w[4] = wfrag(smem, a.l2, 0);
  a.w[5] = wfrag(smem, a.l2 ^ 32, 0);
}
__device__ __forceinline__ void g80_addr(G80& a, int lane, int wb, int xs, int land, int one) {
  const int m = lane & 15, G = lane >> 4;
  a.l0 = fresh(wb + wlane(lane, 0));
  a.l2 = fresh(a.l0 + 4096 - (G >> 1) * 1024);
  const bool vec = m <= 8;
  const int st = vec ? 64 : 0;
  const int sa = vec ? (m == 8 ? xs : land + 256 * m) + 16 * G : (m == 15 ? one : L_ZERO);
  const int sw = m < 8 ? (m & 3) * 64 : 0;
  a.ra = fresh(sa ^ sw);
  a.rb = fresh((sa + st) ^ sw);
  a.rc = fresh(G < 2 ? (sa + 2 * st) ^ sw : L_TAG);
}
template <bool X4, bool NT = false>
__device__ __forceinline__ void ctl_load(u32x4 rq, u32 voff, u32 lds) {
  u32 keep;
  if (NT)
    asm volatile("s_mov_b32 %0, m0\n\ts_mov_b32 m0, %3\n\ts_nop 2\n\tbuffer_load_dwordx4 %1, %2, 0 offen sc1 nt lds\n\ts_mov_b32 m0, %0"
                 : "=&s"(keep) : "v"(voff), "s"(rq), "s"(lds) : "memory");
  else if (X4)
    asm volatile("s_mov_b32 %0, m0\n\ts_mov_b32 m0, %3\n\ts_nop 2\n\tbuffer_load_dwordx4 %1, %2, 0 offen sc1 lds\n\ts_mov_b32 m0, %0"
                 : "=&s"(keep) : "v"(voff), "s"(rq), "s"(lds) : "memory");
  else
    asm volatile("s_mov_b32 %0, m0\n\ts_mov_b32 m0, %3\n\ts_nop 2\n\tbuffer_load_dword %1, %2, 0 offen sc1 lds\n\ts_mov_b32 m0, %0"
                 : "=&s"(keep) : "v"(voff), "s"(rq), "s"(lds) : "memory");
}
__device__ __forceinline__ u32 stamp_poll(unsigned char* smem, u32x4 rq, u32 so, int land, u32 te) {
  u32 sv, budget = SPIN_CAP;
#pragma unroll 1
  for (;;) {
    asm volatile("s_waitcnt vmcnt(0)" ::: "memory");
    sv = *(volatile LAS u32*)(smem + land + 4 * lane_id());
    if (__builtin_amdgcn_ballot_w64((sv & 1u) == 0u) == 0ull || budget == 0u) break;
    --budget;
    __builtin_amdgcn_s_sleep(1);
    ctl_load<false>(rq, so, land);
  }
  const u32 nw = te + 0x800000u;
  const float age = (float)((nw - (sv >> 1)) & 0xffffffu);
  const u32 an = nw - (u32)-max8(-age);
  *(volatile LAS u32x2*)(smem + L_ANC) = u32x2{an, (u32)max8((float)(sv >> 25))};
  return __builtin_amdgcn_readfirstlane(an);
}
#define MF "v_mfma_f32_16x16x32_bf16 "
#define F0 "v[52:55]"
#define F1 "v[56:59]"
#define F2 "v[60:63]"
#define V52 "v52", "v53", "v54", "v55", "v56", "v57", "v58", "v59", "v60", "v61", "v62", "v63"
#define V40 "v40", "v41", "v42", "v43", "v44", "v45", "v46", "v47", "v48", "v49", "v50", "v51"
#define V36 "v36", "v37", "v38", "v39"
#define V32 "v32", "v33", "v34", "v35"
#define S80 "s80", "s81", "s82", "s83", "s84", "s85", "s86", "s87", "s88", "s89", "s90", "s91", "vcc", "scc", "memory"
#define AND3(a, b, c) "v_bitop3_b32 %[t], " a ", " b ", " c " bitop3:0x80\n\t"
#define ALL " row_mask:0xf bank_mask:0xf\n\t"
#define Q1 " quad_perm:[1,0,3,2]" ALL
#define Q2 " quad_perm:[2,3,0,1]" ALL
#define HM " row_half_mirror" ALL
#define R8 " row_ror:8" ALL
#define PK(a, b) "v_pk_add_f32 v[" #a ":" #a "+1], v[" #a ":" #a "+1], v[" #b ":" #b "+1]\n\t"
#define CV "v_cvt_pk_bf16_f32 "
constexpr u32 OOB = 0x80000000u;
#define POLL_HEAD                                                                                                     \
  "s_mov_b32 m0, %[land]\n\ts_mov_b32 s88, 0x10000\n\ts_mov_b32 s80, 0xff00ff\n\ts_mov_b32 s81, s80\n\t"               \
  "s_mov_b64 s[84:85], s[82:83]\n\ts_mov_b64 s[86:87], s[82:83]\n\t"
#define POLL_MASK                                                                                                     \
  "s_sub_u32 s88, s88, 1\n\ts_cbranch_scc1 3f\n\ts_or_b32 s84, vcc_lo, vcc_hi\n\ts_lshr_b32 s85, s84, 16\n\t"          \
  "s_or_b32 s84, s84, s85\n\ts_lshr_b32 s85, s84, 8\n\ts_or_b32 s84, s84, s85\n\t"                                     \
  "s_bitreplicate_b64_b32 s[84:85], s84\n\ts_bitreplicate_b64_b32 s[84:85], s84\n\t"                                   \
  "s_bitreplicate_b64_b32 s[84:85], s84\n\ts_bitreplicate_b64_b32 s[86:87], s85\n\t"                                   \
  "s_bitreplicate_b64_b32 s[84:85], s84\n\ts_and_b64 s[84:85], s[84:85], s[82:83]\n\t"                                 \
  "s_and_b64 s[86:87], s[86:87], s[82:83]\n\t"
#define POLL_LOAD                                                                                                     \
  "s_mov_b64 exec, s[84:85]\n\tbuffer_load_dwordx4 %[vo], %[rq], 0 offen sc1 lds\n\t"                                  \
  "s_mov_b64 exec, s[86:87]\n\tbuffer_load_dwordx4 %[vo], %[rq], 0 offen offset:1024 sc1 lds\n\ts_mov_b64 exec, -1\n\t"
#define POLL_READ "s_waitcnt vmcnt(0)\n\t" POLL_RD
#define POLL_RD \
  "ds_read_b128 " F0 ", %[ra]\n\tds_read_b128 " F1 ", %[rb]\n\tds_read_b128 " F2 ", %[rc]\n\t"
#define USED10 "s_mov_b32 s82, 0x30ff03ff\n\ts_mov_b32 s83, 0xff30ff03\n\t"
#define POLL_FIRST                                                                                           \
  "s_mov_b32 m0, %[land]\n\ts_mov_b32 exec_lo, 0x30ff03ff\n\ts_mov_b32 exec_hi, 0xff30ff03\n\t"               \
  "s_waitcnt lgkmcnt(0)\n\ts_barrier\n\t"                                                                    \
  "buffer_load_dwordx4 %[vo], %[rq], 0 offen sc1 lds\n\tbuffer_load_dwordx4 %[vo], %[rq], 0 offen offset:1024 sc1 lds\n\t" \
  "s_mov_b64 exec, -1\n\t"
#define WIN6(a) [w0] "v"(a[0]), [w1] "v"(a[1]), [w2] "v"(a[2]), [w3] "v"(a[3]), [w4] "v"(a[4]), [w5] "v"(a[5])
__device__ __forceinline__ f32x4 quad4(float r) { return f32x4{r, dpp<0x55>(r), dpp<0xAA>(r), dpp<0xFF>(r)}; }

__device__ __forceinline__ int q_addr(int d) {
  const int w = d >> 1;
  return L_Q + ((w & 1) * 48 + (w >> 1)) * 4 + (d & 1) * 2;
}

__device__ __forceinline__ void q1_finish(unsigned char* smem, int lane, int wave) {
  const int e = wave * 32 + (lane & 7) * 4 + ((lane >> 4) & 1) * 2 + (lane >> 5);
  asm volatile("" ::: "memory");
  const u32x4 v = afrag(smem, L_LAND + wave * 2048 + lane * 16);
  const float t = tsum8(f32x4{bflo(v[0]), bflo(v[1]), bflo(v[2]), bflo(v[3])});
  *reinterpret_cast<u16*>(smem + q_addr(e)) = (u16)cvt2(t, t);
  *reinterpret_cast<float*>(smem + L_CS + e * 4) = tsum8(f32x4{bfhi(v[0]), bfhi(v[1]), bfhi(v[2]), bfhi(v[3])}) * (-SCL / D);
}
__device__ __forceinline__ void x_request(u32x4 rq, int lane, u32 src, int land) {
  asm volatile("s_mov_b32 m0, %2\n\ts_nop 0\n\tbuffer_load_dwordx4 %0, %1, 0 offen sc1 lds\n\t"
               "buffer_load_dwordx4 %0, %1, 0 offen offset:1024 sc1 lds"
               :
               : "v"(src + ((lane * 16) ^ ((lane >> 4) * 64))), "s"(rq), "s"(land)
               : "memory");
}
template <bool POLL>
__device__ __forceinline__ void x_update(unsigned char* smem, u32x4 rq, int lane, u32 src, int land, int i) {
  const int l = lane < 40 ? lane : 0;
  const u32 off = land + 4 * l;
  u32 v[8];
  u32 budget = SPIN_CAP;
#pragma unroll 1
  for (;;) {
    if (POLL) asm volatile("s_waitcnt vmcnt(0)" ::: "memory");
#pragma unroll
    for (int k = 0; k < 8; ++k) v[k] = *reinterpret_cast<const u32*>(smem + (off ^ ((k & 3) * 64)) + 256 * k);
    const u32 a = v[0] & v[1] & v[2] & v[3] & v[4] & v[5] & v[6] & v[7] & 1u;
    if (!POLL || __builtin_amdgcn_ballot_w64(a == 0u) == 0ull || budget == 0u) break;
    --budget;
    x_request(rq, lane, src, land);
  }
  float lo = 0.f, hi = 0.f;
#pragma unroll
  for (int k = 0; k < 8; ++k) {
    lo += bflo(v[k]);
    hi += bfhi(v[k]);
  }
  u32* px = reinterpret_cast<u32*>(smem + L_HB + 160 * i + 4 * l);
  const u32 xo = *px, y = cvt2(lo, hi);
  if (lane < 40) *px = cvt2(bflo(xo) + bflo(y), bfhi(xo) + bfhi(y));
}
template <int N>
__device__ __forceinline__ void col_poll(u32x4 rq, const u32 (&off)[N], float (&y)[N]) {
  u32x2 v[N];
  u32 budget = SPIN_CAP;
#pragma unroll 1
  for (;;) {
    u32 a = 1u;
#pragma unroll
    for (int k = 0; k < N; ++k)
      asm volatile("buffer_load_dwordx2 %0, %1, %2, 0 offen sc1" : "=v"(v[k]) : "v"(off[k]), "s"(rq) : "memory");
    asm volatile("s_waitcnt vmcnt(0)" ::: "memory");
#pragma unroll
    for (int k = 0; k < N; ++k) {
      asm volatile("" : "+v"(v[k]));
      a &= v[k][0] & v[k][1];
    }
    if (__builtin_amdgcn_ballot_w64(a == 0u) == 0ull || budget == 0u) break;
    --budget;
  }
#pragma unroll
  for (int k = 0; k < N; ++k) y[k] = bf16r(tsum8(f32x4{bflo(v[k][0]), bfhi(v[k][0]), bflo(v[k][1]), bfhi(v[k][1])}));
}

#define SJ 31
#define DWIN 250
#define DEQ 0.9f
#define PFB 16
#define AUXQ 18
#define UQ 1
#define UAD 4
#define UB 3
#define UL 5
#define UG 6
#define UK 7
#define UC 8
#define UW 8
#define UF 10
#define UX 11
#define TSTEP 236
#define TSV 232
#define USW 4
#define TSQ 192
#define WOFS 21
#define WBK 60
#define NINO 4
#define NINF 1
#define NINS 2
#define ORDO 0x543210u
#define ORDF 0x34210u
#define NPRE 2
#define NPF 2
#define TPF 220
#define T_FW 10
#define T_FD 12
#define WU0 3
#define WSP 2
#define WPC 100
#define NUN 1
static_assert(NINS + NPF < 5 && NINS <= NINO && NINO + NPRE == 6 && NINF + NPF < 5 && NPF == NPRE, "counts");
static_assert(WU0 + NINO * WSP <= UX + 1, "pieces before the scores");
#define TPV 220
#define TSE TSV
constexpr int wcnt(int u) { return u < WU0 || u >= WU0 + NINO * WSP ? 0 : (20 + ((u - WU0) / WSP < NINF ? 12 : ((u - WU0) / WSP < NINS ? 2 : 0))) / WSP; }
#define PQ(u) ((((u) < USW ? TSTEP : TSV) * (9600 + wcnt(u) * WPC)) / 12800)
#define ROW0 ((wave * 8 + c) * 8)
#define GST 512
#define ROT h
#define PACE_ASM                                                                                                     \
  "1:\n\ts_memrealtime s[98:99]\n\ts_waitcnt lgkmcnt(0)\n\ts_lshl_b32 s98, s98, 3\n\ts_sub_i32 s98, s98, %[tn]\n\t"  \
  "s_cmp_lt_i32 s98, 0\n\ts_cbranch_scc0 2f\n\ts_sleep 1\n\ts_branch 1b\n2:\n\ts_add_i32 %[tn], %[tn], %[ts]\n\t"
__device__ __forceinline__ void pace(u32& tnext, u32 tstep) {
  asm volatile(PACE_ASM : [tn] "+s"(tnext) : [ts] "s"(tstep) : "scc", "s98", "s99");
}
constexpr int kreg(int i) { return i % 3 < 2 ? 64 + 8 * (i / 3) + 4 * (i % 3) : 128 + 4 * (i / 3); }
constexpr int vreg(int i) { return 160 + 24 * (i / 6) + 8 * (i % 3) + 4 * ((i / 3) & 1); }
constexpr int treg(int t) { return t < 24 ? kreg(t) : vreg(t - 24); }
template <int V>
struct Cn {
  static constexpr int v = V;
};
template <int A, int B, class Fn>
__device__ __forceinline__ void slots(Fn& f) {
  if constexpr (A < B) {
    f(Cn<A>{});
    slots<A + 1, B>(f);
  }
}
#define PRT 200
#define STR2(x) #x
#define STR(x) STR2(x)
#define PRIO_ASM                                                                                                     \
  "s_setprio 0\n\ts_cmpk_lt_i32 s98, " STR(PRT) "\n\ts_cbranch_scc1 4f\n\ts_setprio 1\n\ts_cmpk_lt_i32 s98, 3*" STR(PRT) "\n\t"  \
  "s_cbranch_scc1 4f\n\ts_setprio 2\n\ts_cmpk_lt_i32 s98, 6*" STR(PRT) "\n\ts_cbranch_scc1 4f\n\ts_setprio 3\n4:\n\t"
#define LD1(r) "global_load_dwordx4 v[%[" #r "]:%[" #r "]+3], %[p], %[sb] offset:"
#define PACE_B PACE_ASM
#define BURST(u, voff, sbase)                                                                                       \
  asm volatile(PACE_B PRIO_ASM LD1(a) "0 nt\n\t" LD1(b) "128 nt\n\t" LD1(c) "256 nt\n\t"                                    \
               "s_waitcnt vmcnt(%[w]+2)\n\ts_cmp_gt_u32 %[ex], 1\n\ts_cbranch_scc1 3f\n\t"                          \
               "s_waitcnt vmcnt(%[w]+1)\n\ts_cmp_gt_u32 %[ex], 0\n\ts_cbranch_scc1 3f\n\ts_waitcnt vmcnt(%[w])\n3:"  \
               : [tn] "+s"(tnext)                                                                                   \
               : [a] "n"(treg(3 * (u))), [b] "n"(treg(3 * (u) + 1)), [c] "n"(treg(3 * (u) + 2)), [p] "v"(voff),     \
                 [sb] "s"(sbase), [ts] "s"(tstep), [ex] "s"(exa), [w] "n"(6)                                        \
               : "scc", "s96", "s97", "s98", "s99");                                                                              \
  exa = exl;                                                                                                        \
  exl = 0u
#define LOADW(t, voff, sbase)                                                                                       \
  asm volatile(PACE_ASM PRIO_ASM LD1(a) "%[im] nt\n\ts_waitcnt vmcnt(9)"                                                    \
               : [tn] "+s"(tnext)                                                                                   \
               : [a] "n"(treg(t)), [p] "v"(voff), [sb] "s"(sbase), [im] "n"(128 * ((t) % 3)), [ts] "s"(tstep)       \
               : "scc", "s98", "s99")

#define HS_WAIT(COND)                                                                                \
  _Pragma("unroll 1") for (;;) {                                                                     \
    gd = CTL[512 + lane0] - CTL[576 + lane0];                                                        \
    const u64 ch = __builtin_amdgcn_ballot_w64(gd != 0u) & 0x1ffull;                                 \
    if (ch > 0xffull || (COND) || tmo == 0u) break;                                                  \
    if ((tmo & 3u) == 0u && lane0 == 0 && cnt[1] >= 7u)                                              \
      __builtin_amdgcn_raw_buffer_store_b32(hb, rs, WS_HB + h * HBS + c * 4, 0, 16);                 \
    hb += 4u;                                                                                        \
    --tmo;                                                                                           \
    __builtin_amdgcn_s_sleep(3);                                                                     \
    ctl_load<false>(rq, hso, (u32)(smb + L_CTL + 2048));                                             \
    asm volatile("s_waitcnt vmcnt(0)" ::: "memory");                                                 \
  }

struct Att {
  int qad[6], idxq, wa, ra, idx, oa, sa, la, ma, qw;
  u32 dst;
};
template <int K>
__device__ __forceinline__ void att_table(unsigned char* smem, int lane, int wave, u32 go) {
  const int n = lane & 15, G = lane >> 4;
  const int seg = n & 1, pr = (n >> 1) & 1, tq = n >> 2, xy = G >> 1;
  const int qa = L_Q + (pr * 48 + seg * 8 + (G & 1) * 4) * 4;
  u32 t[16];
#pragma unroll
  for (int k = 0; k < 4; ++k) t[k] = tq == k ? qa + xy * 64 : L_ZERO;
#pragma unroll
  for (int k = 0; k < 2; ++k) t[4 + k] = tq == 2 * k + xy ? qa + 128 : L_ZERO;
  t[6] = 0x4444 + pr * 0xAAAA;
  t[7] = L_P + wave * P_W + (n & 1) * P_PAR + ((((G & 1) * 2 + ((n >> 2) & 1)) * 4 + (n >> 3) * 2 + (G >> 1)) * 16) + (n & 2) * 4;
  t[8] = L_P + wave * P_W + ((n >> 2) & 1) * P_U + (n >> 3) * P_PAR + G * 64;
  t[9] = (n & 3) * 0x5555;
  t[10] = L_OM + (wave * 2 + (n >> 3)) * OM_S + (n & 7) * 16 + (G & 1) * 8;
  t[11] = L_OM + (16 + (wave >> 2) * 8) * OM_S + (wave & 3) * 128 + lane * 2;
  t[12] = wave == 3 ? L_OM + 16 * OM_S + lane * 8 : L_OM + ((G & 1) * 4 + tq) * OM_S + wave * 128 + xy * 32 + (n & 3) * 8;
  t[13] = wave == 3 || n == xy ? L_ONE : L_ZERO;
  const int d0 = wave * 64 + n * 16 + G * 4;
  t[14] = (go + (wave < 3 ? (d0 >= 96 ? 2048 - 192 : 0) + d0 * 2 : (lane & 1) * 2048 + 192 + xy * 8)) & 0xffffu;
  t[15] = wave < 6 ? q_addr(wave * 32 + (lane & 7) * 4 + ((lane >> 4) & 1) * 2 + (lane >> 5)) : L_SUM;
  *reinterpret_cast<u32x4*>(smem + L_TAB + wave * 2048 + K * 1024 + lane * 16) =
      u32x4{t[8 * K] | (t[8 * K + 4] << 16), t[8 * K + 1] | (t[8 * K + 5] << 16), t[8 * K + 2] | (t[8 * K + 6] << 16),
            t[8 * K + 3] | (t[8 * K + 7] << 16)};
}
__device__ __forceinline__ void att_setup(Att& r, unsigned char* smem, int lane, int wave) {
  const u32x4 a = afrag(smem, L_TAB + wave * 2048 + lane * 16), b = afrag(smem, L_TAB + wave * 2048 + 1024 + lane * 16);
  r.qw = fresh(b[3] >> 16);
#pragma unroll
  for (int k = 0; k < 4; ++k) r.qad[k] = fresh(a[k] & 0xffffu);
  r.qad[4] = fresh(a[0] >> 16);
  r.qad[5] = fresh(a[1] >> 16);
  r.idxq = fresh(a[2] >> 16);
  r.wa = fresh(a[3] >> 16);
  r.ra = fresh(b[0] & 0xffffu);
  r.idx = fresh(b[1] & 0xffffu);
  r.oa = fresh(b[2] & 0xffffu);
  r.la = fresh(b[3] & 0xffffu);
  r.sa = fresh(b[0] >> 16);
  r.ma = fresh(b[1] >> 16);
  r.dst = fresh((wave < 3 ? lane & 12 : ~lane & 30) ? OOB : b[2] >> 16);
}
#define SM "v_smfmac_f32_16x16x64_bf16 "
#define QNOP "s_nop 0\n\t"
#define OEARLY 0xff
#define SAFEB "0x42200000"

using u32x16 = __attribute__((ext_vector_type(16))) unsigned;
using u32x32 = __attribute__((ext_vector_type(32))) unsigned;
template <int OFF>
__device__ __forceinline__ u16* karg() {
  u64 p;
  asm volatile("s_load_dwordx2 %0, %1, %2\n\ts_waitcnt lgkmcnt(0)" : "=s"(p) : "s"(__builtin_amdgcn_kernarg_segment_ptr()), "n"(OFF));
  return (u16*)p;
}
template <class T>
__device__ __forceinline__ T* mkptr(u32 lo, u32 hi) {
  return (T*)(((u64)hi << 32) | lo);
}

__global__ __launch_bounds__(NT, 2) void mega_kernel(Params) {
  __shared__ __attribute__((aligned(16))) unsigned char smem[L_TOTAL];
  u32x16 ka;
  u32x4 kb4;
  u32 rtt;
  u64 ts64;
  {
    static_assert(AREA == 1 << 18 && A_GY == 1 << 17 && SIDE == 1 << 13, "clear addresses");
    asm volatile("s_load_dwordx16 s[36:51], %[kp], 0x0\n\ts_load_dwordx4 s[52:55], %[kp], 0x40\n\t"
                 "s_getpc_b64 s[58:59]\n.Lmk_pc:\n\t"
                 "s_mov_b32 s62, m0\n\ts_mov_b32 m0, %[dst]\n\t"
                 "s_cmpk_lt_u32 %[b], %[pfb]\n\ts_cselect_b32 s57, 0x140, 0\n\t"
                 "v_lshlrev_b32 v1, 7, %[tid]\n\tv_min_u32 v1, .Lmk_code_end-.Lmk_pc-4, v1\n\t"
                 "v_cmp_gt_u32 vcc, s57, %[tid]\n\ts_mov_b64 exec, vcc\n\t"
                 "global_load_lds_dword v1, s[58:59]\n\ts_mov_b64 exec, -1\n\t"
                 "v_readfirstlane_b32 s56, %[tid]\n\t"
                 "v_lshrrev_b32 v1, 2, %[tid]\n\tv_cmp_eq_u32 vcc, 3, v1\n\tv_mov_b32 v3, 0x3f803f80\n\ts_lshr_b32 s57, %[b], 5\n\t"
                 "v_cndmask_b32 v2, 0, 1, vcc\n\tv_cmp_eq_u32 vcc, 2, v1\n\tv_lshlrev_b32 v1, 2, %[tid]\n\ts_bfe_u32 s60, %[b], 0x20003\n\t"
                 "v_cndmask_b32 v2, v2, v3, vcc\n\tv_cmp_eq_u32 vcc, 52, %[tid]\n\tv_mov_b32 v3, %[scl]\n\t"
                 "v_min_u32 v1, 0xfc, v1\n\tv_cndmask_b32 v2, v2, v3, vcc\n\t"
                 "ds_write_b32 v1, v2 offset:%[sm]\n\t"
                 "v_lshlrev_b32 v2, 4, %[tid]\n\tv_and_b32 v1, 0x3ff, v2\n\t"
                 "s_cmpk_ge_u32 s56, 448\n\ts_cbranch_scc1 1f\n\t"
                 "s_mul_i32 s57, s57, 0xf0000\n\ts_and_b32 s61, %[b], 7\n\ts_lshl2_add_u32 s60, s61, s60\n\t"
                 "v_bfe_u32 v3, %[tid], 2, 4\n\ts_mul_i32 s60, s60, 0x180\n\tv_mul_u32_u24 v3, 0x3000, v3\n\t"
                 "s_add_u32 s57, s57, s56\n\tv_bitop3_b32 v2, v2, %[tid], 32 bitop3:0x78\n\ts_add_u32 s57, s57, s60\n\t"
                 "v_and_or_b32 v3, v2, 48, v3\n\ts_mul_i32 s60, s56, 80\n\tv_add_u32 v3, s57, v3\n\t"
                 "s_add_u32 s60, s60, %[lwq]\n\ts_cmpk_eq_u32 s56, 384\n\ts_cselect_b64 vcc, -1, 0\n\ts_cselect_b32 m0, %[lhb], s60\n\t"
                 "v_cndmask_b32 v3, v3, v1, vcc\n\ts_mov_b32 %[rtt], 0\n\t"
                 "s_waitcnt lgkmcnt(0)\n\ts_cselect_b64 s[58:59], s[36:37], s[42:43]\n\ts_barrier\n\t"
                 "global_load_lds_dwordx4 v3, s[58:59] nt\n\t"
                 "s_memrealtime %[ts]\n\ts_branch 2f\n"
                 "1:\n\ts_lshl_b32 s57, s57, 17\n\ts_lshl_b32 s60, s60, 13\n\ts_and_b32 s61, %[b], 15\n\ts_or_b32 s57, s57, s60\n\t"
                 "s_lshl_b32 s61, s61, 9\n\t"
                 "v_mov_b64 v[4:5], 0\n\ts_or_b32 s57, s57, s61\n\tv_mov_b64 v[6:7], 0\n\t"
                 "v_or_b32 v1, s57, v1\n\tv_xor_b32 v2, 0x2000, v1\n\ts_mov_b32 exec_hi, 0\n\t"
                 "s_waitcnt lgkmcnt(0)\n\t"
                 "s_add_u32 s58, s54, 0x1fffff\n\ts_addc_u32 s59, s55, 0\n\ts_and_b32 s58, s58, 0xffe00000\n\t"
                 "global_store_dwordx4 v1, v[4:7], s[58:59] sc1\n\tglobal_store_dwordx4 v2, v[4:7], s[58:59] sc1\n\t"
                 "s_barrier\n\ts_mov_b64 exec, -1\n\ts_memrealtime %[ts]\n\t"
                 "s_waitcnt vmcnt(1)\n\ts_memtime s[58:59]\n\ts_waitcnt vmcnt(0)\n\ts_memtime s[60:61]\n\ts_waitcnt lgkmcnt(0)\n\t"
                 "s_sub_u32 %[rtt], s60, s58\n"
                 "2:\n\ts_mov_b32 m0, s62\n\ts_waitcnt lgkmcnt(0)"
                 : "=&{s[36:51]}"(ka), "=&{s[52:55]}"(kb4), [rtt] "=&s"(rtt), [ts] "=&s"(ts64)
                 : [kp] "s"(__builtin_amdgcn_kernarg_segment_ptr()), [tid] "v"(threadIdx.x), [b] "s"(blockIdx.x), [dst] "n"(L_PF),
                   [scl] "n"(__builtin_bit_cast(u32, SCL)), [sm] "n"(0), [lwq] "n"(L_WQ), [lhb] "n"(L_HB), [pfb] "n"(PFB)
                 : "memory", "vcc", "scc", "v1", "v2", "v3", "v4", "v5", "v6", "v7", "s56", "s57", "s58", "s59", "s60", "s61", "s62");
  }
  const u32 tstart = (u32)ts64;
  const int tid = threadIdx.x, lane0 = tid & 63;
  const int wave = __builtin_amdgcn_readfirstlane(tid >> 6);
  const int b = blockIdx.x;
  const int g = b & 7, j = b >> 3, hsub = j & 3, c = j >> 2, h = g * 4 + hsub;
  const bool isO = j < 20;
  const bool leader = b == 0 && wave == 6;
  const u32 fslot = (u32)((j & 1) * 16384 + ((j - 20) >> 1) * 128);
  u32 tmo = SPIN_CAP;
  volatile LAS u32* cnt = (volatile LAS u32*)(smem + L_CNT);
  Params P;
  P.x0 = mkptr<const u16>(ka[0], ka[1]);
  P.kc = mkptr<const u16>(ka[2], ka[3]);
  P.vc = mkptr<const u16>(ka[4], ka[5]);
  P.wq = mkptr<const u16>(ka[6], ka[7]);
  P.wo = mkptr<const u16>(ka[8], ka[9]);
  P.w1 = mkptr<const u16>(ka[10], ka[11]);
  P.w3 = mkptr<const u16>(ka[12], ka[13]);
  P.ws = mkptr<u32>(kb4[2], kb4[3]);
  u32* const wsb = (u32*)(((uintptr_t)P.ws + 0x1fffffu) & ~(uintptr_t)0x1fffffu);
  const __amdgpu_buffer_rsrc_t rs = __builtin_amdgcn_make_buffer_rsrc(wsb, 0, (int)WS_END, 0x00020000);
  const u32x4 rq = {(u32)(uintptr_t)wsb, (u32)((uintptr_t)wsb >> 32) & 0xffffu, WS_END, 0x00020000u};

  u32 to2;
  u32 tref = 0u, ar = 0u;
  u32x32 kv0, kv1, kv2, kv3, kv4, kv5;
  asm volatile(""
               : "={v[64:95]}"(kv0), "={v[96:127]}"(kv1), "={v[128:159]}"(kv2), "={v[160:191]}"(kv3), "={v[192:223]}"(kv4),
                 "={v[224:255]}"(kv5));
  const int smb = (int)(uintptr_t)(LAS unsigned char*)smem;
  const u32 gop = A_GO + go_slot(hsub * 2) + c * 256;
  u32 tstep = 4 * PQ(0);
  u32 tnext = tstart * 8u + 5 * TSQ + (u32)(hsub * PQ(0) + wave * WOFS);
  const size_t rowbase = (size_t)h * SK + (SK - WIN) + ROW0;
  const u16* vb = P.vc + rowbase * DH;
  const int rot = ROT;
  const u16* ws0 = isO ? P.wo + (size_t)(768 * g) * D + 32 * j : ((j & 1) ? P.w3 : P.w1) + 192 * g + 32 * ((j - 20) >> 1);
  const u16* w2s = mkptr<const u16>(ka[14], ka[15]) + (size_t)(192 * g) * D + 32 * j;
  const int nin = isO ? NINO : ((((uintptr_t)ws0 >> 7) & 7) == 3 ? NINS : NINF);
  const u32 word = (((uintptr_t)ws0 >> 6) & 1) ? (isO ? ORDO : ORDF) : 0x543210u;
  int wn = 0;
  u32 hb = 0u;
  u32 exa = 0u, exl = 0u;
  {
    constexpr int L_CTL = L_LAND + 6 * 2048;
    volatile LAS u32* const CTL = (volatile LAS u32*)(smem + L_CTL);
    u32 gd;
    const u32 lho = WS_HB + (lane0 >> 1) * HBS + (lane0 & 1) * 16;
    const u32 hso = lane0 < 8 ? WS_HB + (u32)h * HBS + lane0 * 4 : WS_GOF + g * HBS;
    if (wave < 6) {
      const u16* s0 = P.wq + (size_t)(80 * c) * (H * DH) + h * DH + wave * 32;
      u32 tq = tstart * 8u + TSQ + (u32)wave * (TSTEP / 8);
#pragma unroll 1
      for (int p = 1; p < 5; ++p) {
        pace(tq, TSQ);
        dma_piece(s0, H * DH, smem, L_WQ + wave * 5120, p, lane0);
      }
    } else if (wave == 6) {
      u32 keep;
      asm volatile("s_mov_b32 %0, m0\n\ts_mov_b32 m0, %3\n\ts_nop 2\n\tglobal_load_lds_dword %1, %2 offset:1024\n\ts_mov_b32 m0, %0"
                   : "=&s"(keep)
                   : "v"(lane0 * 4), "s"(P.x0), "s"((u32)(smb + L_HB))
                   : "memory");
      if (j == SJ) store16(rs, rp_line(0, lane0 >> 3, g) + (lane0 & 7) * 16, u32x4{tstart, tstart, tstart, tstart});
      if (leader) {
        ctl_load<true>(rq, lho, (u32)(smb + L_CTL));
        ctl_load<false>(rq, WS_GOF + (lane0 & 7) * HBS, (u32)(smb + L_CTL + 2560));
      }
      asm volatile("s_waitcnt vmcnt(0)" ::: "memory");
      const u32x4 lv = afrag(smem, L_HB + lane0 * 16);
      const u32 xb = *reinterpret_cast<const u32*>(smem + L_HB + 1024 + lane0 * 4);
      float v[10];
#pragma unroll
      for (int k = 0; k < 4; ++k) {
        v[2 * k] = bflo(lv[k]);
        v[2 * k + 1] = bfhi(lv[k]);
      }
      v[8] = bflo(xb);
      v[9] = bfhi(xb);
      float sm = 0.f;
#pragma unroll
      for (int k = 0; k < 10; ++k) sm += v[k];
      const float mean = wave_sum(sm) * (1.0f / D);
      float q = 0.f;
#pragma unroll
      for (int k = 0; k < 10; ++k) q = fmaf(v[k] - mean, v[k] - mean, q);
      const float var = wave_sum(q) * (1.0f / D);
      const float mb = bf16r(mean);
      const float ri = __builtin_amdgcn_rcpf(bf16r(__builtin_amdgcn_sqrtf(bf16r(bf16r(var) + 1e-5f))));
      u32 hp[5];
#pragma unroll
      for (int k = 0; k < 5; ++k) {
        const u32 cc = cvt2(v[2 * k] - mb, v[2 * k + 1] - mb);
        hp[k] = cvt2(bflo(cc) * ri, bfhi(cc) * ri);
      }
      *reinterpret_cast<u32x4*>(smem + L_LN + lane0 * 16) = u32x4{hp[0], hp[1], hp[2], hp[3]};
      *reinterpret_cast<u32*>(smem + L_LN + 1024 + lane0 * 4) = hp[4];
      asm volatile("s_waitcnt lgkmcnt(0)" ::: "memory");
      *(volatile LAS u32*)(smem + L_VOTE + 4) = 1u;
    } else {
      ctl_load<false>(rq, hso, (u32)(smb + L_CTL + 2304));
      hb = rtt > PTHR ? 1u << ((g >> 2) ^ (j & 1)) : 0u;
    }
    if (wave == 1) *(volatile LAS u32x2*)(smem + L_CLK) = u32x2{now(), rtnow()};
    *reinterpret_cast<u32x4*>(smem + L_P + wave * P_W + lane0 * 16) = u32x4{0u, 0u, 0u, 0u};
    *reinterpret_cast<u32x4*>(smem + L_P + wave * P_W + 256 + lane0 * 16) = u32x4{0u, 0u, 0u, 0u};
    const u16* kb = P.kc + rowbase * DH;
    const u32 vo = (lane0 >> 3) * 384 + 16 * (lane0 & 7);
    const u32 kw = (u32)(smb + L_W2 + wave * 1024 + lane0 * 16), kr = (u32)(smb + L_W2 + wave * 1024 + (lane0 & 7) * 128 + (lane0 & 8) * 8 + (lane0 >> 4) * 16);
#define KPERM(i)                                                                          \
  asm volatile("ds_write_b128 %[w], v[%[r0]:%[r1]]\n\tds_read_b128 v[%[r0]:%[r1]], %[r]" \
               :                                                                          \
               : [w] "v"(kw), [r] "v"(kr), [r0] "n"(kreg(i)), [r1] "n"(kreg(i) + 3)       \
               : "memory")
#define WNEXT                                                                                  \
  dma_piece(ws0, isO ? D : F, smem, L_WB, wave * (isO ? 6 : 5) + ((word >> (4 * wn)) & 7), fresh6(lane0)); \
  wn = wn == (isO ? 5 : 4) ? 0 : wn + 1;
    const u32 qo = WS_GQ1 + (u32)(h * 8 + (lane0 >> 3)) * Q1S + wave * 128 + (lane0 & 7) * 16;
    auto step = [&](auto uc) __attribute__((always_inline)) {
      constexpr int u = decltype(uc)::v;
      if (u == UAD + 1) tstep += __builtin_amdgcn_readfirstlane(cnt[2]);
      BURST(u, vo, (u < 8 ? kb : vb) + ((u + rot) & 7) * GST * DH);
      if (wcnt(u + 1) != wcnt(u) || u + 1 == USW) {
        tnext += hsub * (PQ(u + 1) - PQ(u));
        tstep += 4 * (PQ(u + 1) - PQ(u));
      }
      if (u >= 2 && u < 10) {
        KPERM((3 * u + 18) % 24);
        KPERM((3 * u + 19) % 24);
        KPERM((3 * u + 20) % 24);
      }
      if (u == UAD - 3 && wave == 7) {
        ctl_load<false>(rq, rp_line(0, g, lane0 & 7), (u32)(smb + L_CTL + 2816));
        ++exa;
      }
      if (u == 0) {
        att_table<0>(smem, fresh6(lane0), wave, gop);
        att_table<1>(smem, fresh6(lane0), wave, gop);
      }
      if (u == UQ) {
        if (wave < 6) {
          const int lane = fresh6(lane0), m = lane & 15, G = lane >> 4;
          const int q_l0 = L_WQ + wave * 5120 + wlane(lane, 0), q_l1 = L_WQ + wave * 5120 + wlane(lane, 1);
          const int q_bk = 4096 - (G >> 1) * 1024;
          const int st = m == 8 ? 64 : 0;
          const int sa = m == 8 ? L_LN + 160 * c + 16 * G : (m == 15 ? L_ONE : L_ZERO);
          const u32 q_dst = WS_GQ1 + (u32)(h * 8 + c) * Q1S + wave * 128 + (lane >> 5) * 64 + m * 4;
          asm volatile("" ::: "memory");
          lds_wait((volatile LAS u32*)(smem + L_VOTE + 4), 1u);
          const u32x4 f0 = afrag(smem, sa), f1 = afrag(smem, sa + st), f2 = afrag(smem, G < 2 ? sa + 2 * st : L_TAG);
          f32x4 q_d0 = {0.f, 0.f, 0.f, 0.f}, q_d1 = q_d0;
          q_d0 = mfma32(f0, wfrag(smem, q_l0, 0), q_d0);
          q_d1 = mfma32(f0, wfrag(smem, q_l1, 0), q_d1);
          q_d0 = mfma32(f1, wfrag(smem, q_l0, 2048), q_d0);
          q_d1 = mfma32(f1, wfrag(smem, q_l1, 2048), q_d1);
          q_d0 = mfma32(f2, wfrag(smem, q_l0 + q_bk, 0), q_d0);
          q_d1 = mfma32(f2, wfrag(smem, q_l1 + q_bk, 0), q_d1);
          auto sw = __builtin_amdgcn_permlane32_swap(__float_as_uint(sum4(q_d0)), __float_as_uint(sum4(q_d1)), false, false);
          auto sx = __builtin_amdgcn_permlane16_swap(sw[1], sw[1], false, false);
          const f32x4 qd = quad4(__uint_as_float(cvt2(__uint_as_float(sx[0]), __uint_as_float(sx[1]))));
          if ((lane0 & 19) == 0) store16<AUXQ>(rs, q_dst, __builtin_bit_cast(u32x4, qd));
          ++exa;
        }
      }
      if (u == UAD - 1 && wave == 7) {
        asm volatile("" ::: "memory");
        const int d = (int)(CTL[704 + lane0] - tstart);
        const float sd = sum8((u32)(d + DWIN) < 2u * DWIN ? (float)d : 0.f);
        cnt[2] = (u32)(int)__builtin_rintf(sd * (DEQ / (15 - NUN - UAD)));
        int len = 5 * TSQ + 3 * NUN * TSE + 7 * (TSE / 8);
#pragma unroll
        for (int k = 0; k < 16 - NUN; ++k) len += 4 * PQ(k);
        *(volatile LAS u32*)(smem + L_AN0) = tstart * 8u + (u32)(len + (int)sd);
      }
      if ((u == UB && wave < 6) || (u == UB - 1 && wave == 7)) {
        asm volatile("" ::: "memory");
        if (u == UB - 1) {
          hb += (__builtin_amdgcn_readfirstlane(CTL[576 + c]) & ~3u) + 4u;
          *(volatile LAS u32*)(smem + L_VOTE + 8) = hb;
          hb += 4u;
        }
        u32 t0, t1, last;
        asm volatile("s_mov_b64 exec, 1\n\tv_mov_b32 %0, 1\n\tv_mov_b32 %1, %[sm]\n\tds_add_rtn_u32 %0, %1, %0 offset:%[c]\n\t"
                     "ds_read_b32 %1, %1 offset:%[v]\n\ts_waitcnt lgkmcnt(0)\n\tv_readfirstlane_b32 %2, %0\n\t"
                     "s_cmp_eq_u32 %2, 6\n\ts_cbranch_scc0 1f\n\tbuffer_store_dword %1, off, %[rq], %[o] sc1\n1:\n\t"
                     "s_cselect_b32 %2, 1, 0\n\ts_mov_b64 exec, -1"
                     : "=&v"(t0), "=&v"(t1), "=&s"(last)
                     : [sm] "s"(smb), [c] "n"(L_CNT + 4), [v] "n"(L_VOTE + 8), [rq] "s"(rq), [o] "s"(WS_HB + h * HBS + c * 4)
                     : "memory", "scc");
        exa += last;
        exl += last;
      }
      if (u == UL && wave == 7) {
        ctl_load<false>(rq, hso, (u32)(smb + L_CTL + 2048));
        ++exa;
        if (UL + 2 < UK) ++exl;
      }
      if (u == UG && leader) {
        ctl_load<true>(rq, lho, (u32)(smb + L_CTL + 1024));
        ++exa;
      }
      if (u == UK && wave == 7) {
        HS_WAIT((u32)ch == 0xffu)
        if (lane0 == 0) cnt[1] = 15u;
      }
      if (u == UC && wave < 6) {
        lds_wait(cnt + 1, 15u);
        ctl_load<true, true>(rq, qo, (u32)(smb + L_LAND + wave * 2048));
        ++exa;
      }
      if (u == UW && leader) {
        const u32x4 xa = afrag(smem, L_CTL + lane0 * 16);
        u32x4 lv;
#pragma unroll 1
        for (;;) {
          asm volatile("" ::: "memory");
          lv = afrag(smem, L_CTL + 1024 + lane0 * 16);
          const bool same = lv[0] == xa[0] || lv[1] == xa[1] || lv[2] == xa[2] || lv[3] == xa[3];
          if (__builtin_amdgcn_ballot_w64(same) == 0ull || tmo == 0u) break;
          --tmo;
          __builtin_amdgcn_s_sleep(4);
          ctl_load<true>(rq, lho, (u32)(smb + L_CTL + 1024));
          asm volatile("s_waitcnt vmcnt(0)" ::: "memory");
        }
        u32 nv = 256u;
#pragma unroll
        for (int e = 0; e < 4; ++e)
          nv += (u32)__builtin_popcountll(__builtin_amdgcn_ballot_w64((lv[e] & 2u) != 0u)) -
                (u32)__builtin_popcountll(__builtin_amdgcn_ballot_w64((lv[e] & 1u) != 0u));
        if (lane0 < 8) __builtin_amdgcn_raw_buffer_store_b32(CTL[640 + lane0] + (nv > 256u ? 2u : 1u), rs, WS_GOF + lane0 * HBS, 0, 16);
        ++exa;
        ++exl;
      }
      if (u == UX && wave == 7) {
        ctl_load<false>(rq, hso, (u32)(smb + L_CTL + 2048));
        ++exa;
      }
      if (u == UX && wave < 6) {
        const int lane = fresh6(lane0);
        const int ca = L_CS + wave * 128 + (((lane >> 1) ^ (lane >> 5)) & 1) * 64 + (lane & 1) * 32;
        const f32x4 c0 = *reinterpret_cast<const f32x4*>(smem + ca), c1 = *reinterpret_cast<const f32x4*>(smem + ca + 16);
#pragma unroll 1
        for (int p = 0; p < 5; ++p) {
          u32x4* const pw = reinterpret_cast<u32x4*>(smem + L_WQ + wave * 5120 + p * 1024 + lane * 16);
          const u32x4 w = *pw;
#define CW(k, c, e) cvt2(fmaf(bflo(w[k]), SCL, c[e]), fmaf(bfhi(w[k]), SCL, c[e + 1]))
          *pw = u32x4{CW(0, c0, 0), CW(1, c0, 2), CW(2, c1, 0), CW(3, c1, 2)};
        }
      }
      if (u == UF) {
        if (wave < 6) {
          q1_finish(smem, fresh6(lane0), wave);
          asm volatile("s_waitcnt lgkmcnt(0)" ::: "memory");
          if (lane0 == 0) lds_add(smem, L_CNT, 1u);
        }
      }
      if (wcnt(u) != 0 && (u - WU0) / WSP < nin && (wave & (WSP - 1)) == (u - WU0) % WSP) {
        u32 tw = tnext - WBK - hsub * PQ(u + 1);
        pace(tw, 0u);
        WNEXT
        ++exa;
        ++exl;
      }
    };
    static_assert(UQ >= 1 && UQ + 2 <= UB && UB < UL && UL + 2 <= UK && UK <= UC && UG + 2 <= UW && UC + 2 <= UF && UF < UX && UX >= 9 && UX + 2 < 16 - NUN && UAD >= 3 && WU0 >= UB, "order");
    slots<0, UX + 1>(step);
  }

#define lane0 lane_id()
#pragma unroll 1
  for (int half = 0;; ++half) {
    {
      u32x4 O0, O1, O2, S0, S1;
      float sk;
      Att at;
      const int lane = fresh6(lane0);
      const u32 gso = (u32)half * GO_SZ;
      {
        const u32 off = fresh(ar + A_GQ + hsub * 16384 + wave * 128 + (wave < 6 ? (lane >> 3) * 896 + (lane & 7) * 16 : (lane & 7) * 896));
        if (half == 1) {
          if (wave == 7) wait_tt(A_Q);
        } else {
          BURST(UX + 1, (u32)((lane >> 3) * 384 + 16 * (lane & 7)), vb + ((UX + 1 + rot) & 7) * GST * DH);
          lds_wait(cnt, 6u);
        }
        asm volatile("s_cmp_eq_u32 %[hf], 0\n\ts_cbranch_scc1 1f\n\ts_cmp_lt_u32 %[wv], 7\n\ts_cselect_b64 exec, -1, 0\n\ts_barrier\n\t"
                     "buffer_load_dwordx4 v[44:47], %[of], %[rq], 0 offen sc1\n\ts_mov_b64 exec, -1\n\ts_setprio 2\n1:"
                     : "={v[44:47]}"(O1)
                     : [hf] "s"(gso), [wv] "s"(wave), [of] "v"(off), [rq] "s"(rq)
                     : "scc", "memory");
        att_setup(at, smem, lane, wave);
        to2 = T_O2;
#define QK(W, a, k0, k1) \
  "s_waitcnt lgkmcnt(" #W ")\n\t" SM "v[32:35], v[" #a ":" #a "+3], v[" #k0 ":" #k0 "+7], %[ix]\n\t" SM "v[36:39], v[" #a ":" #a "+3], v[" #k1 ":" #k1 "+7], %[ix]\n\t"
#define QLD "buffer_load_dwordx4 v[44:47], %[of], %[rq], 0 offen sc1\n"
#define QWT "s_sub_u32 s88, s88, 1\n\ts_waitcnt vmcnt(0)\n\t"
        u32 t, z;
        asm volatile("v_mov_b64 v[32:33], 0\n\tv_mov_b64 v[34:35], 0\n\tv_mov_b64 v[36:37], 0\n\tv_mov_b64 v[38:39], 0\n\t"
                     "v_mov_b32 %[z], 0\n\ts_cmp_eq_u32 %[hf], 0\n\ts_cbranch_scc1 5f\n\t"
                     "s_mov_b32 s88, 0x10000\n\ts_cmp_lt_u32 %[wv], 6\n\ts_cbranch_scc1 2f\n\t"
                     "s_bitcmp0_b32 %[wv], 0\n\ts_cselect_b64 exec, -1, 0\n\ts_branch 3f\n"
                     "6:\n\t" QLD "3:\n\t" QWT
                     "v_add_f32 v48, v44, v45\n\tv_mul_f32 v49, 0x3acccccd, v47\n\tv_add_f32 v48, v48, v46\n\t" AND3("v44", "v45", "v46")
                     "v_add_f32_dpp v49, v49, v49" Q1 "v_add_f32_dpp v48, v48, v48" Q1 AND3("%[t]", "1", "v47")
                     "v_add_f32_dpp v49, v49, v49" Q2 "v_add_f32_dpp v48, v48, v48" Q2 "v_cmp_eq_u32 vcc, 0, %[t]\n\t"
                     "v_add_f32_dpp v49, v49, v49" HM "v_add_f32_dpp v48, v48, v48" HM
                     "v_mul_f32 v49, -v49, v49\n\tv_fmac_f32 v49, 0x3acccccd, v48\n\t" CV "v48, 0, |v49|\n\tv_add_f32 v48, 0x3727c5ac, v48\n\t"
                     CV "v48, 0, v48\n\tv_sqrt_f32 v48, v48\n\ts_cselect_b64 vcc, 0, vcc\n\t" CV "v48, 0, v48\n\tv_rcp_f32 v48, v48\n\t"
                     "s_cbranch_vccnz 6b\n\tds_write_b32 %[qw], v48\n\ts_cbranch_execnz 4f\n\ts_branch 2f\n"
                     "1:\n\t" QLD "2:\n\t" QWT
                     "v_permlane32_swap_b32 v44, v45\n\tv_permlane32_swap_b32 v46, v47\n\t"
                     "v_add_f32 v48, v44, v45\n\tv_add_f32 v49, v46, v47\n\t" AND3("v44", "v45", "v46") AND3("%[t]", "1", "v47")
                     "v_permlane16_swap_b32 v48, v49\n\tv_add_f32 v48, v48, v49\n\t"
                     "v_cmp_eq_u32 vcc, 0, %[t]\n\ts_nop 0\n\tv_add_f32_dpp v48, v48, v48" R8
                     CV "v49, v48, v48\n\tds_write_b16 %[qw], v49\n\ts_cselect_b64 vcc, 0, vcc\n\t"
                     "s_cbranch_vccnz 1b\n\ts_mov_b64 exec, -1\n4:\n\ts_waitcnt lgkmcnt(0)\n\ts_barrier\n"
                     "5:\n\tds_read_b128 v[40:43], %[q0]\n\tds_read_b128 v[44:47], %[q1]\n\t"
                     "ds_read_b128 v[48:51], %[q2]\n\tds_read_b128 v[52:55], %[q3]\n\t"
                     "ds_read_b128 v[56:59], %[q4]\n\tds_read_b128 v[60:63], %[q5]\n\tds_read_b32 %[sk], %[z] offset:%[su]\n\t"
                     QK(6, 40, 64, 96) QK(5, 44, 72, 104) "ds_read_b128 v[40:43], %[z]\n\t"
                     QK(5, 48, 80, 112) "ds_read_b128 v[44:47], %[z]\n\t" QK(5, 52, 88, 120) "ds_read_b128 v[48:51], %[z]\n\t"
                     QK(5, 56, 128, 144) QK(3, 60, 136, 152)
                     : "=&{v[40:43]}"(O0), "+{v[44:47]}"(O1), "=&{v[48:51]}"(O2), "=&{v[32:35]}"(S0), "=&{v[36:39]}"(S1),
                       [t] "=&v"(t), [z] "=&v"(z), [sk] "=&{v31}"(sk)
                     : [hf] "s"(gso), [wv] "s"(wave), [of] "v"(off), [rq] "s"(rq), [qw] "v"(at.qw), [q0] "v"(at.qad[0]),
                       [q1] "v"(at.qad[1]), [q2] "v"(at.qad[2]), [q3] "v"(at.qad[3]), [q4] "v"(at.qad[4]), [q5] "v"(at.qad[5]),
                       [ix] "v"(at.idxq), [su] "n"(L_SUM), "{v[64:95]}"(kv0), "{v[96:127]}"(kv1), "{v[128:159]}"(kv2)
                     : V52, "s88", "vcc", "scc", "memory");
      }
      u32 hq = gso;
      asm volatile("" : "+s"(hq));
      if (__builtin_expect(hq == 0u, 0)) {
        const u32 tn_ = tnext, ts_ = tstep, ea_ = exa;
        u32 tnext = tn_, tstep = ts_, exa = ea_, exl = 0u;
        int wn = nin;
        const int lv_ = fresh6(lane0);
        const u32 vo = (lv_ >> 3) * 384 + 16 * (lv_ & 7);
#pragma unroll
        for (int u = UX + 2; u < 16 - NUN; ++u) {
          BURST(u, vo, vb + ((u + rot) & 7) * GST * DH);
          if (u == UX + 2 && wave == 6) {
            const u32x2 c0 = *(volatile LAS u32x2*)(smem + L_CLK);
            const float r = (float)(rtnow() - c0[1]) * (24.2f * 512.f) * __builtin_amdgcn_rcpf((float)(now() - c0[0])) - CLKO;
            *(volatile LAS u32*)(smem + L_CLK) = ((u32)__builtin_fminf(__builtin_fmaxf(r, 0.f), 127.f) << 25) | 1u;
            wait_tt<L_ZERO>(tnext - 160u);
          }
          if (u == UX + 2 && wave == 7) {
            constexpr int L_CTL = L_LAND + 6 * 2048;
            volatile LAS u32* const CTL = (volatile LAS u32*)(smem + L_CTL);
            const u32 hso = lv_ < 8 ? WS_HB + (u32)h * HBS + lv_ * 4 : WS_GOF + g * HBS;
            u32 gd, tmo = SPIN_CAP;
            HS_WAIT(false)
            if (lane0 == 8) *(volatile LAS u32*)(smem + L_AR) = (u32)(g & 3) * AREA + (((u32)(g >> 2) ^ (gd >> 1)) & 1u) * SIDE;
          }
        }
        tnext += wave * (TSE / 8 - WOFS) - hsub * PQ(15);
        tstep = TSE;
#pragma unroll
        for (int t = 48 - 3 * NUN; t < 48; ++t) {
          LOADW(t, vo, vb + ((t / 3 + rot) & 7) * GST * DH);
        }
        if (wave == 7) {
          const u32 a7 = *(volatile LAS u32*)(smem + L_AR) + (u32)(j >> 3) * 16384u + (u32)(j & 7) * 1024u;
          const u32x4 zz = __builtin_bit_cast(u32x4, zero4());
          store16(rs, lv_ * 16 + a7 + 2 * 16384, zz);
          store16(rs, lv_ * 16 + a7 + (j < 16 ? 6 : 8) * 16384, zz);
          store16(rs, lv_ * 16 + a7 + 12 * 16384, zz);
        }
#pragma unroll 1
        for (int p = 0; p < (isO ? NPRE : NPF); ++p) {
          pace(tnext, isO ? TPV : TPF);
          WNEXT
        }
      }
#define PW(V, L) "s_waitcnt vmcnt(" #V "+%[no]) lgkmcnt(" #L ")\n\t"
#define PX(o, a, k) SM "v[40+4*" #o ":43+4*" #o "], v[" #a ":" #a "+3], v[160+24*" #k "+8*" #o ":167+24*" #k "+8*" #o "], %[ix]\n\t"
#define ID " quad_perm:[0,1,2,3] row_mask:0xf bank_mask:"
#define OW(o) CV "v[40+2*" #o "], v[40+4*" #o "], v[41+4*" #o "]\n\t" CV "v[41+2*" #o "], v[42+4*" #o "], v[43+4*" #o "]\n\t" \
  "ds_write_b64 %[oa], v[40+2*" #o ":41+2*" #o "] offset:128*" #o "\n\t"
#define TR "ds_read_b64_tr_b16 "
#define AREAD "ds_read_b64 v[46:47], v50 offset:%[lar]\n\t"
#define ACHK "s_waitcnt lgkmcnt(0)\n\tv_cmp_ne_u32 vcc, 0, v47\n\t"
      asm volatile("s_mov_b32 s80, 0xff00ff\n\ts_mov_b32 s81, 0xff00ff00\n\ts_mov_b32 s82, " SAFEB "\n\ts_mov_b32 s83, 1\n\t" QNOP
                   "v_add_f32 v32, v32, v34\n\tv_add_f32 v34, v33, v35\n\t"
                   "v_add_f32_dpp v34, v36, v38" ID "0x3\n\tv_add_f32_dpp v32, v37, v39" ID "0xc\n\t"
                   "s_nop 0\n\tv_add_f32_dpp v32, v34, v32" R8 "v_mul_f32 %[sk], v32, %[sk]\n"
                   "1:\n\tv_exp_f32 v33, %[sk]\n\ts_cmp_lt_u32 %[wv], 4\n\t" CV "v33, v33, 0\n\t"
                   "ds_write2_b32 %[wa], v33, v33 offset1:%[o1]\n\t"
                   "ds_read_b128 v[52:55], %[ra]\n\tds_read_b128 v[56:59], %[ra] offset:16\n\t"
                   "ds_read_b128 v[60:63], %[ra] offset:32\n\tds_read_b128 v[36:39], %[ra] offset:48\n\ts_setprio 0\n\t"
                   "v_cmp_nge_f32 vcc, s82, |%[sk]|\n\tds_write_b16 %[la], v33\n\t"
                   PW(18, 4) PX(0, 52, 0) PX(1, 52, 0) PX(2, 52, 0) PW(12, 1) PX(0, 56, 1) PX(1, 56, 1) PX(2, 56, 1)
                   PW(6, 1) PX(0, 60, 2) PW(2, 1) PX(0, 36, 3) PX(1, 60, 2) PW(1, 1) PX(1, 36, 3) PX(2, 60, 2) PW(0, 1) PX(2, 36, 3)
                   "s_cbranch_vccnz 7f\n2:\n\ts_mov_b64 exec, s[80:81]\n\t" OW(0) OW(1) OW(2)
                   "s_mov_b64 exec, -1\n\tv_mov_b32 v50, 0\n\ts_cbranch_scc0 3f\n\t"
                   "v_mov_b32_dpp v51, %[ma] row_shr:2 row_mask:0xf bank_mask:0xf bound_ctrl:0\n\t"
                   "ds_read_b128 v[32:35], %[ma]\n\tds_read_b128 v[36:39], v51\n\t"
                   "s_cmp_lt_u32 %[wv], 3\n\ts_waitcnt lgkmcnt(0)\n\ts_barrier\n\t"
                   TR "v[52:53], %[sa]\n\t" TR "v[54:55], %[sa] offset:%[os]\n\ts_cbranch_scc0 4f\n\t"
                   TR "v[56:57], %[sa] offset:64\n\t" TR "v[58:59], %[sa] offset:%[os]+64\n\t" AREAD
                   "s_waitcnt lgkmcnt(3)\n\t" MF F2 ", " F0 ", v[32:35], 0\n\ts_waitcnt lgkmcnt(1)\n\t" MF F2 ", " F1 ", v[36:39], " F2 "\n\t"
                   ACHK "v_add_u32 v56, %[ds], v46\n\ts_nop 3\n\ts_cbranch_vccnz 8f\n\t"
                   CV "v52, v60, v61\n\t" CV "v53, v62, v63\n\tv_or_b32 v52, 1, v52\n\tv_or_b32 v53, 1, v53\n\t"
                   "buffer_store_dwordx2 v[52:53], v56, %[rq], %[so] offen sc1\n\ts_branch 9f\n"
                   "7:\n\tv_mov_b32 v52, 0\n\tv_mov_b32 v53, 0xff800000\n\tv_cmp_ne_u32 vcc, s82, v52\n\ts_and_b64 exec, vcc, 1\n\t"
                   "ds_write2_b32 v52, v53, v53 offset0:%[lar]/4+1 offset1:%[lar]/4+2\n\t"
                   "s_cmp_lt_u32 %[wv], 4\n\ts_branch 2b\n"
                   "8:\n\tds_max_f32 v50, %[sk] offset:%[lar]+8\n\ts_mov_b32 s82, 0\n\ts_waitcnt lgkmcnt(0)\n\ts_barrier\n\t"
                   "ds_read_b32 v52, v50 offset:%[lar]+8\n\tds_write_b32 v50, v50 offset:%[lar]+4\n\t"
                   "ds_read_b128 v[40:43], v50\n\tds_read_b128 v[44:47], v50\n\tds_read_b128 v[48:51], v50\n\ts_waitcnt lgkmcnt(0)\n\t"
                   "v_readfirstlane_b32 s83, v52\n\tv_sub_f32 %[sk], %[sk], v52\n\ts_bitset1_b32 s83, 0\n\ts_branch 1b\n"
                   "4:\n\t" AREAD "s_waitcnt lgkmcnt(1)\n\t" MF F2 ", " F0 ", v[32:35], 0\n\t"
                   ACHK "v_add_u32 v56, %[ds], v46\n\tv_mov_b32 v58, s83\n\ts_nop 3\n\t"
                   "v_add_f32 v60, v60, v61\n\tv_add_f32 v62, v62, v63\n\tv_add_f32 v59, v60, v62\n\ts_cbranch_vccnz 8b\n\ts_nop 0\n\t"
                   "v_add_f32_dpp v59, v59, v59 row_bcast:15 row_mask:0xa bank_mask:0xf\n\tv_or_b32 v59, 1, v59\n\t"
                   "buffer_store_dwordx2 v[58:59], v56, %[rq], %[so] offen sc1\n\ts_branch 9f\n"
                   "3:\n\ts_waitcnt lgkmcnt(0)\n\ts_barrier\n\t" AREAD "v_bfrev_b32 v56, 1\n\t" ACHK "s_cbranch_vccnz 8b\n\t"
                   "buffer_store_dword v56, v56, %[rq], 0 offen\n9:"
                   : "+{v[40:43]}"(O0), "+{v[44:47]}"(O1), "+{v[48:51]}"(O2), "+{v[32:35]}"(S0), "+{v[36:39]}"(S1), [sk] "+{v31}"(sk)
                   : [ix] "v"(at.idx), [no] "n"(NPRE), [la] "v"(at.la), [oa] "v"(at.oa), [sa] "v"(at.sa), [ma] "v"(at.ma),
                     [wa] "v"(at.wa), [ra] "v"(at.ra), [o1] "n"(P_U / 4 + 1), [ds] "v"(at.dst), [wv] "s"(wave), [rq] "s"(rq),
                     [so] "s"(gso), [lar] "n"(L_AR), [os] "n"(OM_S * 8), "{v[160:191]}"(kv3), "{v[192:223]}"(kv4), "{v[224:255]}"(kv5)
                   : V52, "s80", "s81", "s82", "s83", "vcc", "scc", "memory");
      tref = now();
      ar = __builtin_amdgcn_readfirstlane(O1[2]);
    }
    const u32 go = ar + A_GO + (u32)half * GO_SZ;
    const u32 gyw = A_GY + (u32)half * GY_PAIR;
    const u32 gy = gyw + ar;
    const u32 gh = go + A_GH;
    const int land = L_LAND + wave * 2048;
    if (isO) {
      {
        const int lane = fresh6(lane0);
        const u32 voff = fresh(go + go_slot(wave) + ((lane * 16) ^ ((lane >> 4) * 64)));
        if (wave == 7) {
          if (half)
            wait_until(tref + to2);
          else
            wait_tt<L_AN0>(B_O);
        }
        if (wave == 6 && half == 0) wait_until(tref + 24 * T_OL);
        asm volatile("s_mov_b32 m0, %2\n\ts_barrier\n\t"
                     "buffer_load_dwordx4 %0, %1, 0 offen sc1 lds\n\tbuffer_load_dwordx4 %0, %1, 0 offen offset:1024 sc1 lds"
                     :
                     : "v"(voff), "s"(rq), "s"(land)
                     : "memory");
        const int l0 = fresh(L_WB + wave * 6144 + wlane(lane, 0)), l1 = l0 ^ 32;
        asm volatile("s_waitcnt vmcnt(3)" ::: "memory");
        u32x4 wf[6];
#pragma unroll
        for (int k = 0; k < 3; ++k) {
          wf[2 * k] = wfrag(smem, l0, 2048 * k);
          wf[2 * k + 1] = wfrag(smem, l1, 2048 * k);
        }
        asm volatile("" ::: "memory");
        const int n = lane & 15, G = lane >> 4;
        const int r0_ = land + (n & 7) * 256 + (n & 3) * 64;
        const int ra = fresh(n < 8 ? r0_ + G * 16 : r0_ ^ 192), rb = fresh((r0_ + G * 16) ^ 64), rc = fresh((r0_ + G * 16) ^ 128);
        const int rw = fresh(L_RED + (lane >> 1) * 80 + wave * 8 + (lane & 1) * 4);
        const int rr = fresh(L_RED + n * 80);
        const u32 dst = fresh(gyw + gy_rep(G) + gy_chunk(4 * j + (n >> 2)) + (n & 3) * 4 + g * 256);
        u32 t;
#define RS(r, e) "v_mul_f32_dpp v" #r ", v41, v" #r " row_newbcast:" #e ALL
        asm volatile("s_mov_b64 s[82:83], -1\n\ts_movk_i32 s89, %[sd]\n\t" POLL_HEAD
                     "s_branch 5f\n"
                     "7:\n\tv_max_f32_dpp v41, v52, v52" Q1 "s_nop 1\n\tv_max_f32_dpp v41, v41, v41" Q2
                     "s_nop 1\n\tv_max_f32_dpp v41, v41, v41" HM "v_sub_f32 v41, v52, v41\n\tv_exp_f32 v41, v41\n\ts_nop 0\n\t"
                     "v_mul_f32 v42, v41, v43\n\ts_nop 1\n\tv_add_f32_dpp v42, v42, v42" Q1 "s_nop 1\n\tv_add_f32_dpp v42, v42, v42" Q2
                     "s_nop 1\n\tv_add_f32_dpp v42, v42, v42" HM "v_mov_b32_dpp v41, v41 row_shl:4 row_mask:0x2 bank_mask:0xf\n\t"
                     "v_rcp_f32 v42, v42\n\ts_nop 0\n\t"
                     RS(44, 8) RS(45, 9) RS(46, 10) RS(47, 11) RS(48, 8) RS(49, 9) RS(50, 10) RS(51, 11) "s_branch 3f\n"
                     "2:\n\tv_cmp_eq_u32 vcc, 0, %[t]\n\ts_cbranch_vccz 7b\n\t" POLL_MASK POLL_LOAD
                     "\n5:\n\tv_mov_b32 v41, 1\n\t" POLL_READ
                     "s_waitcnt lgkmcnt(2)\n\t"
                     MF "v[44:47], " F0 ", %[w0], 0\n\tv_add_f32 v43, v53, v55\n\t"
                     "v_or_b32_dpp v41, v52, v41 quad_perm:[0,1,2,3] row_mask:0xf bank_mask:0xc\n\t"
                     MF "v[48:51], " F0 ", %[w1], 0\n\tv_add_f32_dpp v42, v43, v43" Q1 AND3("v52", "v53", "v54")
                     "s_waitcnt lgkmcnt(1)\n\t"
                     MF "v[44:47], " F1 ", %[w2], v[44:47]\n\tv_add_f32_dpp v42, v42, v42" Q2 AND3("%[t]", "v55", "v56")
                     MF "v[48:51], " F1 ", %[w3], v[48:51]\n\tv_add_f32_dpp v42, v42, v42" HM AND3("%[t]", "v57", "v58")
                     "s_waitcnt lgkmcnt(0)\n\t"
                     MF "v[44:47], " F2 ", %[w4], v[44:47]\n\t" MF "v[48:51], " F2 ", %[w5], v[48:51]\n\tv_rcp_f32 v42, v42\n\t"
                     AND3("%[t]", "v59", "v60") AND3("%[t]", "v61", "v62") AND3("%[t]", "1", "v63")
                     "v_cmp_gt_u32 vcc, v41, %[t]\n\ts_cbranch_vccnz 2b\n"
                     "3:\n\ts_mov_b32 exec_hi, 0\n\ts_cmp_eq_u32 %[wv], 0\n\t" PK(44, 46) PK(48, 50)
                     "v_add_f32 v44, v44, v45\n\tv_add_f32 v48, v48, v49\n\ts_nop 1\n\t"
                     "v_permlane16_swap_b32 v44, v48\n\tv_add_f32 v44, v44, v48\n\tv_mul_f32_dpp v44, v42, v44 row_newbcast:8" ALL
                     "ds_write_b32 %[rw], v44\n\ts_cselect_b64 exec, -1, 0\n\ts_waitcnt lgkmcnt(0)\n\ts_barrier\n\t"
                     "ds_read_b128 " F0 ", %[rr]\n\tds_read_b128 " F1 ", %[rr] offset:16\n\tds_read_b128 " F2 ", %[rr] offset:32\n\t"
                     "ds_read_b128 v[44:47], %[rr] offset:48\n\ts_cbranch_execz 9f\n\t"
                     "s_waitcnt lgkmcnt(2)\n\t" PK(52, 54) PK(56, 58) PK(52, 56) "s_waitcnt lgkmcnt(1)\n\t" PK(52, 60) PK(52, 62)
                     "s_waitcnt lgkmcnt(0)\n\t" PK(52, 44) PK(52, 46)
                     CV "v52, v52, v53\n\tv_or_b32 v52, 1, v52\n\t"
                     "buffer_store_dword v52, %[ds], %[rq], 0 offen sc1\n\tbuffer_store_dword v52, %[ds], %[rq], s89 offen sc1\n"
                     "9:\n\ts_mov_b64 exec, -1"
                     : [t] "=&v"(t)
                     : [ra] "v"(ra), [rb] "v"(rb), [rc] "v"(rc), WIN6(wf), [vo] "v"(voff), [rq] "s"(rq), [land] "s"(land), [rw] "v"(rw),
                       [rr] "v"(rr), [ds] "v"(dst), [wv] "s"(wave), [sd] "n"(SIDE)
                     : V40, V52, S80);
      }
      tref = now();
      if (half == 0) {
        if (wave >= 1 && wave < 4) {
          wait_until(tref + T_W2);
#pragma unroll
          for (int p = 0; p < 4; ++p) dma_piece(w2s, D, smem, L_W2, (wave - 1) * 4 + ((j & W2R) ? 3 - p : p), fresh6(lane0));
          asm volatile("s_waitcnt vmcnt(0)" ::: "memory");
          if (lane0 == 0) lds_add(smem, L_CNT + 12, 1u);
        }
        if (wave == 0) {
          wait_until(tref + 24 * T_LAZ0);
          x_request(rq, fresh6(lane0), gy + gy_row(c), land);
          x_update<true>(smem, rq, fresh6(lane0), gy + gy_row(c), land, c);
        }
      } else if (g == 0 && wave == 2) {
        const int lane = fresh6(lane0);
        const int col = j * 32 + (lane & 7) * 4 + ((lane >> 4) & 1) * 2 + (lane >> 5);
        u32 x0v, dummy;
        u16* const po = karg<64>() + col;
        asm volatile("global_load_ushort %0, %2, off\n\tglobal_load_ushort %1, %3, off\n\ts_waitcnt vmcnt(0)"
                     : "=&v"(x0v), "=&v"(dummy)
                     : "v"(karg<0>() + col), "v"(po)
                     : "memory");
        wait_until(tref + 24 * T_LAZY);
        const u32 o = gy + gy_chunk(4 * j + ((lane >> 1) & 3)) + (lane >> 3) * 256 + (lane & 1) * 8;
        const u32 off[3] = {o - GY_PAIR, o - GY_PAIR + GY_INST, o};
        float y[3];
        col_poll<3>(rq, off, y);
        float x3 = bf16r(bf16r(bf16r(bflo(x0v) + y[0]) + y[1]) + y[2]);
        const u32 off2 = fresh(o + GY_INST);
        asm volatile("" : "+v"(x3));
        wait_tt(A_F);
        u32 t;
        asm volatile("s_mov_b32 s88, 0x10000\n"
                     "1:\n\tbuffer_load_dwordx2 v[40:41], %[of], %[rq], 0 offen sc1\n\ts_sub_u32 s88, s88, 1\n\ts_waitcnt vmcnt(0)\n\t"
                     "v_lshlrev_b32 v42, 16, v40\n\tv_and_b32 v43, 0xffff0000, v40\n\t"
                     "v_lshlrev_b32 v44, 16, v41\n\tv_and_b32 v45, 0xffff0000, v41\n\t"
                     "v_permlane32_swap_b32 v42, v43\n\t" AND3("v40", "1", "v41") "v_permlane32_swap_b32 v44, v45\n\t"
                     "v_add_f32 v42, v42, v43\n\tv_add_f32 v44, v44, v45\n\tv_cmp_eq_u32 vcc, 0, %[t]\n\ts_cselect_b64 vcc, 0, vcc\n\t"
                     "v_permlane16_swap_b32 v42, v44\n\tv_add_f32 v42, v42, v44\n\ts_cbranch_vccnz 1b\n\ts_nop 0\n\t"
                     "v_add_f32_dpp v42, v42, v42" R8 CV "v42, 0, v42\n\tv_add_f32 v42, %[x], v42\n\t" CV "v42, v42, v42\n\t"
                     "global_store_short %[po], v42, off"
                     : [t] "=&v"(t)
                     : [of] "v"(off2), [rq] "s"(rq), [x] "v"(x3), [po] "v"(po)
                     : V40, "s88", "vcc", "scc", "memory");
      }
      if (wave >= 6) {
        const int lane = fresh6(lane0), wv = wave - 6;
        const int n = lane & 15, G = lane >> 4;
        const int l0 = fresh(L_W2 + wlane(lane, wv));
        const int l48 = lane < 48 ? lane : 0;
        const u32 off = fresh(gh + (u32)l48 * 16);
        const int aw = fresh(L_ACT + wv * 384 + l48 * 8);
        const int ab = fresh(L_ACT + wv * 384 + 16 * G);
        const u32 dst = fresh((lane & (half ? 49 : 1)) ? OOB : gyw + GY_INST + gy_rep(G) + gy_chunk(4 * j + wv * 2 + (n >> 3)) + (n & 7) * 2 + g * 256);
        if (half) {
          wait_tt(A_H);
        } else {
          wait_tt<L_AN0>(B_H - B_HC);
          wait_until(tref + 24 * T_HL);
        }
        u32 t;
        u32x4 pa, pb;
#define HLD "buffer_load_dwordx4 v[40:43], %[of], %[rq], 0 offen sc1\n\tbuffer_load_dwordx4 v[44:47], %[of], %[rq], s90 offen sc1\n"
        asm volatile("s_movk_i32 s90, 0x4000\n\tv_mov_b32 v48, 0\n\t" HLD
                     "1:\n\tds_read_b32 %[t], v48 offset:%[c]\n\ts_waitcnt lgkmcnt(0)\n\tv_cmp_ne_u32 vcc, 3, %[t]\n\ts_cbranch_vccnz 1b"
                     : "=&{v[40:43]}"(pa), "=&{v[44:47]}"(pb), [t] "=&v"(t)
                     : [of] "v"(off), [rq] "s"(rq), [c] "n"(L_CNT + 12)
                     : "v48", "s90", "vcc", "memory");
        u32x4 wf[6];
#pragma unroll
        for (int k = 0; k < 6; ++k) wf[k] = wfrag(smem, l0, 2048 * k);
#define HK(W, a, w, c) "s_waitcnt lgkmcnt(" #W ")\n\t" MF "v[36:39], v[" #a ":" #a "+3], %[" #w "], " c "\n\t"
        asm volatile("s_mov_b32 s88, 0x10000\n\ts_movk_i32 s89, %[sd]\n\ts_movk_i32 s90, 0x4000\n\ts_branch 2f\n"
                     "1:\n\ts_waitcnt lgkmcnt(0)\n\t" HLD "2:\n\ts_sub_u32 s88, s88, 1\n\ts_waitcnt vmcnt(0)\n\t"
                     "v_pk_mul_f32 v[48:49], v[40:41], v[44:45]\n\tv_pk_mul_f32 v[50:51], v[42:43], v[46:47]\n\t"
                     CV "v48, v48, v49\n\t" CV "v49, v50, v51\n\tds_write_b64 %[aw], v[48:49]\n\t"
                     "ds_read_b128 v[48:51], %[ab]\n\tds_read_b128 v[52:55], %[ab] offset:64\n\t"
                     "ds_read_b128 v[56:59], %[ab] offset:128\n\tds_read_b128 v[60:63], %[ab] offset:192\n\t"
                     AND3("v40", "v41", "v42") AND3("%[t]", "v43", "v44") AND3("%[t]", "v45", "v46") AND3("%[t]", "1", "v47")
                     "ds_read_b128 v[40:43], %[ab] offset:256\n\tds_read_b128 v[44:47], %[ab] offset:320\n\t"
                     "v_cmp_eq_u32 vcc, 0, %[t]\n\ts_cselect_b64 vcc, 0, vcc\n\ts_cbranch_vccnz 1b\n\t"
                     HK(5, 48, w0, "0") HK(4, 52, w1, "v[36:39]") HK(3, 56, w2, "v[36:39]") HK(2, 60, w3, "v[36:39]")
                     HK(1, 40, w4, "v[36:39]") HK(0, 44, w5, "v[36:39]")
                     "s_nop 7\n\tv_mov_b32_dpp v37, v36" Q1 CV "v36, v36, v37\n\tv_or_b32 v36, 1, v36\n\t"
                     "buffer_store_dword v36, %[ds], %[rq], 0 offen sc1\n\tbuffer_store_dword v36, %[ds], %[rq], s89 offen sc1"
                     : [t] "=&v"(t), "+{v[40:43]}"(pa), "+{v[44:47]}"(pb)
                     : WIN6(wf), [of] "v"(off), [rq] "s"(rq), [aw] "v"(aw), [ab] "v"(ab), [ds] "v"(dst), [sd] "n"(SIDE)
                     : V36, "v48", "v49", "v50", "v51", V52, "s88", "s89", "s90", "vcc", "scc", "memory");
      }
    } else {
      const int lane = fresh6(lane0);
      const int n = lane & 15, G = lane >> 4;
      if (half == 1) {
        x_update<true>(smem, rq, lane, gy - GY_PAIR + GY_INST + gy_row(wave), land + L_LAND2, wave);
      } else {
        int wn = nin + NPF;
#pragma unroll 1
        for (u32 tw = tref + 24 * T_FW; wn != 0; tw += 24 * T_FD) {
          wait_until(tw);
          WNEXT
        }
      }
      G80 a;
      g80_addr(a, lane, L_WB + wave * 5120, L_HB + 160 * wave, land, L_ONE);
      const u32 voff = fresh(gy + gy_row(wave) + ((lane * 16) ^ ((lane >> 4) * 64)));
      const int rw = fresh(L_RED + n * 144 + wave * 16 + G * 4);
      const int sw_ = fresh(n == 8 ? L_ST + wave * 16 + G * 4 : L_ACT + lane * 4);
      const int rr = fresh(L_RED + (lane & 31) * 144 + (lane >> 5) * 64), rs_ = fresh(L_ST + 16 * (lane & 7));
      const u32 dst = fresh(lane < 32 ? gh + fslot + lane * 4 : OOB);
      if (wave == 7) {
        if (half)
          wait_tt(A_Y);
        else
          wait_tt<L_AN0>(B_Y);
      }
      if (wave == 6 && half == 0) wait_until(tref + 24 * T_YL);
      asm volatile(POLL_FIRST "s_waitcnt vmcnt(2)"
                   :
                   : [vo] "v"(voff), [rq] "s"(rq), [land] "s"(land)
                   : "memory");
      g80_load(a, smem);
      u32 t;
#define SH4 " row_shr:4" ALL
      asm volatile(USED10 "s_mov_b32 s89, 0x810000\n\t" POLL_HEAD "s_branch 1f\n2:\n\t" POLL_MASK POLL_LOAD "\n1:\n\t" POLL_READ
                   "s_waitcnt lgkmcnt(2)\n\t"
                   MF "v[40:43], " F0 ", " F0 ", 0\n\t" AND3("v52", "v53", "v54")
                   MF "v[44:47], " F0 ", %[w0], 0\n\t" MF "v[48:51], " F0 ", %[w1], 0\n\t"
                   "s_waitcnt lgkmcnt(1)\n\t"
                   MF "v[40:43], " F1 ", " F1 ", v[40:43]\n\t" AND3("%[t]", "v55", "v56") AND3("%[t]", "v57", "v58")
                   "s_waitcnt lgkmcnt(0)\n\t"
                   MF "v[40:43], " F2 ", " F2 ", v[40:43]\n\t" AND3("%[t]", "v59", "v60") AND3("%[t]", "v61", "v62")
                   MF "v[44:47], " F1 ", %[w2], v[44:47]\n\t" AND3("%[t]", "1", "v63") "v_cmp_eq_u32 vcc, 0, %[t]\n\t"
                   MF "v[48:51], " F1 ", %[w3], v[48:51]\n\ts_and_b64 vcc, vcc, s[80:81]\n\ts_cbranch_scc1 2b\n"
                   "3:\n\t" MF "v[44:47], " F2 ", %[w4], v[44:47]\n\t" PK(40, 42) "v_add_f32 v40, v40, v41\n\t"
                   MF "v[48:51], " F2 ", %[w5], v[48:51]\n\ts_cmp_eq_u32 %[wv], 0\n\t"
                   "v_add_f32_dpp v40, v40, v40" Q1 "s_nop 1\n\tv_add_f32_dpp v40, v40, v40" Q2 PK(44, 46)
                   "v_add_f32 v44, v44, v45\n\tv_add_f32_dpp v41, v40, v40" SH4 PK(48, 50) "v_add_f32 v48, v48, v49\n\t"
                   "v_add_f32_dpp v40, v41, v40" SH4
                   "ds_write2st64_b32 %[rw], v44, v48 offset1:9\n\tds_write_b32 %[sw], v40\n\t"
                   "s_cselect_b64 exec, -1, 0\n\ts_bitcmp1_b32 %[j], 0\n\ts_waitcnt lgkmcnt(0)\n\ts_barrier\n\t"
                   "ds_read_b128 v[40:43], %[rs]\n\tds_read_b128 " F0 ", %[rr]\n\tds_read_b128 " F1 ", %[rr] offset:16\n\t"
                   "ds_read_b128 " F2 ", %[rr] offset:32\n\tds_read_b128 v[44:47], %[rr] offset:48\n\ts_cbranch_execz 9f\n\t"
                   "s_waitcnt lgkmcnt(4)\n\tv_add_f32 v40, v40, v41\n\tv_add_f32 v42, v40, v42\n\tv_add_f32_dpp v43, v43, v43" Q1
                   "s_waitcnt lgkmcnt(2)\n\tv_add_f32_dpp v42, v42, v42" Q1 "v_add_f32_dpp v43, v43, v43" Q2 PK(52, 56)
                   "v_add_f32_dpp v42, v42, v42" Q2 "v_add_f32_dpp v43, v43, v43" HM PK(54, 58) "v_add_f32_dpp v42, v42, v42" HM
                   "v_mul_f32 v43, 0x3acccccd, v43\n\tv_mul_f32 v42, 0x3acccccd, v42\n\tv_fma_f32 v42, -v43, v43, v42\n\t"
                   "s_waitcnt lgkmcnt(0)\n\t" PK(52, 60) PK(54, 62) PK(52, 44) PK(54, 46)
                   CV "v43, 0, v43\n\tv_add_f32 v52, v52, v53\n\tv_fma_f32 v54, -v43, v55, v54\n\t"
                   "v_add_f32 v53, v52, v54\n\tv_add_f32 v52, v52, v54\n\t"
                   CV "v42, 0, |v42|\n\tv_add_f32 v42, 0x3727c5ac, v42\n\t" CV "v42, 0, v42\n\tv_sqrt_f32 v42, v42\n\t"
                   "v_permlane32_swap_b32 v52, v53\n\t" CV "v42, 0, v42\n\tv_rcp_f32 v42, v42\n\tv_add_f32 v52, v52, v53\n\t"
                   "v_mul_f32 v52, v52, v42\n\t" CV "v53, 0, v52\n\tv_mul_f32 v54, 0xbfb8aa3b, v53\n\tv_exp_f32 v54, v54\n\t"
                   "s_cbranch_scc1 8f\n\tv_add_f32 v54, 1.0, v54\n\tv_rcp_f32 v54, v54\n\ts_nop 0\n\tv_mul_f32 v52, v53, v54\n"
                   "8:\n\t" CV "v52, s89, v52\n\tbuffer_store_dword v52, %[ds], %[rq], 0 offen sc1\n"
                   "9:\n\ts_mov_b64 exec, -1"
                   : [t] "=&v"(t)
                   : [ra] "v"(a.ra), [rb] "v"(a.rb), [rc] "v"(a.rc), WIN6(a.w), [vo] "v"(voff), [rq] "s"(rq), [land] "s"(land),
                     [rw] "v"(rw), [sw] "v"(sw_), [rr] "v"(rr), [rs] "v"(rs_), [ds] "v"(dst), [wv] "s"(wave), [j] "s"(j)
                   : V40, V52, S80);
      tref = now();
      if (half == 0 && wave == 1 && j == 20 && lane0 < 8) {
        const u32 nw = rtnow() * 8u, an = *(volatile LAS u32*)(smem + L_AN0);
        const u32 sv = (int)(an + B_H - B_T - nw) >= 0 ? an + B_E : nw + D_E;
        __builtin_amdgcn_raw_buffer_store_b32(((sv & 0xffffffu) << 1) | *(volatile LAS u32*)(smem + L_CLK), rs, gy_rep(fresh6(lane0)) + st_line(g), 0, 16);
      }
#define GET_REF                                                                                \
  u32 te = __builtin_amdgcn_readfirstlane(*(volatile LAS u32*)(smem + L_AN0)) + (B_H - B_A);  \
  const u32 so = fresh(ar + st_line(fresh6(lane0) & 7));                                       \
  pace(te, B_E - (B_H - B_A));                                                                 \
  ctl_load<false>(rq, so, L_PF);                                                               \
  if (stamp_poll(smem, rq, so, L_PF, te) != te) wait_tt(0u);
      if (half == 0 && wave == 7) {
        GET_REF
      } else if (half == 0 && wave != 0) {
#pragma unroll 1
        for (int i = wave;; i = (i + 1) & 7) {
          x_update<false>(smem, rq, fresh6(lane0), 0u, L_LAND + i * 2048, i);
          if (i < 6) break;
        }
      }
    }
    if (half == 1) break;
    {
      if (wave < 7) {
        const u32 gq = ar + A_GQ + hsub * 16384 + c * 896 + wave * 128;
        G80 a;
        u32 t;
        if (wave == 6) {
          const int lane = fresh6(lane0);
          const int n = lane & 15;
          const u32 voff = fresh(gy + GY_INST + gy_row(c) + ((lane * 16) ^ ((lane >> 4) * 64)));
          asm volatile(POLL_FIRST : : [vo] "v"(voff), [rq] "s"(rq), [land] "s"(land) : "memory");
          g80_addr(a, lane, 0, L_HB + 160 * c, land, L_ONE);
          const u32 dst = fresh((gq + (lane >> 4) * 4) | (u32)((n ^ 8) + 15) >> 4 << 31);
          asm volatile(USED10 POLL_HEAD "s_branch 1f\n"
                       "2:\n\t" POLL_MASK POLL_LOAD "\n1:\n\t" POLL_READ
                       "s_waitcnt lgkmcnt(2)\n\t" MF "v[44:47], " F0 ", " F0 ", 0\n\t" AND3("v52", "v53", "v54")
                       "s_waitcnt lgkmcnt(1)\n\t" MF "v[44:47], " F1 ", " F1 ", v[44:47]\n\t" AND3("%[t]", "v55", "v56") AND3("%[t]", "v57", "v58")
                       "s_waitcnt lgkmcnt(0)\n\t" MF "v[44:47], " F2 ", " F2 ", v[44:47]\n\t" AND3("%[t]", "v59", "v60")
                       AND3("%[t]", "v61", "v62") AND3("%[t]", "1", "v63")
                       "v_cmp_eq_u32 vcc, 0, %[t]\n\ts_and_b64 vcc, vcc, s[80:81]\n\ts_cbranch_scc1 2b\n3:\n\ts_nop 1\n\t"
                       PK(44, 46) "v_add_f32 v44, v44, v45\n\ts_nop 1\n\tv_add_f32_dpp v44, v44, v44" Q1
                       "s_nop 1\n\tv_add_f32_dpp v44, v44, v44" Q2 "s_nop 1\n\tv_add_f32_dpp v45, v44, v44" SH4
                       "s_nop 1\n\tv_add_f32_dpp v44, v45, v44" SH4 "v_or_b32 v44, 1, v44\n\t"
                       "buffer_store_dword v44, %[ds], %[rq], 0 offen sc1"
                       : [t] "=&v"(t)
                       : [ra] "v"(a.ra), [rb] "v"(a.rb), [rc] "v"(a.rc), [vo] "v"(voff), [rq] "s"(rq), [land] "s"(land), [ds] "v"(dst)
                       : V40, V52, S80);
        } else {
        const int lane = fresh6(lane0);
        const u32 voff = fresh(gy + GY_INST + gy_row(c) + ((lane * 16) ^ ((lane >> 4) * 64)));
        g80_addr(a, lane, L_WQ + wave * 5120, L_HB + 160 * c, land, L_ZERO);
        g80_load(a, smem);
        const u32 dst = fresh((gq + lane * 4) | (u32)lane >> 5 << 31);
        if (wave == 4) {
          wait_tt<L_AN0>(B_E);
        }
        if (wave == 5 && isO) {
          wait_until(tref + 24 * T_AO);
          GET_REF
        }
        asm volatile(POLL_FIRST USED10 POLL_HEAD "s_branch 1f\n"
                     "2:\n\t" POLL_MASK POLL_LOAD "\n1:\n\t" POLL_READ
                     "s_waitcnt lgkmcnt(2)\n\t"
                     MF "v[44:47], " F0 ", %[w0], 0\n\t" AND3("v52", "v53", "v54") MF "v[48:51], " F0 ", %[w1], 0\n\t"
                     "s_waitcnt lgkmcnt(1)\n\t"
                     MF "v[44:47], " F1 ", %[w2], v[44:47]\n\t" AND3("%[t]", "v55", "v56") AND3("%[t]", "v57", "v58")
                     MF "v[48:51], " F1 ", %[w3], v[48:51]\n\t"
                     "s_waitcnt lgkmcnt(0)\n\t"
                     MF "v[44:47], " F2 ", %[w4], v[44:47]\n\t" MF "v[48:51], " F2 ", %[w5], v[48:51]\n\t"
                     AND3("%[t]", "v59", "v60") AND3("%[t]", "v61", "v62") AND3("%[t]", "1", "v63")
                     "v_cmp_eq_u32 vcc, 0, %[t]\n\ts_and_b64 vcc, vcc, s[80:81]\n\ts_cbranch_scc1 2b\n"
                     "3:\n\ts_nop 0\n\t" PK(44, 46) PK(48, 50) "v_add_f32 v44, v44, v45\n\tv_add_f32 v48, v48, v49\n\ts_nop 1\n\t"
                     "v_permlane16_swap_b32 v44, v48\n\tv_add_f32 v45, v44, v48\n\tv_add_f32 v44, v44, v48\n\ts_nop 1\n\t"
                     "v_permlane32_swap_b32 v44, v45\n\tv_add_f32 v44, v44, v45\n\tv_or_b32 v44, 1, v44\n\t"
                     "buffer_store_dword v44, %[ds], %[rq], 0 offen sc1"
                     : [t] "=&v"(t)
                     : [ra] "v"(a.ra), [rb] "v"(a.rb), [rc] "v"(a.rc), WIN6(a.w), [vo] "v"(voff), [rq] "s"(rq), [land] "s"(land),
                       [ds] "v"(dst)
                     : V40, V52, S80);
        }
      } else {
        bar();
      }
      if (!isO) x_request(rq, fresh6(lane0), gy + GY_INST + gy_row(wave), land + L_LAND2);
    }
  }
#undef RS
#define RS(n) "s_memrealtime s[" #n ":" #n "+1]\n\ts_nop 1\n\t"
#define RD(n) "s_sub_i32 s80, s80, s" #n "\n\t"
  asm volatile("s_branch 9f\n.Lmk_wait:\n\t"
               "ds_read_b64 v[62:63], v62\n\ts_waitcnt lgkmcnt(0)\n\t"
               "v_readfirstlane_b32 s81, v63\n\tv_readfirstlane_b32 s82, v62\n\t"
               "s_add_i32 s82, s82, s80\n\ts_mul_i32 s80, s80, s81\n\ts_lshr_b32 s80, s80, 9\n\ts_add_i32 s80, s80, s82\n\t"
               "s_mul_i32 s81, s81, -3\n\ts_addk_i32 s81, 1549\n"
               "1:\n\ts_memrealtime s[98:99]\n\ts_waitcnt lgkmcnt(0)\n\ts_lshl_b32 s98, s98, 3\n\ts_sub_i32 s98, s80, s98\n\t"
               "s_cmpk_gt_i32 s98, 8*20\n\ts_cbranch_scc1 1b\n\ts_cmpk_lt_i32 s98, 8*9\n\ts_cbranch_scc1 3f\n\t"
               RS(84) RS(86) RS(88) RS(90) RS(92) RS(94) RS(96) "s_memrealtime s[98:99]\n\t"
               "s_getpc_b64 s[82:83]\n2:\n\ts_add_u32 s82, s82, 3f-2b\n\ts_addc_u32 s83, s83, 0\n\ts_waitcnt lgkmcnt(0)\n\t"
               RD(84) RD(86) RD(88) RD(90) RD(92) RD(94) RD(96) RD(98)
               "s_mul_i32 s80, s80, s81\n\ts_ashr_i32 s80, s80, 10\n\ts_sub_i32 s80, s80, 100\n\t"
               "s_max_i32 s80, s80, 0\n\ts_min_i32 s80, s80, 4*26\n\ts_andn2_b32 s80, s80, 3\n\t"
               "s_sub_u32 s82, s82, s80\n\ts_subb_u32 s83, s83, 0\n\ts_setpc_b64 s[82:83]\n\t"
               ".rept 26\n\ts_nop 1\n\t.endr\n3:\n\ts_setpc_b64 s[78:79]\n9:"
               :);
  asm volatile(".Lmk_code_end:");
}

}

extern "C" void kernel_launch(void* const* d_in, const int* in_sizes, int n_in, void* d_out, int out_size, void* d_ws,
                              size_t ws_size, hipStream_t stream) {
  Params P;
  P.x0 = (const u16*)d_in[0];
  P.kc = (const u16*)d_in[1];
  P.vc = (const u16*)d_in[2];
  P.wq = (const u16*)d_in[3];
  P.wo = (const u16*)d_in[4];
  P.w1 = (const u16*)d_in[6];
  P.w3 = (const u16*)d_in[7];
  P.w2 = (const u16*)d_in[8];
  P.out = (u16*)d_out;
  P.ws = (u32*)d_ws;
  mega_kernel<<<NWG, NT, 0, stream>>>(P);
}
```

```cpp
#include <hip/hip_runtime.h>
#include <hip/hip_bf16.h>
#include <stdint.h>

namespace {

typedef unsigned int u32;
typedef unsigned short u16;
typedef unsigned long long u64;
using u32x2 = __attribute__((ext_vector_type(2))) unsigned;
using u32x4 = __attribute__((ext_vector_type(4))) unsigned;
using f32x2 = __attribute__((ext_vector_type(2))) float;
using f32x4 = __attribute__((ext_vector_type(4))) float;
using s16x4 = __attribute__((ext_vector_type(4))) short;
using bf16x2 = __attribute__((ext_vector_type(2))) __bf16;
using bf16x8 = __attribute__((ext_vector_type(8))) __bf16;
#define LAS __attribute__((address_space(3)))

constexpr int D = 640, H = 32, DH = 192, WIN = 4096, SK = 32768, F = 1536;
constexpr int NWG = 256, NT = 512;
constexpr u32 SPIN_CAP = 1u << 16;
constexpr float SCL = 0.07216878364870322f * 1.4426950408889634f;

#define T_AO 70
#define T_LAZ0 40
#define B_O 975
#define B_Y 1630
#define B_H 2295
#define CLKO 510.5f
#define T_W2 0
#define W2R 1
#define T_OL 20
#define T_YL 100
#define T_HL 90
#define B_HC 30
#define B_A -185
#define B_E 2772
#define B_T 35
#define PTHR 60
#define T_LAZY 60

#define D_E 800
#define D_Q 415
#define T_O2 100
#define D_Y 1482
#define D_H 575
#define D_F 366
#define A_Q D_Q
#define A_Y (A_Q + D_Y)
#define A_H (A_Y + D_H)
#define A_F (A_H + D_F)

constexpr u32 SIDE = 8192, AREA = 16 * 16384;
constexpr u32 A_GO = 0;
constexpr u32 A_GQ = 4 * 16384;
constexpr u32 A_GH = A_GQ + 7168;
constexpr u32 A_GY = 8 * 16384;
constexpr u32 GO_SZ = 32768, GY_PAIR = 65536, GY_INST = 32768;
constexpr u32 WS_CLR = 4 * AREA;
constexpr u32 WS_PB = WS_CLR;
#define HBS 4352
constexpr u32 WS_GOF = WS_PB + (128 << 10), WS_HB = WS_GOF + 8 * HBS;
__device__ __forceinline__ u32 rp_line(int e, int r, int w) { return WS_PB + (u32)((e * 8 + r) * 8 + w) * 384u; }
#define Q1S 2048
constexpr u32 WS_GQ1 = WS_GOF + (192 << 10);
constexpr u32 WS_END = WS_GQ1 + 256 * 2048;
static_assert(WS_END <= (2u << 20) && WS_CLR == 256 * 4096, "layout");
__device__ __forceinline__ u32 go_slot(int w) { return (u32)(w >> 2) * 16384u + (u32)(w & 3) * 2048u; }
__device__ __forceinline__ u32 gy_row(int i) { return go_slot(i); }
__device__ __forceinline__ u32 gy_chunk(int u) {
  const u32 i = __umul24(u, 205) >> 11;
  return (i >> 2) * 8192u + __umul24(i, 2048 - 160) + (u32)u * 16u;
}
__device__ __forceinline__ u32 st_line(int g) { return A_GH + 768u + (u32)(g >> 1) * 16384u + (u32)(g & 1) * 128u; }
__device__ __forceinline__ u32 gy_rep(int r) { return (u32)(r & 3) * AREA + (u32)(r >> 2) * SIDE; }

constexpr int L_ZERO = 0;
constexpr int L_CNT = 16;
constexpr int L_ONE = 32;
constexpr int L_TAG = 48;
constexpr int L_ST = 64;
constexpr int L_VOTE = L_ST;
constexpr int L_ANC = 192;
constexpr int L_CLK = 200;
constexpr int L_SUM = 208;
constexpr int L_AN0 = 216;
constexpr int L_AR = 224;
constexpr int L_Q = 256;
constexpr int L_CS = 640;
constexpr int L_HB = L_CS + 768;
constexpr int L_ACT = L_HB + D * 2;
constexpr int L_RED = L_ACT + 2 * 384;
constexpr int L_P = L_RED + 32 * 144;
constexpr int P_W = 1280, P_U = 592, P_PAR = 288;
constexpr int OM_S = 416;
constexpr int L_OM = L_P + 8 * P_W;
constexpr int L_LN = L_OM;
constexpr int L_LAND = (L_OM + 24 * OM_S + 512 + 255) / 256 * 256;
constexpr int L_WQ = L_LAND + 8 * 2048;
constexpr int L_WB = L_WQ + 6 * 5120;
constexpr int L_W2 = L_WB + 768 * 64;
constexpr int L_LAND2 = L_WB + 640 * 64 - L_LAND;
constexpr int L_PF = L_W2 + 192 * 64;
constexpr int L_TAB = L_PF + 256;
constexpr int L_TOTAL = L_TAB + 8 * 2048;
static_assert(L_LAND2 % 256 == 0 && L_LAND % 256 == 0 && L_WB % 64 == 0 && L_P % 16 == 0 && L_WQ % 16 == 0 && L_HB % 16 == 0 && L_RED % 16 == 0, "alignment");

struct Params {
  const u16* x0;
  const u16* kc;
  const u16* vc;
  const u16* wq;
  const u16* wo;
  const u16* w1;
  const u16* w3;
  const u16* w2;
  u16* out;
  u32* ws;
};

__device__ __forceinline__ u32 cvt2(float lo, float hi) {
  return __builtin_bit_cast(u32, __builtin_convertvector(f32x2{lo, hi}, bf16x2));
}
__device__ __forceinline__ float bflo(u32 w) { return __uint_as_float(w << 16); }
__device__ __forceinline__ float bfhi(u32 w) { return __uint_as_float(w & 0xffff0000u); }
__device__ __forceinline__ float bf16r(float x) { return bflo(cvt2(x, x)); }

template <int CTRL>
__device__ __forceinline__ float dpp(float x) {
  return __builtin_bit_cast(float, __builtin_amdgcn_mov_dpp(__builtin_bit_cast(int, x), CTRL, 0xf, 0xf, true));
}
constexpr int XOR1 = 0xB1, XOR2 = 0x4E, XOR7 = 0x141, ROR8 = 0x128;
__device__ __forceinline__ float fmx(float a, float b) {
  float r;
  asm("v_max_f32 %0, %1, %2" : "=v"(r) : "v"(a), "v"(b));
  return r;
}
__device__ __forceinline__ float xrow16_max(float x) {
  auto s = __builtin_amdgcn_permlane16_swap(__float_as_uint(x), __float_as_uint(x), false, false);
  x = fmx(__uint_as_float(s[0]), __uint_as_float(s[1]));
  auto t = __builtin_amdgcn_permlane32_swap(__float_as_uint(x), __float_as_uint(x), false, false);
  return fmx(__uint_as_float(t[0]), __uint_as_float(t[1]));
}
__device__ __forceinline__ float xrow16_sum(float x) {
  auto s = __builtin_amdgcn_permlane16_swap(__float_as_uint(x), __float_as_uint(x), false, false);
  x = __uint_as_float(s[0]) + __uint_as_float(s[1]);
  auto t = __builtin_amdgcn_permlane32_swap(__float_as_uint(x), __float_as_uint(x), false, false);
  return __uint_as_float(t[0]) + __uint_as_float(t[1]);
}
__device__ __forceinline__ float sum8(float v) {
  v += dpp<XOR1>(v);
  v += dpp<XOR2>(v);
  v += dpp<XOR7>(v);
  return v;
}
__device__ __forceinline__ float max8(float v) {
  asm("s_nop 1\n\tv_max_f32_dpp %0, %0, %0 quad_perm:[1,0,3,2] row_mask:0xf bank_mask:0xf\n\t"
      "s_nop 1\n\tv_max_f32_dpp %0, %0, %0 quad_perm:[2,3,0,1] row_mask:0xf bank_mask:0xf\n\t"
      "s_nop 1\n\tv_max_f32_dpp %0, %0, %0 row_half_mirror row_mask:0xf bank_mask:0xf"
      : "+v"(v));
  return v;
}
__device__ __forceinline__ float max_ror8(float v) {
  asm("s_nop 1\n\tv_max_f32_dpp %0, %0, %0 row_ror:8 row_mask:0xf bank_mask:0xf" : "+v"(v));
  return v;
}
__device__ __forceinline__ float wave_sum(float v) {
  v = sum8(v);
  v += dpp<ROR8>(v);
  return xrow16_sum(v);
}
__device__ __forceinline__ float tsum8(f32x4 a) {
  auto s0 = __builtin_amdgcn_permlane32_swap(__float_as_uint(a[0]), __float_as_uint(a[1]), false, false);
  auto s1 = __builtin_amdgcn_permlane32_swap(__float_as_uint(a[2]), __float_as_uint(a[3]), false, false);
  const float r0 = __uint_as_float(s0[0]) + __uint_as_float(s0[1]);
  const float r1 = __uint_as_float(s1[0]) + __uint_as_float(s1[1]);
  auto t = __builtin_amdgcn_permlane16_swap(__float_as_uint(r0), __float_as_uint(r1), false, false);
  float r = __uint_as_float(t[0]) + __uint_as_float(t[1]);
  r += dpp<ROR8>(r);
  return r;
}

__device__ __forceinline__ u32 now() { return (u32)__builtin_amdgcn_s_memtime(); }
__device__ __forceinline__ void wait_until(u32 t) {
#pragma unroll 1
  while ((int)(now() - t) < -160) __builtin_amdgcn_s_sleep(1);
#pragma unroll 1
  while ((int)(now() - t) < 0) {
  }
}
__device__ __forceinline__ u32 rtnow() { return (u32)__builtin_amdgcn_s_memrealtime(); }
template <int AN = L_ANC>
__device__ __forceinline__ void wait_tt(u32 a) {
  asm volatile("s_mov_b32 s80, %0\n\tv_mov_b32 v62, %1\n\ts_call_b64 s[78:79], .Lmk_wait"
               :
               : "is"(a), "n"(AN)
               : "s78", "s79", "s80", "s81", "s82", "s83", "s84", "s85", "s86", "s87", "s88", "s89", "s90", "s91", "s92", "s93", "s94",
                 "s95", "s96", "s97", "s98", "s99", "v62", "v63", "scc", "memory");
}
__device__ __forceinline__ void lds_wait(volatile LAS u32* p, u32 want) {
  u32 spins = 0;
#pragma unroll 1
  while (__builtin_amdgcn_readfirstlane(*p) != want) {
    if (++spins > (SPIN_CAP << 6)) break;
    __builtin_amdgcn_s_sleep(1);
  }
  asm volatile("" ::: "memory");
}
__device__ __forceinline__ void lds_add(unsigned char* smem, int off, u32 v) {
  __hip_atomic_fetch_add((LAS u32*)(smem + off), v, __ATOMIC_RELAXED, __HIP_MEMORY_SCOPE_WORKGROUP);
}

__device__ __forceinline__ int fresh(int t) {
  asm volatile("" : "+v"(t));
  return t;
}
__device__ __forceinline__ int fresh6(int t) {
  t = fresh(t);
  __builtin_assume((unsigned)t < 64u);
  return t;
}
__device__ __forceinline__ int lane_id() {
  int t;
  asm volatile("v_mbcnt_lo_u32_b32 %0, -1, 0\n\tv_mbcnt_hi_u32_b32 %0, -1, %0" : "=v"(t));
  __builtin_assume((unsigned)t < 64u);
  return t;
}
__device__ __forceinline__ f32x4 zero4() {
  f32x2 a, b;
  asm volatile("v_mov_b64 %0, 0\n\tv_mov_b64 %1, 0" : "=v"(a), "=v"(b));
  return f32x4{a[0], a[1], b[0], b[1]};
}
__device__ __forceinline__ void bar() { asm volatile("s_waitcnt lgkmcnt(0)\n\ts_barrier" ::: "memory"); }

__device__ __forceinline__ u32x2 trread(unsigned char* smem, int off) {
  return __builtin_bit_cast(u32x2, __builtin_amdgcn_ds_read_tr16_b64_v4i16((LAS s16x4*)(smem + off)));
}
__device__ __forceinline__ u32x4 wfrag(unsigned char* smem, int lb, int off) {
  const u32x2 a = trread(smem, lb + off), b = trread(smem, lb + off + 256);
  return u32x4{a[0], a[1], b[0], b[1]};
}
__device__ __forceinline__ int wlane(int lane, int cb) {
  const int i = lane & 15, G = lane >> 4;
  return (8 * G + (i >> 2)) * 64 + (i & 3) * 8 + ((cb ^ (G & 1)) * 32);
}
__device__ __forceinline__ u32x4 afrag(unsigned char* smem, int a) { return *reinterpret_cast<const u32x4*>(smem + a); }
__device__ __forceinline__ f32x4 mfma32(u32x4 w, u32x4 a, f32x4 c) {
  return __builtin_amdgcn_mfma_f32_16x16x32_bf16(__builtin_bit_cast(bf16x8, w), __builtin_bit_cast(bf16x8, a), c, 0, 0, 0);
}

__device__ __forceinline__ void dma_piece(const u16* src, int ld, unsigned char* smem, int ldsoff, int p, int lane) {
  const int row = 16 * p + (lane >> 2), part = lane & 3;
  const int cb = (part >> 1) ^ (lane >> 5);
  const u32 voff = (u32)(row * ld + cb * 16 + (part & 1) * 8) * 2u;
  const u32 dst = (u32)(uintptr_t)(LAS unsigned char*)smem + (u32)(ldsoff + p * 1024);
  u32 keep;
  asm volatile("s_mov_b32 %0, m0\n\ts_mov_b32 m0, %2\n\ts_nop 2\n\tglobal_load_lds_dwordx4 %1, %3 nt\n\ts_mov_b32 m0, %0"
               : "=&s"(keep)
               : "v"(voff), "s"(dst), "s"(src)
               : "memory");
}

__device__ __forceinline__ float sum4(f32x4 d) { return (d[0] + d[1]) + (d[2] + d[3]); }
template <int AUX = 16>
__device__ __forceinline__ void store16(__amdgpu_buffer_rsrc_t rs, u32 off, u32x4 v) {
  __builtin_amdgcn_raw_buffer_store_b128(v, rs, off, 0, AUX);
}

struct G80 {
  u32x4 w[6];
  int ra, rb, rc, l0, l2;
};
__device__ __forceinline__ void g80_load(G80& a, unsigned char* smem) {
  a.w[0] = wfrag(smem, a.l0, 0);
  a.w[1] = wfrag(smem, a.l0 ^ 32, 0);
  a.w[2] = wfrag(smem, a.l0, 2048);
  a.w[3] = wfrag(smem, a.l0 ^ 32, 2048);
  a.w[4] = wfrag(smem, a.l2, 0);
  a.w[5] = wfrag(smem, a.l2 ^ 32, 0);
}
__device__ __forceinline__ void g80_addr(G80& a, int lane, int wb, int xs, int land, int one) {
  const int m = lane & 15, G = lane >> 4;
  a.l0 = fresh(wb + wlane(lane, 0));
  a.l2 = fresh(a.l0 + 4096 - (G >> 1) * 1024);
  const bool vec = m <= 8;
  const int st = vec ? 64 : 0;
  const int sa = vec ? (m == 8 ? xs : land + 256 * m) + 16 * G : (m == 15 ? one : L_ZERO);
  const int sw = m < 8 ? (m & 3) * 64 : 0;
  a.ra = fresh(sa ^ sw);
  a.rb = fresh((sa + st) ^ sw);
  a.rc = fresh(G < 2 ? (sa + 2 * st) ^ sw : L_TAG);
}
template <bool X4, bool NT = false>
__device__ __forceinline__ void ctl_load(u32x4 rq, u32 voff, u32 lds) {
  u32 keep;
  if (NT)
    asm volatile("s_mov_b32 %0, m0\n\ts_mov_b32 m0, %3\n\ts_nop 2\n\tbuffer_load_dwordx4 %1, %2, 0 offen sc1 nt lds\n\ts_mov_b32 m0, %0"
                 : "=&s"(keep) : "v"(voff), "s"(rq), "s"(lds) : "memory");
  else if (X4)
    asm volatile("s_mov_b32 %0, m0\n\ts_mov_b32 m0, %3\n\ts_nop 2\n\tbuffer_load_dwordx4 %1, %2, 0 offen sc1 lds\n\ts_mov_b32 m0, %0"
                 : "=&s"(keep) : "v"(voff), "s"(rq), "s"(lds) : "memory");
  else
    asm volatile("s_mov_b32 %0, m0\n\ts_mov_b32 m0, %3\n\ts_nop 2\n\tbuffer_load_dword %1, %2, 0 offen sc1 lds\n\ts_mov_b32 m0, %0"
                 : "=&s"(keep) : "v"(voff), "s"(rq), "s"(lds) : "memory");
}
__device__ __forceinline__ u32 stamp_poll(unsigned char* smem, u32x4 rq, u32 so, u32 te) {
  u32 sv, budget = SPIN_CAP;
#pragma unroll 1
  for (;;) {
    asm volatile("buffer_load_dword %0, %1, %2, 0 offen sc1\n\ts_waitcnt vmcnt(0)" : "=&v"(sv) : "v"(so), "s"(rq) : "memory");
    if (__builtin_amdgcn_ballot_w64((sv & 1u) == 0u) == 0ull || budget == 0u) break;
    --budget;
    __builtin_amdgcn_s_sleep(1);
  }
  const u32 nw = te + 0x800000u;
  const float age = (float)((nw - (sv >> 1)) & 0xffffffu);
  const u32 an = nw - (u32)-max8(-age);
  *(volatile LAS u32x2*)(smem + L_ANC) = u32x2{an, (u32)max8((float)(sv >> 25))};
  return __builtin_amdgcn_readfirstlane(an);
}
#define MF "v_mfma_f32_16x16x32_bf16 "
#define F0 "v[52:55]"
#define F1 "v[56:59]"
#define F2 "v[60:63]"
#define V52 "v52", "v53", "v54", "v55", "v56", "v57", "v58", "v59", "v60", "v61", "v62", "v63"
#define V40 "v40", "v41", "v42", "v43", "v44", "v45", "v46", "v47", "v48", "v49", "v50", "v51"
#define V36 "v36", "v37", "v38", "v39"
#define V32 "v32", "v33", "v34", "v35"
#define S80 "s80", "s81", "s82", "s83", "s84", "s85", "s86", "s87", "s88", "s89", "s90", "s91", "vcc", "scc", "memory"
#define AND3(a, b, c) "v_bitop3_b32 %[t], " a ", " b ", " c " bitop3:0x80\n\t"
#define ALL " row_mask:0xf bank_mask:0xf\n\t"
#define Q1 " quad_perm:[1,0,3,2]" ALL
#define Q2 " quad_perm:[2,3,0,1]" ALL
#define HM " row_half_mirror" ALL
#define R8 " row_ror:8" ALL
#define PK(a, b) "v_pk_add_f32 v[" #a ":" #a "+1], v[" #a ":" #a "+1], v[" #b ":" #b "+1]\n\t"
#define CV "v_cvt_pk_bf16_f32 "
constexpr u32 OOB = 0x80000000u;
#define POLL_HEAD                                                                                                     \
  "s_mov_b32 m0, %[land]\n\ts_mov_b32 s88, 0x10000\n\ts_mov_b32 s80, 0xff00ff\n\ts_mov_b32 s81, s80\n\t"               \
  "s_mov_b64 s[84:85], s[82:83]\n\ts_mov_b64 s[86:87], s[82:83]\n\t"
#define POLL_MASK                                                                                                     \
  "s_sub_u32 s88, s88, 1\n\ts_cbranch_scc1 3f\n\ts_or_b32 s84, vcc_lo, vcc_hi\n\ts_lshr_b32 s85, s84, 16\n\t"          \
  "s_or_b32 s84, s84, s85\n\ts_lshr_b32 s85, s84, 8\n\ts_or_b32 s84, s84, s85\n\t"                                     \
  "s_bitreplicate_b64_b32 s[84:85], s84\n\ts_bitreplicate_b64_b32 s[84:85], s84\n\t"                                   \
  "s_bitreplicate_b64_b32 s[84:85], s84\n\ts_bitreplicate_b64_b32 s[86:87], s85\n\t"                                   \
  "s_bitreplicate_b64_b32 s[84:85], s84\n\ts_and_b64 s[84:85], s[84:85], s[82:83]\n\t"                                 \
  "s_and_b64 s[86:87], s[86:87], s[82:83]\n\t"
#define POLL_LOAD                                                                                                     \
  "s_mov_b64 exec, s[84:85]\n\tbuffer_load_dwordx4 %[vo], %[rq], 0 offen sc1 lds\n\t"                                  \
  "s_mov_b64 exec, s[86:87]\n\tbuffer_load_dwordx4 %[vo], %[rq], 0 offen offset:1024 sc1 lds\n\ts_mov_b64 exec, -1\n\t"
#define POLL_READ "s_waitcnt vmcnt(0)\n\t" POLL_RD
#define POLL_RD \
  "ds_read_b128 " F0 ", %[ra]\n\tds_read_b128 " F1 ", %[rb]\n\tds_read_b128 " F2 ", %[rc]\n\t"
#define USED10 "s_mov_b32 s82, 0x30ff03ff\n\ts_mov_b32 s83, 0xff30ff03\n\t"
#define POLL_FIRST                                                                                           \
  "s_mov_b32 m0, %[land]\n\ts_mov_b32 exec_lo, 0x30ff03ff\n\ts_mov_b32 exec_hi, 0xff30ff03\n\t"               \
  "s_waitcnt lgkmcnt(0)\n\ts_barrier\n\t"                                                                    \
  "buffer_load_dwordx4 %[vo], %[rq], 0 offen sc1 lds\n\tbuffer_load_dwordx4 %[vo], %[rq], 0 offen offset:1024 sc1 lds\n\t" \
  "s_mov_b64 exec, -1\n\t"
#define WIN6(a) [w0] "v"(a[0]), [w1] "v"(a[1]), [w2] "v"(a[2]), [w3] "v"(a[3]), [w4] "v"(a[4]), [w5] "v"(a[5])
__device__ __forceinline__ f32x4 quad4(float r) { return f32x4{r, dpp<0x55>(r), dpp<0xAA>(r), dpp<0xFF>(r)}; }

__device__ __forceinline__ int q_addr(int d) {
  const int w = d >> 1;
  return L_Q + ((w & 1) * 48 + (w >> 1)) * 4 + (d & 1) * 2;
}

__device__ __forceinline__ void q1_finish(unsigned char* smem, int lane, int wave) {
  const int e = wave * 32 + (lane & 7) * 4 + ((lane >> 4) & 1) * 2 + (lane >> 5);
  asm volatile("" ::: "memory");
  const u32x4 v = afrag(smem, L_LAND + wave * 2048 + lane * 16);
  const float t = tsum8(f32x4{bflo(v[0]), bflo(v[1]), bflo(v[2]), bflo(v[3])});
  *reinterpret_cast<u16*>(smem + q_addr(e)) = (u16)cvt2(t, t);
  *reinterpret_cast<float*>(smem + L_CS + e * 4) = tsum8(f32x4{bfhi(v[0]), bfhi(v[1]), bfhi(v[2]), bfhi(v[3])}) * (-SCL / D);
}
__device__ __forceinline__ void x_request(u32x4 rq, int lane, u32 src, int land) {
  asm volatile("s_mov_b32 m0, %2\n\ts_nop 0\n\tbuffer_load_dwordx4 %0, %1, 0 offen sc1 lds\n\t"
               "buffer_load_dwordx4 %0, %1, 0 offen offset:1024 sc1 lds"
               :
               : "v"(src + ((lane * 16) ^ ((lane >> 4) * 64))), "s"(rq), "s"(land)
               : "memory");
}
template <bool POLL>
__device__ __forceinline__ void x_update(unsigned char* smem, u32x4 rq, int lane, u32 src, int land, int i) {
  const int l = lane < 40 ? lane : 0;
  const u32 off = land + 4 * l;
  u32 v[8];
  u32 budget = SPIN_CAP;
#pragma unroll 1
  for (;;) {
    if (POLL) asm volatile("s_waitcnt vmcnt(0)" ::: "memory");
#pragma unroll
    for (int k = 0; k < 8; ++k) v[k] = *reinterpret_cast<const u32*>(smem + (off ^ ((k & 3) * 64)) + 256 * k);
    const u32 a = v[0] & v[1] & v[2] & v[3] & v[4] & v[5] & v[6] & v[7] & 1u;
    if (!POLL || __builtin_amdgcn_ballot_w64(a == 0u) == 0ull || budget == 0u) break;
    --budget;
    x_request(rq, lane, src, land);
  }
  float lo = 0.f, hi = 0.f;
#pragma unroll
  for (int k = 0; k < 8; ++k) {
    lo += bflo(v[k]);
    hi += bfhi(v[k]);
  }
  u32* px = reinterpret_cast<u32*>(smem + L_HB + 160 * i + 4 * l);
  const u32 xo = *px, y = cvt2(lo, hi);
  if (lane < 40) *px = cvt2(bflo(xo) + bflo(y), bfhi(xo) + bfhi(y));
}
template <int N>
__device__ __forceinline__ void col_poll(u32x4 rq, const u32 (&off)[N], float (&y)[N]) {
  u32x2 v[N];
  u32 budget = SPIN_CAP;
#pragma unroll 1
  for (;;) {
    u32 a = 1u;
#pragma unroll
    for (int k = 0; k < N; ++k)
      asm volatile("buffer_load_dwordx2 %0, %1, %2, 0 offen sc1" : "=v"(v[k]) : "v"(off[k]), "s"(rq) : "memory");
    asm volatile("s_waitcnt vmcnt(0)" ::: "memory");
#pragma unroll
    for (int k = 0; k < N; ++k) {
      asm volatile("" : "+v"(v[k]));
      a &= v[k][0] & v[k][1];
    }
    if (__builtin_amdgcn_ballot_w64(a == 0u) == 0ull || budget == 0u) break;
    --budget;
  }
#pragma unroll
  for (int k = 0; k < N; ++k) y[k] = bf16r(tsum8(f32x4{bflo(v[k][0]), bfhi(v[k][0]), bflo(v[k][1]), bfhi(v[k][1])}));
}

#define SJ 31
#define DWIN 250
#define DEQ 0.9f
#define PFB 16
#define AUXQ 18
#define UQ 1
#define UAD 4
#define UB 3
#define UL 5
#define UG 6
#define UK 7
#define UC 8
#define UW 8
#define UF 10
#define UX 11
#define TSTEP 240
#define TSV 236
#define USW 4
#define TSQ 192
#define WOFS 21
#define WBK 60
#define NINO 4
#define NINF 1
#define NINS 2
#define ORDO 0x543210u
#define ORDF 0x34210u
#define NPRE 2
#define NPF 2
#define TPF 220
#define T_FW 10
#define T_FD 12
#define WU0 3
#define WSP 2
#define WPC 100
#define NUN 1
static_assert(NINS + NPF < 5 && NINS <= NINO && NINO + NPRE == 6 && NINF + NPF < 5 && NPF == NPRE, "counts");
static_assert(WU0 + NINO * WSP <= UX + 1, "pieces before the scores");
#define TPV 220
#define TSE TSV
constexpr int wcnt(int u) { return u < WU0 || u >= WU0 + NINO * WSP ? 0 : (20 + ((u - WU0) / WSP < NINF ? 12 : ((u - WU0) / WSP < NINS ? 2 : 0))) / WSP; }
#define PQ(u) ((((u) < USW ? TSTEP : TSV) * (9600 + wcnt(u) * WPC)) / 12800)
#define ROW0 ((wave * 8 + c) * 8)
#define GST 512
#define ROT h
#define PACE_ASM                                                                                                     \
  "1:\n\ts_memrealtime s[98:99]\n\ts_waitcnt lgkmcnt(0)\n\ts_lshl_b32 s98, s98, 3\n\ts_sub_i32 s98, s98, %[tn]\n\t"  \
  "s_cmp_lt_i32 s98, 0\n\ts_cbranch_scc0 2f\n\ts_sleep 1\n\ts_branch 1b\n2:\n\ts_add_i32 %[tn], %[tn], %[ts]\n\t"
__device__ __forceinline__ void pace(u32& tnext, u32 tstep) {
  asm volatile(PACE_ASM : [tn] "+s"(tnext) : [ts] "s"(tstep) : "scc", "s98", "s99");
}
constexpr int kreg(int i) { return i % 3 < 2 ? 64 + 8 * (i / 3) + 4 * (i % 3) : 128 + 4 * (i / 3); }
constexpr int vreg(int i) { return 160 + 24 * (i / 6) + 8 * (i % 3) + 4 * ((i / 3) & 1); }
constexpr int treg(int t) { return t < 24 ? kreg(t) : vreg(t - 24); }
template <int V>
struct Cn {
  static constexpr int v = V;
};
template <int A, int B, class Fn>
__device__ __forceinline__ void slots(Fn& f) {
  if constexpr (A < B) {
    f(Cn<A>{});
    slots<A + 1, B>(f);
  }
}
#define PRT 200
#define STR2(x) #x
#define STR(x) STR2(x)
#define PRIO_ASM                                                                                                     \
  "s_setprio 0\n\ts_cmpk_lt_i32 s98, " STR(PRT) "\n\ts_cbranch_scc1 4f\n\ts_setprio 1\n\ts_cmpk_lt_i32 s98, 3*" STR(PRT) "\n\t"  \
  "s_cbranch_scc1 4f\n\ts_setprio 2\n\ts_cmpk_lt_i32 s98, 6*" STR(PRT) "\n\ts_cbranch_scc1 4f\n\ts_setprio 3\n4:\n\t"
#define LD1(r) "global_load_dwordx4 v[%[" #r "]:%[" #r "]+3], %[p], %[sb] offset:"
#define PACE_B PACE_ASM
#define BURST(u, voff, sbase)                                                                                       \
  asm volatile(PACE_B PRIO_ASM LD1(a) "0 nt\n\t" LD1(b) "128 nt\n\t" LD1(c) "256 nt\n\t"                                    \
               "s_waitcnt vmcnt(%[w]+2)\n\ts_cmp_gt_u32 %[ex], 1\n\ts_cbranch_scc1 3f\n\t"                          \
               "s_waitcnt vmcnt(%[w]+1)\n\ts_cmp_gt_u32 %[ex], 0\n\ts_cbranch_scc1 3f\n\ts_waitcnt vmcnt(%[w])\n3:"  \
               : [tn] "+s"(tnext)                                                                                   \
               : [a] "n"(treg(3 * (u))), [b] "n"(treg(3 * (u) + 1)), [c] "n"(treg(3 * (u) + 2)), [p] "v"(voff),     \
                 [sb] "s"(sbase), [ts] "s"(tstep), [ex] "s"(exa), [w] "n"(6)                                        \
               : "scc", "s96", "s97", "s98", "s99");                                                                              \
  exa = exl;                                                                                                        \
  exl = 0u
#define LOADW(t, voff, sbase)                                                                                       \
  asm volatile(PACE_ASM PRIO_ASM LD1(a) "%[im] nt\n\ts_waitcnt vmcnt(9)"                                                    \
               : [tn] "+s"(tnext)                                                                                   \
               : [a] "n"(treg(t)), [p] "v"(voff), [sb] "s"(sbase), [im] "n"(128 * ((t) % 3)), [ts] "s"(tstep)       \
               : "scc", "s98", "s99")

#define HS_WAIT(COND)                                                                                \
  _Pragma("unroll 1") for (;;) {                                                                     \
    gd = CTL[512 + lane0] - CTL[576 + lane0];                                                        \
    const u64 ch = __builtin_amdgcn_ballot_w64(gd != 0u) & 0x1ffull;                                 \
    if (ch > 0xffull || (COND) || tmo == 0u) break;                                                  \
    if ((tmo & 3u) == 0u && lane0 == 0 && cnt[1] >= 7u)                                              \
      __builtin_amdgcn_raw_buffer_store_b32(hb, rs, WS_HB + h * HBS + c * 4, 0, 16);                 \
    hb += 4u;                                                                                        \
    --tmo;                                                                                           \
    __builtin_amdgcn_s_sleep(3);                                                                     \
    ctl_load<false>(rq, hso, (u32)(smb + L_CTL + 2048));                                             \
    asm volatile("s_waitcnt vmcnt(0)" ::: "memory");                                                 \
  }

struct Att {
  int qad[6], idxq, wa, ra, idx, oa, sa, la, ma, qw;
  u32 dst;
};
template <int K>
__device__ __forceinline__ void att_table(unsigned char* smem, int lane, int wave, u32 go) {
  const int n = lane & 15, G = lane >> 4;
  const int seg = n & 1, pr = (n >> 1) & 1, tq = n >> 2, xy = G >> 1;
  const int qa = L_Q + (pr * 48 + seg * 8 + (G & 1) * 4) * 4;
  u32 t[16];
#pragma unroll
  for (int k = 0; k < 4; ++k) t[k] = tq == k ? qa + xy * 64 : L_ZERO;
#pragma unroll
  for (int k = 0; k < 2; ++k) t[4 + k] = tq == 2 * k + xy ? qa + 128 : L_ZERO;
  t[6] = 0x4444 + pr * 0xAAAA;
  t[7] = L_P + wave * P_W + (n & 1) * P_PAR + ((((G & 1) * 2 + ((n >> 2) & 1)) * 4 + (n >> 3) * 2 + (G >> 1)) * 16) + (n & 2) * 4;
  t[8] = L_P + wave * P_W + ((n >> 2) & 1) * P_U + (n >> 3) * P_PAR + G * 64;
  t[9] = (n & 3) * 0x5555;
  t[10] = L_OM + (wave * 2 + (n >> 3)) * OM_S + (n & 7) * 16 + (G & 1) * 8;
  t[11] = L_OM + (16 + (wave >> 2) * 8) * OM_S + (wave & 3) * 128 + lane * 2;
  t[12] = wave == 3 ? L_OM + 16 * OM_S + lane * 8 : L_OM + ((G & 1) * 4 + tq) * OM_S + wave * 128 + xy * 32 + (n & 3) * 8;
  t[13] = wave == 3 || n == xy ? L_ONE : L_ZERO;
  const int d0 = wave * 64 + n * 16 + G * 4;
  t[14] = (go + (wave < 3 ? (d0 >= 96 ? 2048 - 192 : 0) + d0 * 2 : (lane & 1) * 2048 + 192 + xy * 8)) & 0xffffu;
  t[15] = wave < 6 ? q_addr(wave * 32 + (lane & 7) * 4 + ((lane >> 4) & 1) * 2 + (lane >> 5)) : L_SUM;
  *reinterpret_cast<u32x4*>(smem + L_TAB + wave * 2048 + K * 1024 + lane * 16) =
      u32x4{t[8 * K] | (t[8 * K + 4] << 16), t[8 * K + 1] | (t[8 * K + 5] << 16), t[8 * K + 2] | (t[8 * K + 6] << 16),
            t[8 * K + 3] | (t[8 * K + 7] << 16)};
}
__device__ __forceinline__ void att_setup(Att& r, unsigned char* smem, int lane, int wave) {
  const u32x4 a = afrag(smem, L_TAB + wave * 2048 + lane * 16), b = afrag(smem, L_TAB + wave * 2048 + 1024 + lane * 16);
  r.qw = fresh(b[3] >> 16);
#pragma unroll
  for (int k = 0; k < 4; ++k) r.qad[k] = fresh(a[k] & 0xffffu);
  r.qad[4] = fresh(a[0] >> 16);
  r.qad[5] = fresh(a[1] >> 16);
  r.idxq = fresh(a[2] >> 16);
  r.wa = fresh(a[3] >> 16);
  r.ra = fresh(b[0] & 0xffffu);
  r.idx = fresh(b[1] & 0xffffu);
  r.oa = fresh(b[2] & 0xffffu);
  r.la = fresh(b[3] & 0xffffu);
  r.sa = fresh(b[0] >> 16);
  r.ma = fresh(b[1] >> 16);
  r.dst = fresh((wave < 3 ? lane & 12 : ~lane & 30) ? OOB : b[2] >> 16);
}
#define SM "v_smfmac_f32_16x16x64_bf16 "
#define QNOP "s_nop 0\n\t"
#define OEARLY 0xff
#define SAFEB "0x42200000"

using u32x16 = __attribute__((ext_vector_type(16))) unsigned;
using u32x32 = __attribute__((ext_vector_type(32))) unsigned;
template <int OFF>
__device__ __forceinline__ u16* karg() {
  u64 p;
  asm volatile("s_load_dwordx2 %0, %1, %2\n\ts_waitcnt lgkmcnt(0)" : "=s"(p) : "s"(__builtin_amdgcn_kernarg_segment_ptr()), "n"(OFF));
  return (u16*)p;
}
template <class T>
__device__ __forceinline__ T* mkptr(u32 lo, u32 hi) {
  return (T*)(((u64)hi << 32) | lo);
}

__global__ __launch_bounds__(NT, 2) void mega_kernel(Params) {
  __shared__ __attribute__((aligned(16))) unsigned char smem[L_TOTAL];
  u32x16 ka;
  u32x4 kb4;
  u32 rtt;
  u64 ts64;
  {
    static_assert(AREA == 1 << 18 && A_GY == 1 << 17 && SIDE == 1 << 13, "clear addresses");
    asm volatile("s_load_dwordx16 s[36:51], %[kp], 0x0\n\ts_load_dwordx4 s[52:55], %[kp], 0x40\n\t"
                 "s_getpc_b64 s[58:59]\n.Lmk_pc:\n\t"
                 "s_mov_b32 s62, m0\n\ts_mov_b32 m0, %[dst]\n\t"
                 "s_cmpk_lt_u32 %[b], %[pfb]\n\ts_cselect_b32 s57, 0x140, 0\n\t"
                 "v_lshlrev_b32 v1, 7, %[tid]\n\tv_min_u32 v1, .Lmk_code_end-.Lmk_pc-4, v1\n\t"
                 "v_cmp_gt_u32 vcc, s57, %[tid]\n\ts_mov_b64 exec, vcc\n\t"
                 "global_load_lds_dword v1, s[58:59]\n\ts_mov_b64 exec, -1\n\t"
                 "v_readfirstlane_b32 s56, %[tid]\n\t"
                 "v_lshrrev_b32 v1, 2, %[tid]\n\tv_cmp_eq_u32 vcc, 3, v1\n\tv_mov_b32 v3, 0x3f803f80\n\ts_lshr_b32 s57, %[b], 5\n\t"
                 "v_cndmask_b32 v2, 0, 1, vcc\n\tv_cmp_eq_u32 vcc, 2, v1\n\tv_lshlrev_b32 v1, 2, %[tid]\n\ts_bfe_u32 s60, %[b], 0x20003\n\t"
                 "v_cndmask_b32 v2, v2, v3, vcc\n\tv_cmp_eq_u32 vcc, 52, %[tid]\n\tv_mov_b32 v3, %[scl]\n\t"
                 "v_min_u32 v1, 0xfc, v1\n\tv_cndmask_b32 v2, v2, v3, vcc\n\t"
                 "ds_write_b32 v1, v2 offset:%[sm]\n\t"
                 "v_lshlrev_b32 v2, 4, %[tid]\n\tv_and_b32 v1, 0x3ff, v2\n\t"
                 "s_cmpk_ge_u32 s56, 448\n\ts_cbranch_scc1 1f\n\t"
                 "s_mul_i32 s57, s57, 0xf0000\n\ts_and_b32 s61, %[b], 7\n\ts_lshl2_add_u32 s60, s61, s60\n\t"
                 "v_bfe_u32 v3, %[tid], 2, 4\n\ts_mul_i32 s60, s60, 0x180\n\tv_mul_u32_u24 v3, 0x3000, v3\n\t"
                 "s_add_u32 s57, s57, s56\n\tv_bitop3_b32 v2, v2, %[tid], 32 bitop3:0x78\n\ts_add_u32 s57, s57, s60\n\t"
                 "v_and_or_b32 v3, v2, 48, v3\n\ts_mul_i32 s60, s56, 80\n\tv_add_u32 v3, s57, v3\n\t"
                 "s_add_u32 s60, s60, %[lwq]\n\ts_cmpk_eq_u32 s56, 384\n\ts_cselect_b64 vcc, -1, 0\n\ts_cselect_b32 m0, %[lhb], s60\n\t"
                 "v_cndmask_b32 v3, v3, v1, vcc\n\ts_mov_b32 %[rtt], 0\n\t"
                 "s_waitcnt lgkmcnt(0)\n\ts_cselect_b64 s[58:59], s[36:37], s[42:43]\n\ts_barrier\n\t"
                 "global_load_lds_dwordx4 v3, s[58:59] nt\n\t"
                 "s_memrealtime %[ts]\n\ts_branch 2f\n"
                 "1:\n\ts_lshl_b32 s57, s57, 17\n\ts_lshl_b32 s60, s60, 13\n\ts_and_b32 s61, %[b], 15\n\ts_or_b32 s57, s57, s60\n\t"
                 "s_lshl_b32 s61, s61, 9\n\t"
                 "v_mov_b64 v[4:5], 0\n\ts_or_b32 s57, s57, s61\n\tv_mov_b64 v[6:7], 0\n\t"
                 "v_or_b32 v1, s57, v1\n\tv_xor_b32 v2, 0x2000, v1\n\ts_mov_b32 exec_hi, 0\n\t"
                 "s_waitcnt lgkmcnt(0)\n\t"
                 "s_add_u32 s58, s54, 0x1fffff\n\ts_addc_u32 s59, s55, 0\n\ts_and_b32 s58, s58, 0xffe00000\n\t"
                 "global_store_dwordx4 v1, v[4:7], s[58:59] sc1\n\tglobal_store_dwordx4 v2, v[4:7], s[58:59] sc1\n\t"
                 "s_barrier\n\ts_mov_b64 exec, -1\n\ts_memrealtime %[ts]\n\t"
                 "s_waitcnt vmcnt(1)\n\ts_memtime s[58:59]\n\ts_waitcnt vmcnt(0)\n\ts_memtime s[60:61]\n\ts_waitcnt lgkmcnt(0)\n\t"
                 "s_sub_u32 %[rtt], s60, s58\n"
                 "2:\n\ts_mov_b32 m0, s62\n\ts_waitcnt lgkmcnt(0)"
                 : "=&{s[36:51]}"(ka), "=&{s[52:55]}"(kb4), [rtt] "=&s"(rtt), [ts] "=&s"(ts64)
                 : [kp] "s"(__builtin_amdgcn_kernarg_segment_ptr()), [tid] "v"(threadIdx.x), [b] "s"(blockIdx.x), [dst] "n"(L_PF),
                   [scl] "n"(__builtin_bit_cast(u32, SCL)), [sm] "n"(0), [lwq] "n"(L_WQ), [lhb] "n"(L_HB), [pfb] "n"(PFB)
                 : "memory", "vcc", "scc", "v1", "v2", "v3", "v4", "v5", "v6", "v7", "s56", "s57", "s58", "s59", "s60", "s61", "s62");
  }
  const u32 tstart = (u32)ts64;
  const int tid = threadIdx.x, lane0 = tid & 63;
  const int wave = __builtin_amdgcn_readfirstlane(tid >> 6);
  const int b = blockIdx.x;
  const int g = b & 7, j = b >> 3, hsub = j & 3, c = j >> 2, h = g * 4 + hsub;
  const bool isO = j < 20;
  const bool leader = b == 0 && wave == 6;
  const u32 fslot = (u32)((j & 1) * 16384 + ((j - 20) >> 1) * 128);
  u32 tmo = SPIN_CAP;
  volatile LAS u32* cnt = (volatile LAS u32*)(smem + L_CNT);
  Params P;
  P.x0 = mkptr<const u16>(ka[0], ka[1]);
  P.kc = mkptr<const u16>(ka[2], ka[3]);
  P.vc = mkptr<const u16>(ka[4], ka[5]);
  P.wq = mkptr<const u16>(ka[6], ka[7]);
  P.wo = mkptr<const u16>(ka[8], ka[9]);
  P.w1 = mkptr<const u16>(ka[10], ka[11]);
  P.w3 = mkptr<const u16>(ka[12], ka[13]);
  P.ws = mkptr<u32>(kb4[2], kb4[3]);
  u32* const wsb = (u32*)(((uintptr_t)P.ws + 0x1fffffu) & ~(uintptr_t)0x1fffffu);
  const __amdgpu_buffer_rsrc_t rs = __builtin_amdgcn_make_buffer_rsrc(wsb, 0, (int)WS_END, 0x00020000);
  const u32x4 rq = {(u32)(uintptr_t)wsb, (u32)((uintptr_t)wsb >> 32) & 0xffffu, WS_END, 0x00020000u};

  u32 to2;
  u32 tref = 0u, ar = 0u;
  u32x32 kv0, kv1, kv2, kv3, kv4, kv5;
  asm volatile(""
               : "={v[64:95]}"(kv0), "={v[96:127]}"(kv1), "={v[128:159]}"(kv2), "={v[160:191]}"(kv3), "={v[192:223]}"(kv4),
                 "={v[224:255]}"(kv5));
  const int smb = (int)(uintptr_t)(LAS unsigned char*)smem;
  const u32 gop = A_GO + go_slot(hsub * 2) + c * 256;
  u32 tstep = 4 * PQ(0);
  u32 tnext = tstart * 8u + 5 * TSQ + (u32)(hsub * PQ(0) + wave * WOFS);
  const size_t rowbase = (size_t)h * SK + (SK - WIN) + ROW0;
  const u16* vb = P.vc + rowbase * DH;
  const int rot = ROT;
  const u16* ws0 = isO ? P.wo + (size_t)(768 * g) * D + 32 * j : ((j & 1) ? P.w3 : P.w1) + 192 * g + 32 * ((j - 20) >> 1);
  const u16* w2s = mkptr<const u16>(ka[14], ka[15]) + (size_t)(192 * g) * D + 32 * j;
  const int nin = isO ? NINO : ((((uintptr_t)ws0 >> 7) & 7) == 3 ? NINS : NINF);
  const u32 word = (((uintptr_t)ws0 >> 6) & 1) ? (isO ? ORDO : ORDF) : 0x543210u;
  int wn = 0;
  u32 hb = 0u;
  u32 exa = 0u, exl = 0u;
  {
    constexpr int L_CTL = L_LAND + 6 * 2048;
    volatile LAS u32* const CTL = (volatile LAS u32*)(smem + L_CTL);
    u32 gd;
    const u32 lho = WS_HB + (lane0 >> 1) * HBS + (lane0 & 1) * 16;
    const u32 hso = lane0 < 8 ? WS_HB + (u32)h * HBS + lane0 * 4 : WS_GOF + g * HBS;
    if (wave < 6) {
      const u16* s0 = P.wq + (size_t)(80 * c) * (H * DH) + h * DH + wave * 32;
      u32 tq = tstart * 8u + TSQ + (u32)wave * (TSTEP / 8);
#pragma unroll 1
      for (int p = 1; p < 5; ++p) {
        pace(tq, TSQ);
        dma_piece(s0, H * DH, smem, L_WQ + wave * 5120, p, lane0);
      }
    } else if (wave == 6) {
      u32 keep;
      asm volatile("s_mov_b32 %0, m0\n\ts_mov_b32 m0, %3\n\ts_nop 2\n\tglobal_load_lds_dword %1, %2 offset:1024\n\ts_mov_b32 m0, %0"
                   : "=&s"(keep)
                   : "v"(lane0 * 4), "s"(P.x0), "s"((u32)(smb + L_HB))
                   : "memory");
      if (j == SJ) store16(rs, rp_line(0, lane0 >> 3, g) + (lane0 & 7) * 16, u32x4{tstart, tstart, tstart, tstart});
      if (leader) {
        ctl_load<true>(rq, lho, (u32)(smb + L_CTL));
        ctl_load<false>(rq, WS_GOF + (lane0 & 7) * HBS, (u32)(smb + L_CTL + 2560));
      }
      asm volatile("s_waitcnt vmcnt(0)" ::: "memory");
      const u32x4 lv = afrag(smem, L_HB + lane0 * 16);
      const u32 xb = *reinterpret_cast<const u32*>(smem + L_HB + 1024 + lane0 * 4);
      float v[10];
#pragma unroll
      for (int k = 0; k < 4; ++k) {
        v[2 * k] = bflo(lv[k]);
        v[2 * k + 1] = bfhi(lv[k]);
      }
      v[8] = bflo(xb);
      v[9] = bfhi(xb);
      float sm = 0.f;
#pragma unroll
      for (int k = 0; k < 10; ++k) sm += v[k];
      const float mean = wave_sum(sm) * (1.0f / D);
      float q = 0.f;
#pragma unroll
      for (int k = 0; k < 10; ++k) q = fmaf(v[k] - mean, v[k] - mean, q);
      const float var = wave_sum(q) * (1.0f / D);
      const float mb = bf16r(mean);
      const float ri = __builtin_amdgcn_rcpf(bf16r(__builtin_amdgcn_sqrtf(bf16r(bf16r(var) + 1e-5f))));
      u32 hp[5];
#pragma unroll
      for (int k = 0; k < 5; ++k) {
        const u32 cc = cvt2(v[2 * k] - mb, v[2 * k + 1] - mb);
        hp[k] = cvt2(bflo(cc) * ri, bfhi(cc) * ri);
      }
      *reinterpret_cast<u32x4*>(smem + L_LN + lane0 * 16) = u32x4{hp[0], hp[1], hp[2], hp[3]};
      *reinterpret_cast<u32*>(smem + L_LN + 1024 + lane0 * 4) = hp[4];
      asm volatile("s_waitcnt lgkmcnt(0)" ::: "memory");
      *(volatile LAS u32*)(smem + L_VOTE + 4) = 1u;
    } else {
      ctl_load<false>(rq, hso, (u32)(smb + L_CTL + 2304));
      hb = rtt > PTHR ? 1u << ((g >> 2) ^ (j & 1)) : 0u;
    }
    if (wave == 1) *(volatile LAS u32x2*)(smem + L_CLK) = u32x2{now(), rtnow()};
    *reinterpret_cast<u32x4*>(smem + L_P + wave * P_W + lane0 * 16) = u32x4{0u, 0u, 0u, 0u};
    *reinterpret_cast<u32x4*>(smem + L_P + wave * P_W + 256 + lane0 * 16) = u32x4{0u, 0u, 0u, 0u};
    const u16* kb = P.kc + rowbase * DH;
    const u32 vo = (lane0 >> 3) * 384 + 16 * (lane0 & 7);
    const u32 kw = (u32)(smb + L_W2 + wave * 1024 + lane0 * 16), kr = (u32)(smb + L_W2 + wave * 1024 + (lane0 & 7) * 128 + (lane0 & 8) * 8 + (lane0 >> 4) * 16);
#define KPERM(i)                                                                          \
  asm volatile("ds_write_b128 %[w], v[%[r0]:%[r1]]\n\tds_read_b128 v[%[r0]:%[r1]], %[r]" \
               :                                                                          \
               : [w] "v"(kw), [r] "v"(kr), [r0] "n"(kreg(i)), [r1] "n"(kreg(i) + 3)       \
               : "memory")
#define WNEXT                                                                                  \
  dma_piece(ws0, isO ? D : F, smem, L_WB, wave * (isO ? 6 : 5) + ((word >> (4 * wn)) & 7), fresh6(lane0)); \
  wn = wn == (isO ? 5 : 4) ? 0 : wn + 1;
    const u32 qo = WS_GQ1 + (u32)(h * 8 + (lane0 >> 3)) * Q1S + wave * 128 + (lane0 & 7) * 16;
    auto step = [&](auto uc) __attribute__((always_inline)) {
      constexpr int u = decltype(uc)::v;
      if (u == UAD + 1) tstep += __builtin_amdgcn_readfirstlane(cnt[2]);
      BURST(u, vo, (u < 8 ? kb : vb) + ((u + rot) & 7) * GST * DH);
      if (wcnt(u + 1) != wcnt(u) || u + 1 == USW) {
        tnext += hsub * (PQ(u + 1) - PQ(u));
        tstep += 4 * (PQ(u + 1) - PQ(u));
      }
      if (u >= 2 && u < 10) {
        KPERM((3 * u + 18) % 24);
        KPERM((3 * u + 19) % 24);
        KPERM((3 * u + 20) % 24);
      }
      if (u == UAD - 3 && wave == 7) {
        ctl_load<false>(rq, rp_line(0, g, lane0 & 7), (u32)(smb + L_CTL + 2816));
        ++exa;
      }
      if (u == 0) {
        att_table<0>(smem, fresh6(lane0), wave, gop);
        att_table<1>(smem, fresh6(lane0), wave, gop);
      }
      if (u == UQ) {
        if (wave < 6) {
          const int lane = fresh6(lane0), m = lane & 15, G = lane >> 4;
          const int q_l0 = L_WQ + wave * 5120 + wlane(lane, 0), q_l1 = L_WQ + wave * 5120 + wlane(lane, 1);
          const int q_bk = 4096 - (G >> 1) * 1024;
          const int st = m == 8 ? 64 : 0;
          const int sa = m == 8 ? L_LN + 160 * c + 16 * G : (m == 15 ? L_ONE : L_ZERO);
          const u32 q_dst = WS_GQ1 + (u32)(h * 8 + c) * Q1S + wave * 128 + (lane >> 5) * 64 + m * 4;
          asm volatile("" ::: "memory");
          lds_wait((volatile LAS u32*)(smem + L_VOTE + 4), 1u);
          const u32x4 f0 = afrag(smem, sa), f1 = afrag(smem, sa + st), f2 = afrag(smem, G < 2 ? sa + 2 * st : L_TAG);
          f32x4 q_d0 = {0.f, 0.f, 0.f, 0.f}, q_d1 = q_d0;
          q_d0 = mfma32(f0, wfrag(smem, q_l0, 0), q_d0);
          q_d1 = mfma32(f0, wfrag(smem, q_l1, 0), q_d1);
          q_d0 = mfma32(f1, wfrag(smem, q_l0, 2048), q_d0);
          q_d1 = mfma32(f1, wfrag(smem, q_l1, 2048), q_d1);
          q_d0 = mfma32(f2, wfrag(smem, q_l0 + q_bk, 0), q_d0);
          q_d1 = mfma32(f2, wfrag(smem, q_l1 + q_bk, 0), q_d1);
          auto sw = __builtin_amdgcn_permlane32_swap(__float_as_uint(sum4(q_d0)), __float_as_uint(sum4(q_d1)), false, false);
          auto sx = __builtin_amdgcn_permlane16_swap(sw[1], sw[1], false, false);
          const f32x4 qd = quad4(__uint_as_float(cvt2(__uint_as_float(sx[0]), __uint_as_float(sx[1]))));
          if ((lane0 & 19) == 0) store16<AUXQ>(rs, q_dst, __builtin_bit_cast(u32x4, qd));
          ++exa;
        }
      }
      if (u == UAD - 1 && wave == 7) {
        asm volatile("" ::: "memory");
        const int d = (int)(CTL[704 + lane0] - tstart);
        const float sd = sum8((u32)(d + DWIN) < 2u * DWIN ? (float)d : 0.f);
        cnt[2] = (u32)(int)__builtin_rintf(sd * (DEQ / (15 - NUN - UAD)));
        int len = 5 * TSQ + 3 * NUN * TSE + 7 * (TSE / 8);
#pragma unroll
        for (int k = 0; k < 16 - NUN; ++k) len += 4 * PQ(k);
        *(volatile LAS u32*)(smem + L_AN0) = tstart * 8u + (u32)(len + (int)sd);
      }
      if ((u == UB && wave < 6) || (u == UB - 1 && wave == 7)) {
        asm volatile("" ::: "memory");
        if (u == UB - 1) {
          hb += (__builtin_amdgcn_readfirstlane(CTL[576 + c]) & ~3u) + 4u;
          *(volatile LAS u32*)(smem + L_VOTE + 8) = hb;
          hb += 4u;
        }
        u32 t0, t1, last;
        asm volatile("s_mov_b64 exec, 1\n\tv_mov_b32 %0, 1\n\tv_mov_b32 %1, %[sm]\n\tds_add_rtn_u32 %0, %1, %0 offset:%[c]\n\t"
                     "ds_read_b32 %1, %1 offset:%[v]\n\ts_waitcnt lgkmcnt(0)\n\tv_readfirstlane_b32 %2, %0\n\t"
                     "s_cmp_eq_u32 %2, 6\n\ts_cbranch_scc0 1f\n\tbuffer_store_dword %1, off, %[rq], %[o] sc1\n1:\n\t"
                     "s_cselect_b32 %2, 1, 0\n\ts_mov_b64 exec, -1"
                     : "=&v"(t0), "=&v"(t1), "=&s"(last)
                     : [sm] "s"(smb), [c] "n"(L_CNT + 4), [v] "n"(L_VOTE + 8), [rq] "s"(rq), [o] "s"(WS_HB + h * HBS + c * 4)
                     : "memory", "scc");
        exa += last;
        exl += last;
      }
      if (u == UL && wave == 7) {
        ctl_load<false>(rq, hso, (u32)(smb + L_CTL + 2048));
        ++exa;
        if (UL + 2 < UK) ++exl;
      }
      if (u == UG && leader) {
        ctl_load<true>(rq, lho, (u32)(smb + L_CTL + 1024));
        ++exa;
      }
      if (u == UK && wave == 7) {
        HS_WAIT((u32)ch == 0xffu)
        if (lane0 == 0) cnt[1] = 15u;
      }
      if (u == UC && wave < 6) {
        lds_wait(cnt + 1, 15u);
        ctl_load<true, true>(rq, qo, (u32)(smb + L_LAND + wave * 2048));
        ++exa;
      }
      if (u == UW && leader) {
        const u32x4 xa = afrag(smem, L_CTL + lane0 * 16);
        u32x4 lv;
#pragma unroll 1
        for (;;) {
          asm volatile("" ::: "memory");
          lv = afrag(smem, L_CTL + 1024 + lane0 * 16);
          const bool same = lv[0] == xa[0] || lv[1] == xa[1] || lv[2] == xa[2] || lv[3] == xa[3];
          if (__builtin_amdgcn_ballot_w64(same) == 0ull || tmo == 0u) break;
          --tmo;
          __builtin_amdgcn_s_sleep(4);
          ctl_load<true>(rq, lho, (u32)(smb + L_CTL + 1024));
          asm volatile("s_waitcnt vmcnt(0)" ::: "memory");
        }
        u32 nv = 256u;
#pragma unroll
        for (int e = 0; e < 4; ++e)
          nv += (u32)__builtin_popcountll(__builtin_amdgcn_ballot_w64((lv[e] & 2u) != 0u)) -
                (u32)__builtin_popcountll(__builtin_amdgcn_ballot_w64((lv[e] & 1u) != 0u));
        if (lane0 < 8) __builtin_amdgcn_raw_buffer_store_b32(CTL[640 + lane0] + (nv > 256u ? 2u : 1u), rs, WS_GOF + lane0 * HBS, 0, 16);
        ++exa;
        ++exl;
      }
      if (u == UX && wave == 7) {
        ctl_load<false>(rq, hso, (u32)(smb + L_CTL + 2048));
        ++exa;
      }
      if (u == UX && wave < 6) {
        const int lane = fresh6(lane0);
        const int ca = L_CS + wave * 128 + (((lane >> 1) ^ (lane >> 5)) & 1) * 64 + (lane & 1) * 32;
        const f32x4 c0 = *reinterpret_cast<const f32x4*>(smem + ca), c1 = *reinterpret_cast<const f32x4*>(smem + ca + 16);
#pragma unroll 1
        for (int p = 0; p < 5; ++p) {
          u32x4* const pw = reinterpret_cast<u32x4*>(smem + L_WQ + wave * 5120 + p * 1024 + lane * 16);
          const u32x4 w = *pw;
#define CW(k, c, e) cvt2(fmaf(bflo(w[k]), SCL, c[e]), fmaf(bfhi(w[k]), SCL, c[e + 1]))
          *pw = u32x4{CW(0, c0, 0), CW(1, c0, 2), CW(2, c1, 0), CW(3, c1, 2)};
        }
      }
      if (u == UF) {
        if (wave < 6) {
          q1_finish(smem, fresh6(lane0), wave);
          asm volatile("s_waitcnt lgkmcnt(0)" ::: "memory");
          if (lane0 == 0) lds_add(smem, L_CNT, 1u);
        }
      }
      if (wcnt(u) != 0 && (u - WU0) / WSP < nin && (wave & (WSP - 1)) == (u - WU0) % WSP) {
        u32 tw = tnext - WBK - hsub * PQ(u + 1);
        pace(tw, 0u);
        WNEXT
        ++exa;
        ++exl;
      }
    };
    static_assert(UQ >= 1 && UQ + 2 <= UB && UB < UL && UL + 2 <= UK && UK <= UC && UG + 2 <= UW && UC + 2 <= UF && UF < UX && UX >= 9 && UX + 2 < 16 - NUN && UAD >= 3 && WU0 >= UB, "order");
    slots<0, UX + 1>(step);
  }

#define lane0 lane_id()
#pragma unroll 1
  for (int half = 0;; ++half) {
    {
      u32x4 O0, O1, O2, S0, S1;
      float sk;
      Att at;
      const int lane = fresh6(lane0);
      const u32 gso = (u32)half * GO_SZ;
      {
        const u32 off = fresh(ar + A_GQ + hsub * 16384 + wave * 128 + (wave < 6 ? (lane >> 3) * 896 + (lane & 7) * 16 : (lane & 7) * 896));
        if (half == 1) {
          if (wave == 7) wait_tt(A_Q);
        } else {
          BURST(UX + 1, (u32)((lane >> 3) * 384 + 16 * (lane & 7)), vb + ((UX + 1 + rot) & 7) * GST * DH);
          lds_wait(cnt, 6u);
        }
        asm volatile("s_cmp_eq_u32 %[hf], 0\n\ts_cbranch_scc1 1f\n\ts_cmp_lt_u32 %[wv], 7\n\ts_cselect_b64 exec, -1, 0\n\ts_barrier\n\t"
                     "buffer_load_dwordx4 v[44:47], %[of], %[rq], 0 offen sc1\n\ts_mov_b64 exec, -1\n\ts_setprio 2\n1:"
                     : "={v[44:47]}"(O1)
                     : [hf] "s"(gso), [wv] "s"(wave), [of] "v"(off), [rq] "s"(rq)
                     : "scc", "memory");
        att_setup(at, smem, lane, wave);
        to2 = T_O2;
#define QK(W, a, k0, k1) \
  "s_waitcnt lgkmcnt(" #W ")\n\t" SM "v[32:35], v[" #a ":" #a "+3], v[" #k0 ":" #k0 "+7], %[ix]\n\t" SM "v[36:39], v[" #a ":" #a "+3], v[" #k1 ":" #k1 "+7], %[ix]\n\t"
#define QLD "buffer_load_dwordx4 v[44:47], %[of], %[rq], 0 offen sc1\n"
#define QWT "s_sub_u32 s88, s88, 1\n\ts_waitcnt vmcnt(0)\n\t"
        u32 t, z;
        asm volatile("v_mov_b64 v[32:33], 0\n\tv_mov_b64 v[34:35], 0\n\tv_mov_b64 v[36:37], 0\n\tv_mov_b64 v[38:39], 0\n\t"
                     "v_mov_b32 %[z], 0\n\ts_cmp_eq_u32 %[hf], 0\n\ts_cbranch_scc1 5f\n\t"
                     "s_mov_b32 s88, 0x10000\n\ts_cmp_lt_u32 %[wv], 6\n\ts_cbranch_scc1 2f\n\t"
                     "s_bitcmp0_b32 %[wv], 0\n\ts_cselect_b64 exec, -1, 0\n\ts_branch 3f\n"
                     "6:\n\t" QLD "3:\n\t" QWT
                     "v_add_f32 v48, v44, v45\n\tv_mul_f32 v49, 0x3acccccd, v47\n\tv_add_f32 v48, v48, v46\n\t" AND3("v44", "v45", "v46")
                     "v_add_f32_dpp v49, v49, v49" Q1 "v_add_f32_dpp v48, v48, v48" Q1 AND3("%[t]", "1", "v47")
                     "v_add_f32_dpp v49, v49, v49" Q2 "v_add_f32_dpp v48, v48, v48" Q2 "v_cmp_eq_u32 vcc, 0, %[t]\n\t"
                     "v_add_f32_dpp v49, v49, v49" HM "v_add_f32_dpp v48, v48, v48" HM
                     "v_mul_f32 v49, -v49, v49\n\tv_fmac_f32 v49, 0x3acccccd, v48\n\t" CV "v48, 0, |v49|\n\tv_add_f32 v48, 0x3727c5ac, v48\n\t"
                     CV "v48, 0, v48\n\tv_sqrt_f32 v48, v48\n\ts_cselect_b64 vcc, 0, vcc\n\t" CV "v48, 0, v48\n\tv_rcp_f32 v48, v48\n\t"
                     "s_cbranch_vccnz 6b\n\tds_write_b32 %[qw], v48\n\ts_cbranch_execnz 4f\n\ts_branch 2f\n"
                     "1:\n\t" QLD "2:\n\t" QWT
                     "v_permlane32_swap_b32 v44, v45\n\tv_permlane32_swap_b32 v46, v47\n\t"
                     "v_add_f32 v48, v44, v45\n\tv_add_f32 v49, v46, v47\n\t" AND3("v44", "v45", "v46") AND3("%[t]", "1", "v47")
                     "v_permlane16_swap_b32 v48, v49\n\tv_add_f32 v48, v48, v49\n\t"
                     "v_cmp_eq_u32 vcc, 0, %[t]\n\ts_nop 0\n\tv_add_f32_dpp v48, v48, v48" R8
                     CV "v49, v48, v48\n\tds_write_b16 %[qw], v49\n\ts_cselect_b64 vcc, 0, vcc\n\t"
                     "s_cbranch_vccnz 1b\n\ts_mov_b64 exec, -1\n4:\n\ts_waitcnt lgkmcnt(0)\n\ts_barrier\n"
                     "5:\n\tds_read_b128 v[40:43], %[q0]\n\tds_read_b128 v[44:47], %[q1]\n\t"
                     "ds_read_b128 v[48:51], %[q2]\n\tds_read_b128 v[52:55], %[q3]\n\t"
                     "ds_read_b128 v[56:59], %[q4]\n\tds_read_b128 v[60:63], %[q5]\n\tds_read_b32 %[sk], %[z] offset:%[su]\n\t"
                     QK(6, 40, 64, 96) QK(5, 44, 72, 104) "ds_read_b128 v[40:43], %[z]\n\t"
                     QK(5, 48, 80, 112) "ds_read_b128 v[44:47], %[z]\n\t" QK(5, 52, 88, 120) "ds_read_b128 v[48:51], %[z]\n\t"
                     QK(5, 56, 128, 144) QK(3, 60, 136, 152)
                     : "=&{v[40:43]}"(O0), "+{v[44:47]}"(O1), "=&{v[48:51]}"(O2), "=&{v[32:35]}"(S0), "=&{v[36:39]}"(S1),
                       [t] "=&v"(t), [z] "=&v"(z), [sk] "=&{v31}"(sk)
                     : [hf] "s"(gso), [wv] "s"(wave), [of] "v"(off), [rq] "s"(rq), [qw] "v"(at.qw), [q0] "v"(at.qad[0]),
                       [q1] "v"(at.qad[1]), [q2] "v"(at.qad[2]), [q3] "v"(at.qad[3]), [q4] "v"(at.qad[4]), [q5] "v"(at.qad[5]),
                       [ix] "v"(at.idxq), [su] "n"(L_SUM), "{v[64:95]}"(kv0), "{v[96:127]}"(kv1), "{v[128:159]}"(kv2)
                     : V52, "s88", "vcc", "scc", "memory");
      }
      u32 hq = gso;
      asm volatile("" : "+s"(hq));
      if (__builtin_expect(hq == 0u, 0)) {
        const u32 tn_ = tnext, ts_ = tstep, ea_ = exa;
        u32 tnext = tn_, tstep = ts_, exa = ea_, exl = 0u;
        int wn = nin;
        const int lv_ = fresh6(lane0);
        const u32 vo = (lv_ >> 3) * 384 + 16 * (lv_ & 7);
#pragma unroll
        for (int u = UX + 2; u < 16 - NUN; ++u) {
          BURST(u, vo, vb + ((u + rot) & 7) * GST * DH);
          if (u == UX + 2 && wave == 6) {
            const u32x2 c0 = *(volatile LAS u32x2*)(smem + L_CLK);
            const float r = (float)(rtnow() - c0[1]) * (24.2f * 512.f) * __builtin_amdgcn_rcpf((float)(now() - c0[0])) - CLKO;
            *(volatile LAS u32*)(smem + L_CLK) = ((u32)__builtin_fminf(__builtin_fmaxf(r, 0.f), 127.f) << 25) | 1u;
            wait_tt<L_ZERO>(tnext - 160u);
          }
          if (u == UX + 2 && wave == 7) {
            constexpr int L_CTL = L_LAND + 6 * 2048;
            volatile LAS u32* const CTL = (volatile LAS u32*)(smem + L_CTL);
            const u32 hso = lv_ < 8 ? WS_HB + (u32)h * HBS + lv_ * 4 : WS_GOF + g * HBS;
            u32 gd, tmo = SPIN_CAP;
            HS_WAIT(false)
            if (lane0 == 8) *(volatile LAS u32*)(smem + L_AR) = (u32)(g & 3) * AREA + (((u32)(g >> 2) ^ (gd >> 1)) & 1u) * SIDE;
          }
        }
        tnext += wave * (TSE / 8 - WOFS) - hsub * PQ(15);
        tstep = TSE;
#pragma unroll
        for (int t = 48 - 3 * NUN; t < 48; ++t) {
          LOADW(t, vo, vb + ((t / 3 + rot) & 7) * GST * DH);
        }
        if (wave == 7) {
          const u32 a7 = *(volatile LAS u32*)(smem + L_AR) + (u32)(j >> 3) * 16384u + (u32)(j & 7) * 1024u;
          const u32x4 zz = __builtin_bit_cast(u32x4, zero4());
          store16(rs, lv_ * 16 + a7 + 2 * 16384, zz);
          store16(rs, lv_ * 16 + a7 + (j < 16 ? 6 : 8) * 16384, zz);
          store16(rs, lv_ * 16 + a7 + 12 * 16384, zz);
        }
#pragma unroll 1
        for (int p = 0; p < (isO ? NPRE : NPF); ++p) {
          pace(tnext, isO ? TPV : TPF);
          WNEXT
        }
      }
#define PW(V, L) "s_waitcnt vmcnt(" #V "+%[no]) lgkmcnt(" #L ")\n\t"
#define PX(o, a, k) SM "v[40+4*" #o ":43+4*" #o "], v[" #a ":" #a "+3], v[160+24*" #k "+8*" #o ":167+24*" #k "+8*" #o "], %[ix]\n\t"
#define ID " quad_perm:[0,1,2,3] row_mask:0xf bank_mask:"
#define OW(o) CV "v[40+2*" #o "], v[40+4*" #o "], v[41+4*" #o "]\n\t" CV "v[41+2*" #o "], v[42+4*" #o "], v[43+4*" #o "]\n\t" \
  "ds_write_b64 %[oa], v[40+2*" #o ":41+2*" #o "] offset:128*" #o "\n\t"
#define TR "ds_read_b64_tr_b16 "
#define AREAD "ds_read_b64 v[46:47], v50 offset:%[lar]\n\t"
#define ACHK "s_waitcnt lgkmcnt(0)\n\tv_cmp_ne_u32 vcc, 0, v47\n\t"
      asm volatile("s_mov_b32 s80, 0xff00ff\n\ts_mov_b32 s81, 0xff00ff00\n\ts_mov_b32 s82, " SAFEB "\n\ts_mov_b32 s83, 1\n\t" QNOP
                   "v_add_f32 v32, v32, v34\n\tv_add_f32 v34, v33, v35\n\t"
                   "v_add_f32_dpp v34, v36, v38" ID "0x3\n\tv_add_f32_dpp v32, v37, v39" ID "0xc\n\t"
                   "s_nop 0\n\tv_add_f32_dpp v32, v34, v32" R8 "v_mul_f32 %[sk], v32, %[sk]\n"
                   "1:\n\tv_exp_f32 v33, %[sk]\n\ts_cmp_lt_u32 %[wv], 4\n\t" CV "v33, v33, 0\n\t"
                   "ds_write2_b32 %[wa], v33, v33 offset1:%[o1]\n\t"
                   "ds_read_b128 v[52:55], %[ra]\n\tds_read_b128 v[56:59], %[ra] offset:16\n\t"
                   "ds_read_b128 v[60:63], %[ra] offset:32\n\tds_read_b128 v[36:39], %[ra] offset:48\n\ts_setprio 0\n\t"
                   "v_cmp_nge_f32 vcc, s82, |%[sk]|\n\tds_write_b16 %[la], v33\n\t"
                   PW(18, 4) PX(0, 52, 0) PX(1, 52, 0) PX(2, 52, 0) PW(12, 1) PX(0, 56, 1) PX(1, 56, 1) PX(2, 56, 1)
                   PW(6, 1) PX(0, 60, 2) PW(2, 1) PX(0, 36, 3) PX(1, 60, 2) PW(1, 1) PX(1, 36, 3) PX(2, 60, 2) PW(0, 1) PX(2, 36, 3)
                   "s_cbranch_vccnz 7f\n2:\n\ts_mov_b64 exec, s[80:81]\n\t" OW(0) OW(1) OW(2)
                   "s_mov_b64 exec, -1\n\tv_mov_b32 v50, 0\n\ts_cbranch_scc0 3f\n\t"
                   "v_mov_b32_dpp v51, %[ma] row_shr:2 row_mask:0xf bank_mask:0xf bound_ctrl:0\n\t"
                   "ds_read_b128 v[32:35], %[ma]\n\tds_read_b128 v[36:39], v51\n\t"
                   "s_cmp_lt_u32 %[wv], 3\n\ts_waitcnt lgkmcnt(0)\n\ts_barrier\n\t"
                   TR "v[52:53], %[sa]\n\t" TR "v[54:55], %[sa] offset:%[os]\n\ts_cbranch_scc0 4f\n\t"
                   TR "v[56:57], %[sa] offset:64\n\t" TR "v[58:59], %[sa] offset:%[os]+64\n\t" AREAD
                   "s_waitcnt lgkmcnt(3)\n\t" MF F2 ", " F0 ", v[32:35], 0\n\ts_waitcnt lgkmcnt(1)\n\t" MF F2 ", " F1 ", v[36:39], " F2 "\n\t"
                   ACHK "v_add_u32 v56, %[ds], v46\n\ts_nop 3\n\ts_cbranch_vccnz 8f\n\t"
                   CV "v52, v60, v61\n\t" CV "v53, v62, v63\n\tv_or_b32 v52, 1, v52\n\tv_or_b32 v53, 1, v53\n\t"
                   "buffer_store_dwordx2 v[52:53], v56, %[rq], %[so] offen sc1\n\ts_branch 9f\n"
                   "7:\n\tv_mov_b32 v52, 0\n\tv_mov_b32 v53, 0xff800000\n\tv_cmp_ne_u32 vcc, s82, v52\n\ts_and_b64 exec, vcc, 1\n\t"
                   "ds_write2_b32 v52, v53, v53 offset0:%[lar]/4+1 offset1:%[lar]/4+2\n\t"
                   "s_cmp_lt_u32 %[wv], 4\n\ts_branch 2b\n"
                   "8:\n\tds_max_f32 v50, %[sk] offset:%[lar]+8\n\ts_mov_b32 s82, 0\n\ts_waitcnt lgkmcnt(0)\n\ts_barrier\n\t"
                   "ds_read_b32 v52, v50 offset:%[lar]+8\n\tds_write_b32 v50, v50 offset:%[lar]+4\n\t"
                   "ds_read_b128 v[40:43], v50\n\tds_read_b128 v[44:47], v50\n\tds_read_b128 v[48:51], v50\n\ts_waitcnt lgkmcnt(0)\n\t"
                   "v_readfirstlane_b32 s83, v52\n\tv_sub_f32 %[sk], %[sk], v52\n\ts_bitset1_b32 s83, 0\n\ts_branch 1b\n"
                   "4:\n\t" AREAD "s_waitcnt lgkmcnt(1)\n\t" MF F2 ", " F0 ", v[32:35], 0\n\t"
                   ACHK "v_add_u32 v56, %[ds], v46\n\tv_mov_b32 v58, s83\n\ts_nop 3\n\t"
                   "v_add_f32 v60, v60, v61\n\tv_add_f32 v62, v62, v63\n\tv_add_f32 v59, v60, v62\n\ts_cbranch_vccnz 8b\n\ts_nop 0\n\t"
                   "v_add_f32_dpp v59, v59, v59 row_bcast:15 row_mask:0xa bank_mask:0xf\n\tv_or_b32 v59, 1, v59\n\t"
                   "buffer_store_dwordx2 v[58:59], v56, %[rq], %[so] offen sc1\n\ts_branch 9f\n"
                   "3:\n\ts_waitcnt lgkmcnt(0)\n\ts_barrier\n\t" AREAD "v_bfrev_b32 v56, 1\n\t" ACHK "s_cbranch_vccnz 8b\n\t"
                   "buffer_store_dword v56, v56, %[rq], 0 offen\n9:"
                   : "+{v[40:43]}"(O0), "+{v[44:47]}"(O1), "+{v[48:51]}"(O2), "+{v[32:35]}"(S0), "+{v[36:39]}"(S1), [sk] "+{v31}"(sk)
                   : [ix] "v"(at.idx), [no] "n"(NPRE), [la] "v"(at.la), [oa] "v"(at.oa), [sa] "v"(at.sa), [ma] "v"(at.ma),
                     [wa] "v"(at.wa), [ra] "v"(at.ra), [o1] "n"(P_U / 4 + 1), [ds] "v"(at.dst), [wv] "s"(wave), [rq] "s"(rq),
                     [so] "s"(gso), [lar] "n"(L_AR), [os] "n"(OM_S * 8), "{v[160:191]}"(kv3), "{v[192:223]}"(kv4), "{v[224:255]}"(kv5)
                   : V52, "s80", "s81", "s82", "s83", "vcc", "scc", "memory");
      tref = now();
      ar = __builtin_amdgcn_readfirstlane(O1[2]);
    }
    const u32 go = ar + A_GO + (u32)half * GO_SZ;
    const u32 gyw = A_GY + (u32)half * GY_PAIR;
    const u32 gy = gyw + ar;
    const u32 gh = go + A_GH;
    const int land = L_LAND + wave * 2048;
    if (isO) {
      {
        const int lane = fresh6(lane0);
        const u32 voff = fresh(go + go_slot(wave) + ((lane * 16) ^ ((lane >> 4) * 64)));
        if (wave == 7) {
          if (half)
            wait_until(tref + to2);
          else
            wait_tt<L_AN0>(B_O);
        }
        if (wave == 6 && half == 0) wait_until(tref + 24 * T_OL);
        asm volatile("s_mov_b32 m0, %2\n\ts_barrier\n\t"
                     "buffer_load_dwordx4 %0, %1, 0 offen sc1 lds\n\tbuffer_load_dwordx4 %0, %1, 0 offen offset:1024 sc1 lds"
                     :
                     : "v"(voff), "s"(rq), "s"(land)
                     : "memory");
        const int l0 = fresh(L_WB + wave * 6144 + wlane(lane, 0)), l1 = l0 ^ 32;
        asm volatile("s_waitcnt vmcnt(3)" ::: "memory");
        u32x4 wf[6];
#pragma unroll
        for (int k = 0; k < 3; ++k) {
          wf[2 * k] = wfrag(smem, l0, 2048 * k);
          wf[2 * k + 1] = wfrag(smem, l1, 2048 * k);
        }
        asm volatile("" ::: "memory");
        const int n = lane & 15, G = lane >> 4;
        const int r0_ = land + (n & 7) * 256 + (n & 3) * 64;
        const int ra = fresh(n < 8 ? r0_ + G * 16 : r0_ ^ 192), rb = fresh((r0_ + G * 16) ^ 64), rc = fresh((r0_ + G * 16) ^ 128);
        const int rw = fresh(L_RED + (lane >> 1) * 80 + wave * 8 + (lane & 1) * 4);
        const int rr = fresh(L_RED + n * 80);
        const u32 dst = fresh(gyw + gy_rep(G) + gy_chunk(4 * j + (n >> 2)) + (n & 3) * 4 + g * 256);
        u32 t;
#define RS(r, e) "v_mul_f32_dpp v" #r ", v41, v" #r " row_newbcast:" #e ALL
        asm volatile("s_mov_b64 s[82:83], -1\n\ts_movk_i32 s89, %[sd]\n\t" POLL_HEAD
                     "s_branch 5f\n"
                     "7:\n\tv_max_f32_dpp v41, v52, v52" Q1 "s_nop 1\n\tv_max_f32_dpp v41, v41, v41" Q2
                     "s_nop 1\n\tv_max_f32_dpp v41, v41, v41" HM "v_sub_f32 v41, v52, v41\n\tv_exp_f32 v41, v41\n\ts_nop 0\n\t"
                     "v_mul_f32 v42, v41, v43\n\ts_nop 1\n\tv_add_f32_dpp v42, v42, v42" Q1 "s_nop 1\n\tv_add_f32_dpp v42, v42, v42" Q2
                     "s_nop 1\n\tv_add_f32_dpp v42, v42, v42" HM "v_mov_b32_dpp v41, v41 row_shl:4 row_mask:0x2 bank_mask:0xf\n\t"
                     "v_rcp_f32 v42, v42\n\ts_nop 0\n\t"
                     RS(44, 8) RS(45, 9) RS(46, 10) RS(47, 11) RS(48, 8) RS(49, 9) RS(50, 10) RS(51, 11) "s_branch 3f\n"
                     "2:\n\tv_cmp_eq_u32 vcc, 0, %[t]\n\ts_cbranch_vccz 7b\n\t" POLL_MASK POLL_LOAD
                     "\n5:\n\tv_mov_b32 v41, 1\n\t" POLL_READ
                     "s_waitcnt lgkmcnt(2)\n\t"
                     MF "v[44:47], " F0 ", %[w0], 0\n\tv_add_f32 v43, v53, v55\n\t"
                     "v_or_b32_dpp v41, v52, v41 quad_perm:[0,1,2,3] row_mask:0xf bank_mask:0xc\n\t"
                     MF "v[48:51], " F0 ", %[w1], 0\n\tv_add_f32_dpp v42, v43, v43" Q1 AND3("v52", "v53", "v54")
                     "s_waitcnt lgkmcnt(1)\n\t"
                     MF "v[44:47], " F1 ", %[w2], v[44:47]\n\tv_add_f32_dpp v42, v42, v42" Q2 AND3("%[t]", "v55", "v56")
                     MF "v[48:51], " F1 ", %[w3], v[48:51]\n\tv_add_f32_dpp v42, v42, v42" HM AND3("%[t]", "v57", "v58")
                     "s_waitcnt lgkmcnt(0)\n\t"
                     MF "v[44:47], " F2 ", %[w4], v[44:47]\n\t" MF "v[48:51], " F2 ", %[w5], v[48:51]\n\tv_rcp_f32 v42, v42\n\t"
                     AND3("%[t]", "v59", "v60") AND3("%[t]", "v61", "v62") AND3("%[t]", "1", "v63")
                     "v_cmp_gt_u32 vcc, v41, %[t]\n\ts_cbranch_vccnz 2b\n"
                     "3:\n\ts_mov_b32 exec_hi, 0\n\ts_cmp_eq_u32 %[wv], 0\n\t" PK(44, 46) PK(48, 50)
                     "v_add_f32 v44, v44, v45\n\tv_add_f32 v48, v48, v49\n\ts_nop 1\n\t"
                     "v_permlane16_swap_b32 v44, v48\n\tv_add_f32 v44, v44, v48\n\tv_mul_f32_dpp v44, v42, v44 row_newbcast:8" ALL
                     "ds_write_b32 %[rw], v44\n\ts_cselect_b64 exec, -1, 0\n\ts_waitcnt lgkmcnt(0)\n\ts_barrier\n\t"
                     "ds_read_b128 " F0 ", %[rr]\n\tds_read_b128 " F1 ", %[rr] offset:16\n\tds_read_b128 " F2 ", %[rr] offset:32\n\t"
                     "ds_read_b128 v[44:47], %[rr] offset:48\n\ts_cbranch_execz 9f\n\t"
                     "s_waitcnt lgkmcnt(2)\n\t" PK(52, 54) PK(56, 58) PK(52, 56) "s_waitcnt lgkmcnt(1)\n\t" PK(52, 60) PK(52, 62)
                     "s_waitcnt lgkmcnt(0)\n\t" PK(52, 44) PK(52, 46)
                     CV "v52, v52, v53\n\tv_or_b32 v52, 1, v52\n\t"
                     "buffer_store_dword v52, %[ds], %[rq], 0 offen sc1\n\tbuffer_store_dword v52, %[ds], %[rq], s89 offen sc1\n"
                     "9:\n\ts_mov_b64 exec, -1"
                     : [t] "=&v"(t)
                     : [ra] "v"(ra), [rb] "v"(rb), [rc] "v"(rc), WIN6(wf), [vo] "v"(voff), [rq] "s"(rq), [land] "s"(land), [rw] "v"(rw),
                       [rr] "v"(rr), [ds] "v"(dst), [wv] "s"(wave), [sd] "n"(SIDE)
                     : V40, V52, S80);
      }
      tref = now();
      if (half == 0) {
        if (wave >= 1 && wave < 4) {
          wait_until(tref + T_W2);
#pragma unroll
          for (int p = 0; p < 4; ++p) dma_piece(w2s, D, smem, L_W2, (wave - 1) * 4 + ((j & W2R) ? 3 - p : p), fresh6(lane0));
          asm volatile("s_waitcnt vmcnt(0)" ::: "memory");
          if (lane0 == 0) lds_add(smem, L_CNT + 12, 1u);
        }
        if (wave == 0) {
          wait_until(tref + 24 * T_LAZ0);
          x_request(rq, fresh6(lane0), gy + gy_row(c), land);
          x_update<true>(smem, rq, fresh6(lane0), gy + gy_row(c), land, c);
        }
      } else if (g == 0 && wave == 2) {
        const int lane = fresh6(lane0);
        const int col = j * 32 + (lane & 7) * 4 + ((lane >> 4) & 1) * 2 + (lane >> 5);
        u32 x0v, dummy;
        u16* const po = karg<64>() + col;
        asm volatile("global_load_ushort %0, %2, off\n\tglobal_load_ushort %1, %3, off\n\ts_waitcnt vmcnt(0)"
                     : "=&v"(x0v), "=&v"(dummy)
                     : "v"(karg<0>() + col), "v"(po)
                     : "memory");
        wait_until(tref + 24 * T_LAZY);
        const u32 o = gy + gy_chunk(4 * j + ((lane >> 1) & 3)) + (lane >> 3) * 256 + (lane & 1) * 8;
        const u32 off[3] = {o - GY_PAIR, o - GY_PAIR + GY_INST, o};
        float y[3];
        col_poll<3>(rq, off, y);
        float x3 = bf16r(bf16r(bf16r(bflo(x0v) + y[0]) + y[1]) + y[2]);
        const u32 off2 = fresh(o + GY_INST);
        asm volatile("" : "+v"(x3));
        wait_tt(A_F);
        u32 t;
        asm volatile("s_mov_b32 s88, 0x10000\n"
                     "1:\n\tbuffer_load_dwordx2 v[40:41], %[of], %[rq], 0 offen sc1\n\ts_sub_u32 s88, s88, 1\n\ts_waitcnt vmcnt(0)\n\t"
                     "v_lshlrev_b32 v42, 16, v40\n\tv_and_b32 v43, 0xffff0000, v40\n\t"
                     "v_lshlrev_b32 v44, 16, v41\n\tv_and_b32 v45, 0xffff0000, v41\n\t"
                     "v_permlane32_swap_b32 v42, v43\n\t" AND3("v40", "1", "v41") "v_permlane32_swap_b32 v44, v45\n\t"
                     "v_add_f32 v42, v42, v43\n\tv_add_f32 v44, v44, v45\n\tv_cmp_eq_u32 vcc, 0, %[t]\n\ts_cselect_b64 vcc, 0, vcc\n\t"
                     "v_permlane16_swap_b32 v42, v44\n\tv_add_f32 v42, v42, v44\n\ts_cbranch_vccnz 1b\n\ts_nop 0\n\t"
                     "v_add_f32_dpp v42, v42, v42" R8 CV "v42, 0, v42\n\tv_add_f32 v42, %[x], v42\n\t" CV "v42, v42, v42\n\t"
                     "global_store_short %[po], v42, off"
                     : [t] "=&v"(t)
                     : [of] "v"(off2), [rq] "s"(rq), [x] "v"(x3), [po] "v"(po)
                     : V40, "s88", "vcc", "scc", "memory");
      }
      if (wave >= 6) {
        const int lane = fresh6(lane0), wv = wave - 6;
        const int n = lane & 15, G = lane >> 4;
        const int l0 = fresh(L_W2 + wlane(lane, wv));
        const int l48 = lane < 48 ? lane : 0;
        const u32 off = fresh(gh + (u32)l48 * 16);
        const int aw = fresh(L_ACT + wv * 384 + l48 * 8);
        const int ab = fresh(L_ACT + wv * 384 + 16 * G);
        const u32 dst = fresh((lane & (half ? 49 : 1)) ? OOB : gyw + GY_INST + gy_rep(G) + gy_chunk(4 * j + wv * 2 + (n >> 3)) + (n & 7) * 2 + g * 256);
        if (half) {
          wait_tt(A_H);
        } else {
          wait_tt<L_AN0>(B_H - B_HC);
          wait_until(tref + 24 * T_HL);
        }
        u32 t;
        u32x4 pa, pb;
#define HLD "buffer_load_dwordx4 v[40:43], %[of], %[rq], 0 offen sc1\n\tbuffer_load_dwordx4 v[44:47], %[of], %[rq], s90 offen sc1\n"
        asm volatile("s_movk_i32 s90, 0x4000\n\tv_mov_b32 v48, 0\n\t" HLD
                     "1:\n\tds_read_b32 %[t], v48 offset:%[c]\n\ts_waitcnt lgkmcnt(0)\n\tv_cmp_ne_u32 vcc, 3, %[t]\n\ts_cbranch_vccnz 1b"
                     : "=&{v[40:43]}"(pa), "=&{v[44:47]}"(pb), [t] "=&v"(t)
                     : [of] "v"(off), [rq] "s"(rq), [c] "n"(L_CNT + 12)
                     : "v48", "s90", "vcc", "memory");
        u32x4 wf[6];
#pragma unroll
        for (int k = 0; k < 6; ++k) wf[k] = wfrag(smem, l0, 2048 * k);
#define HK(W, a, w, c) "s_waitcnt lgkmcnt(" #W ")\n\t" MF "v[36:39], v[" #a ":" #a "+3], %[" #w "], " c "\n\t"
        asm volatile("s_mov_b32 s88, 0x10000\n\ts_movk_i32 s89, %[sd]\n\ts_movk_i32 s90, 0x4000\n\ts_branch 2f\n"
                     "1:\n\ts_waitcnt lgkmcnt(0)\n\t" HLD "2:\n\ts_sub_u32 s88, s88, 1\n\ts_waitcnt vmcnt(0)\n\t"
                     "v_pk_mul_f32 v[48:49], v[40:41], v[44:45]\n\tv_pk_mul_f32 v[50:51], v[42:43], v[46:47]\n\t"
                     CV "v48, v48, v49\n\t" CV "v49, v50, v51\n\tds_write_b64 %[aw], v[48:49]\n\t"
                     "ds_read_b128 v[48:51], %[ab]\n\tds_read_b128 v[52:55], %[ab] offset:64\n\t"
                     "ds_read_b128 v[56:59], %[ab] offset:128\n\tds_read_b128 v[60:63], %[ab] offset:192\n\t"
                     AND3("v40", "v41", "v42") AND3("%[t]", "v43", "v44") AND3("%[t]", "v45", "v46") AND3("%[t]", "1", "v47")
                     "ds_read_b128 v[40:43], %[ab] offset:256\n\tds_read_b128 v[44:47], %[ab] offset:320\n\t"
                     "v_cmp_eq_u32 vcc, 0, %[t]\n\ts_cselect_b64 vcc, 0, vcc\n\ts_cbranch_vccnz 1b\n\t"
                     HK(5, 48, w0, "0") HK(4, 52, w1, "v[36:39]") HK(3, 56, w2, "v[36:39]") HK(2, 60, w3, "v[36:39]")
                     HK(1, 40, w4, "v[36:39]") HK(0, 44, w5, "v[36:39]")
                     "s_nop 7\n\tv_mov_b32_dpp v37, v36" Q1 CV "v36, v36, v37\n\tv_or_b32 v36, 1, v36\n\t"
                     "buffer_store_dword v36, %[ds], %[rq], 0 offen sc1\n\tbuffer_store_dword v36, %[ds], %[rq], s89 offen sc1"
                     : [t] "=&v"(t), "+{v[40:43]}"(pa), "+{v[44:47]}"(pb)
                     : WIN6(wf), [of] "v"(off), [rq] "s"(rq), [aw] "v"(aw), [ab] "v"(ab), [ds] "v"(dst), [sd] "n"(SIDE)
                     : V36, "v48", "v49", "v50", "v51", V52, "s88", "s89", "s90", "vcc", "scc", "memory");
      }
    } else {
      const int lane = fresh6(lane0);
      const int n = lane & 15, G = lane >> 4;
      if (half == 1) {
        x_update<true>(smem, rq, lane, gy - GY_PAIR + GY_INST + gy_row(wave), land + L_LAND2, wave);
      } else {
        int wn = nin + NPF;
#pragma unroll 1
        for (u32 tw = tref + 24 * T_FW; wn != 0; tw += 24 * T_FD) {
          wait_until(tw);
          WNEXT
        }
      }
      G80 a;
      g80_addr(a, lane, L_WB + wave * 5120, L_HB + 160 * wave, land, L_ONE);
      const u32 voff = fresh(gy + gy_row(wave) + ((lane * 16) ^ ((lane >> 4) * 64)));
      const int rw = fresh(L_RED + n * 144 + wave * 16 + G * 4);
      const int sw_ = fresh(n == 8 ? L_ST + wave * 16 + G * 4 : L_ACT + lane * 4);
      const int rr = fresh(L_RED + (lane & 31) * 144 + (lane >> 5) * 64), rs_ = fresh(L_ST + 16 * (lane & 7));
      const u32 dst = fresh(lane < 32 ? gh + fslot + lane * 4 : OOB);
      if (wave == 7) {
        if (half)
          wait_tt(A_Y);
        else
          wait_tt<L_AN0>(B_Y);
      }
      if (wave == 6 && half == 0) wait_until(tref + 24 * T_YL);
      asm volatile(POLL_FIRST "s_waitcnt vmcnt(2)"
                   :
                   : [vo] "v"(voff), [rq] "s"(rq), [land] "s"(land)
                   : "memory");
      g80_load(a, smem);
      u32 t;
#define SH4 " row_shr:4" ALL
      asm volatile(USED10 "s_mov_b32 s89, 0x810000\n\t" POLL_HEAD "s_branch 1f\n2:\n\t" POLL_MASK POLL_LOAD "\n1:\n\t" POLL_READ
                   "s_waitcnt lgkmcnt(2)\n\t"
                   MF "v[40:43], " F0 ", " F0 ", 0\n\t" AND3("v52", "v53", "v54")
                   MF "v[44:47], " F0 ", %[w0], 0\n\t" MF "v[48:51], " F0 ", %[w1], 0\n\t"
                   "s_waitcnt lgkmcnt(1)\n\t"
                   MF "v[40:43], " F1 ", " F1 ", v[40:43]\n\t" AND3("%[t]", "v55", "v56") AND3("%[t]", "v57", "v58")
                   "s_waitcnt lgkmcnt(0)\n\t"
                   MF "v[40:43], " F2 ", " F2 ", v[40:43]\n\t" AND3("%[t]", "v59", "v60") AND3("%[t]", "v61", "v62")
                   MF "v[44:47], " F1 ", %[w2], v[44:47]\n\t" AND3("%[t]", "1", "v63") "v_cmp_eq_u32 vcc, 0, %[t]\n\t"
                   MF "v[48:51], " F1 ", %[w3], v[48:51]\n\ts_and_b64 vcc, vcc, s[80:81]\n\ts_cbranch_scc1 2b\n"
                   "3:\n\t" MF "v[44:47], " F2 ", %[w4], v[44:47]\n\t" PK(40, 42) "v_add_f32 v40, v40, v41\n\t"
                   MF "v[48:51], " F2 ", %[w5], v[48:51]\n\ts_cmp_eq_u32 %[wv], 0\n\t"
                   "v_add_f32_dpp v40, v40, v40" Q1 "s_nop 1\n\tv_add_f32_dpp v40, v40, v40" Q2 PK(44, 46)
                   "v_add_f32 v44, v44, v45\n\tv_add_f32_dpp v41, v40, v40" SH4 PK(48, 50) "v_add_f32 v48, v48, v49\n\t"
                   "v_add_f32_dpp v40, v41, v40" SH4
                   "ds_write2st64_b32 %[rw], v44, v48 offset1:9\n\tds_write_b32 %[sw], v40\n\t"
                   "s_cselect_b64 exec, -1, 0\n\ts_bitcmp1_b32 %[j], 0\n\ts_waitcnt lgkmcnt(0)\n\ts_barrier\n\t"
                   "ds_read_b128 v[40:43], %[rs]\n\tds_read_b128 " F0 ", %[rr]\n\tds_read_b128 " F1 ", %[rr] offset:16\n\t"
                   "ds_read_b128 " F2 ", %[rr] offset:32\n\tds_read_b128 v[44:47], %[rr] offset:48\n\ts_cbranch_execz 9f\n\t"
                   "s_waitcnt lgkmcnt(4)\n\tv_add_f32 v40, v40, v41\n\tv_add_f32 v42, v40, v42\n\tv_add_f32_dpp v43, v43, v43" Q1
                   "s_waitcnt lgkmcnt(2)\n\tv_add_f32_dpp v42, v42, v42" Q1 "v_add_f32_dpp v43, v43, v43" Q2 PK(52, 56)
                   "v_add_f32_dpp v42, v42, v42" Q2 "v_add_f32_dpp v43, v43, v43" HM PK(54, 58) "v_add_f32_dpp v42, v42, v42" HM
                   "v_mul_f32 v43, 0x3acccccd, v43\n\tv_mul_f32 v42, 0x3acccccd, v42\n\tv_fma_f32 v42, -v43, v43, v42\n\t"
                   "s_waitcnt lgkmcnt(0)\n\t" PK(52, 60) PK(54, 62) PK(52, 44) PK(54, 46)
                   CV "v43, 0, v43\n\tv_add_f32 v52, v52, v53\n\tv_fma_f32 v54, -v43, v55, v54\n\t"
                   "v_add_f32 v53, v52, v54\n\tv_add_f32 v52, v52, v54\n\t"
                   CV "v42, 0, |v42|\n\tv_add_f32 v42, 0x3727c5ac, v42\n\t" CV "v42, 0, v42\n\tv_sqrt_f32 v42, v42\n\t"
                   "v_permlane32_swap_b32 v52, v53\n\t" CV "v42, 0, v42\n\tv_rcp_f32 v42, v42\n\tv_add_f32 v52, v52, v53\n\t"
                   "v_mul_f32 v52, v52, v42\n\t" CV "v53, 0, v52\n\tv_mul_f32 v54, 0xbfb8aa3b, v53\n\tv_exp_f32 v54, v54\n\t"
                   "s_cbranch_scc1 8f\n\tv_add_f32 v54, 1.0, v54\n\tv_rcp_f32 v54, v54\n\ts_nop 0\n\tv_mul_f32 v52, v53, v54\n"
                   "8:\n\t" CV "v52, s89, v52\n\tbuffer_store_dword v52, %[ds], %[rq], 0 offen sc1\n"
                   "9:\n\ts_mov_b64 exec, -1"
                   : [t] "=&v"(t)
                   : [ra] "v"(a.ra), [rb] "v"(a.rb), [rc] "v"(a.rc), WIN6(a.w), [vo] "v"(voff), [rq] "s"(rq), [land] "s"(land),
                     [rw] "v"(rw), [sw] "v"(sw_), [rr] "v"(rr), [rs] "v"(rs_), [ds] "v"(dst), [wv] "s"(wave), [j] "s"(j)
                   : V40, V52, S80);
      tref = now();
      if (half == 0 && wave == 1 && j == 20 && lane0 < 8) {
        const u32 nw = rtnow() * 8u, an = *(volatile LAS u32*)(smem + L_AN0);
        const u32 sv = (int)(an + B_H - B_T - nw) >= 0 ? an + B_E : nw + D_E;
        __builtin_amdgcn_raw_buffer_store_b32(((sv & 0xffffffu) << 1) | *(volatile LAS u32*)(smem + L_CLK), rs, gy_rep(fresh6(lane0)) + st_line(g), 0, 16);
      }
#define GET_REF                                                                            \
  const u32 te = __builtin_amdgcn_readfirstlane(*(volatile LAS u32*)(smem + L_AN0)) + B_E;  \
  const u32 so = fresh(ar + st_line(fresh6(lane0) & 7));                                    \
  wait_tt<L_AN0>(B_H - B_A);                                                                \
  if (stamp_poll(smem, rq, so, te) != te) wait_tt(0u);
      if (half == 0 && wave == 7) {
        GET_REF
      } else if (half == 0 && wave != 0) {
#pragma unroll 1
        for (int i = wave;; i = (i + 1) & 7) {
          x_update<false>(smem, rq, fresh6(lane0), 0u, L_LAND + i * 2048, i);
          if (i < 6) break;
        }
      }
    }
    if (half == 1) break;
    {
      if (wave < 7) {
        const u32 gq = ar + A_GQ + hsub * 16384 + c * 896 + wave * 128;
        G80 a;
        u32 t;
        if (wave == 6) {
          const int lane = fresh6(lane0);
          const int n = lane & 15;
          const u32 voff = fresh(gy + GY_INST + gy_row(c) + ((lane * 16) ^ ((lane >> 4) * 64)));
          asm volatile(POLL_FIRST : : [vo] "v"(voff), [rq] "s"(rq), [land] "s"(land) : "memory");
          g80_addr(a, lane, 0, L_HB + 160 * c, land, L_ONE);
          const u32 dst = fresh((gq + (lane >> 4) * 4) | (u32)((n ^ 8) + 15) >> 4 << 31);
          asm volatile(USED10 POLL_HEAD "s_branch 1f\n"
                       "2:\n\t" POLL_MASK POLL_LOAD "\n1:\n\t" POLL_READ
                       "s_waitcnt lgkmcnt(2)\n\t" MF "v[44:47], " F0 ", " F0 ", 0\n\t" AND3("v52", "v53", "v54")
                       "s_waitcnt lgkmcnt(1)\n\t" MF "v[44:47], " F1 ", " F1 ", v[44:47]\n\t" AND3("%[t]", "v55", "v56") AND3("%[t]", "v57", "v58")
                       "s_waitcnt lgkmcnt(0)\n\t" MF "v[44:47], " F2 ", " F2 ", v[44:47]\n\t" AND3("%[t]", "v59", "v60")
                       AND3("%[t]", "v61", "v62") AND3("%[t]", "1", "v63")
                       "v_cmp_eq_u32 vcc, 0, %[t]\n\ts_and_b64 vcc, vcc, s[80:81]\n\ts_cbranch_scc1 2b\n3:\n\ts_nop 1\n\t"
                       PK(44, 46) "v_add_f32 v44, v44, v45\n\ts_nop 1\n\tv_add_f32_dpp v44, v44, v44" Q1
                       "s_nop 1\n\tv_add_f32_dpp v44, v44, v44" Q2 "s_nop 1\n\tv_add_f32_dpp v45, v44, v44" SH4
                       "s_nop 1\n\tv_add_f32_dpp v44, v45, v44" SH4 "v_or_b32 v44, 1, v44\n\t"
                       "buffer_store_dword v44, %[ds], %[rq], 0 offen sc1"
                       : [t] "=&v"(t)
                       : [ra] "v"(a.ra), [rb] "v"(a.rb), [rc] "v"(a.rc), [vo] "v"(voff), [rq] "s"(rq), [land] "s"(land), [ds] "v"(dst)
                       : V40, V52, S80);
        } else {
        const int lane = fresh6(lane0);
        const u32 voff = fresh(gy + GY_INST + gy_row(c) + ((lane * 16) ^ ((lane >> 4) * 64)));
        g80_addr(a, lane, L_WQ + wave * 5120, L_HB + 160 * c, land, L_ZERO);
        g80_load(a, smem);
        const u32 dst = fresh((gq + lane * 4) | (u32)lane >> 5 << 31);
        if (wave == 4) {
          wait_tt<L_AN0>(B_E);
        }
        if (wave == 5 && isO) {
          wait_until(tref + 24 * T_AO);
          GET_REF
        }
        asm volatile(POLL_FIRST USED10 POLL_HEAD "s_branch 1f\n"
                     "2:\n\t" POLL_MASK POLL_LOAD "\n1:\n\t" POLL_READ
                     "s_waitcnt lgkmcnt(2)\n\t"
                     MF "v[44:47], " F0 ", %[w0], 0\n\t" AND3("v52", "v53", "v54") MF "v[48:51], " F0 ", %[w1], 0\n\t"
                     "s_waitcnt lgkmcnt(1)\n\t"
                     MF "v[44:47], " F1 ", %[w2], v[44:47]\n\t" AND3("%[t]", "v55", "v56") AND3("%[t]", "v57", "v58")
                     MF "v[48:51], " F1 ", %[w3], v[48:51]\n\t"
                     "s_waitcnt lgkmcnt(0)\n\t"
                     MF "v[44:47], " F2 ", %[w4], v[44:47]\n\t" MF "v[48:51], " F2 ", %[w5], v[48:51]\n\t"
                     AND3("%[t]", "v59", "v60") AND3("%[t]", "v61", "v62") AND3("%[t]", "1", "v63")
                     "v_cmp_eq_u32 vcc, 0, %[t]\n\ts_and_b64 vcc, vcc, s[80:81]\n\ts_cbranch_scc1 2b\n"
                     "3:\n\ts_nop 0\n\t" PK(44, 46) PK(48, 50) "v_add_f32 v44, v44, v45\n\tv_add_f32 v48, v48, v49\n\ts_nop 1\n\t"
                     "v_permlane16_swap_b32 v44, v48\n\tv_add_f32 v45, v44, v48\n\tv_add_f32 v44, v44, v48\n\ts_nop 1\n\t"
                     "v_permlane32_swap_b32 v44, v45\n\tv_add_f32 v44, v44, v45\n\tv_or_b32 v44, 1, v44\n\t"
                     "buffer_store_dword v44, %[ds], %[rq], 0 offen sc1"
                     : [t] "=&v"(t)
                     : [ra] "v"(a.ra), [rb] "v"(a.rb), [rc] "v"(a.rc), WIN6(a.w), [vo] "v"(voff), [rq] "s"(rq), [land] "s"(land),
                       [ds] "v"(dst)
                     : V40, V52, S80);
        }
      } else {
        bar();
      }
      if (!isO) x_request(rq, fresh6(lane0), gy + GY_INST + gy_row(wave), land + L_LAND2);
    }
  }
#undef RS
#define RS(n) "s_memrealtime s[" #n ":" #n "+1]\n\ts_nop 1\n\t"
#define RD(n) "s_sub_i32 s80, s80, s" #n "\n\t"
  asm volatile("s_branch 9f\n.Lmk_wait:\n\t"
               "ds_read_b64 v[62:63], v62\n\ts_waitcnt lgkmcnt(0)\n\t"
               "v_readfirstlane_b32 s81, v63\n\tv_readfirstlane_b32 s82, v62\n\t"
               "s_add_i32 s82, s82, s80\n\ts_mul_i32 s80, s80, s81\n\ts_lshr_b32 s80, s80, 9\n\ts_add_i32 s80, s80, s82\n\t"
               "s_mul_i32 s81, s81, -3\n\ts_addk_i32 s81, 1549\n"
               "1:\n\ts_memrealtime s[98:99]\n\ts_waitcnt lgkmcnt(0)\n\ts_lshl_b32 s98, s98, 3\n\ts_sub_i32 s98, s80, s98\n\t"
               "s_cmpk_gt_i32 s98, 8*20\n\ts_cbranch_scc1 1b\n\ts_cmpk_lt_i32 s98, 8*9\n\ts_cbranch_scc1 3f\n\t"
               RS(84) RS(86) RS(88) RS(90) RS(92) RS(94) RS(96) "s_memrealtime s[98:99]\n\t"
               "s_getpc_b64 s[82:83]\n2:\n\ts_add_u32 s82, s82, 3f-2b\n\ts_addc_u32 s83, s83, 0\n\ts_waitcnt lgkmcnt(0)\n\t"
               RD(84) RD(86) RD(88) RD(90) RD(92) RD(94) RD(96) RD(98)
               "s_mul_i32 s80, s80, s81\n\ts_ashr_i32 s80, s80, 10\n\ts_sub_i32 s80, s80, 100\n\t"
               "s_max_i32 s80, s80, 0\n\ts_min_i32 s80, s80, 4*26\n\ts_andn2_b32 s80, s80, 3\n\t"
               "s_sub_u32 s82, s82, s80\n\ts_subb_u32 s83, s83, 0\n\ts_setpc_b64 s[82:83]\n\t"
               ".rept 26\n\ts_nop 1\n\t.endr\n3:\n\ts_setpc_b64 s[78:79]\n9:"
               :);
  asm volatile(".Lmk_code_end:");
}

}

extern "C" void kernel_launch(void* const* d_in, const int* in_sizes, int n_in, void* d_out, int out_size, void* d_ws,
                              size_t ws_size, hipStream_t stream) {
  Params P;
  P.x0 = (const u16*)d_in[0];
  P.kc = (const u16*)d_in[1];
  P.vc = (const u16*)d_in[2];
  P.wq = (const u16*)d_in[3];
  P.wo = (const u16*)d_in[4];
  P.w1 = (const u16*)d_in[6];
  P.w3 = (const u16*)d_in[7];
  P.w2 = (const u16*)d_in[8];
  P.out = (u16*)d_out;
  P.ws = (u32*)d_ws;
  mega_kernel<<<NWG, NT, 0, stream>>>(P);
}
```

```cpp
#include <hip/hip_runtime.h>
#include <hip/hip_bf16.h>
#include <stdint.h>

namespace {

typedef unsigned int u32;
typedef unsigned short u16;
typedef unsigned long long u64;
using u32x2 = __attribute__((ext_vector_type(2))) unsigned;
using u32x4 = __attribute__((ext_vector_type(4))) unsigned;
using f32x2 = __attribute__((ext_vector_type(2))) float;
using f32x4 = __attribute__((ext_vector_type(4))) float;
using s16x4 = __attribute__((ext_vector_type(4))) short;
using bf16x2 = __attribute__((ext_vector_type(2))) __bf16;
using bf16x8 = __attribute__((ext_vector_type(8))) __bf16;
#define LAS __attribute__((address_space(3)))

constexpr int D = 640, H = 32, DH = 192, WIN = 4096, SK = 32768, F = 1536;
constexpr int NWG = 256, NT = 512;
constexpr u32 SPIN_CAP = 1u << 16;
constexpr float SCL = 0.07216878364870322f * 1.4426950408889634f;

#define T_AO 70
#define T_LAZ0 40
#define B_O 975
#define B_Y 1640
#define B_H 2303
#define CLKO 510.5f
#define T_W2 0
#define W2R 1
#define T_OL 20
#define T_YL 100
#define T_HL 90
#define B_HC 30
#define B_A -185
#define B_E 2780
#define B_T 35
#define PTHR 60
#define T_LAZY 60

#define D_E 800
#define D_Q 425
#define T_O2 100
#define D_Y 1490
#define D_H 575
#define D_F 366
#define A_Q D_Q
#define A_Y (A_Q + D_Y)
#define A_H (A_Y + D_H)
#define A_F (A_H + D_F)

constexpr u32 SIDE = 8192, AREA = 16 * 16384;
constexpr u32 A_GO = 0;
constexpr u32 A_GQ = 4 * 16384;
constexpr u32 A_GH = A_GQ + 7168;
constexpr u32 A_GY = 8 * 16384;
constexpr u32 GO_SZ = 32768, GY_PAIR = 65536, GY_INST = 32768;
constexpr u32 WS_CLR = 4 * AREA;
constexpr u32 WS_PB = WS_CLR;
#define HBS 4352
constexpr u32 WS_GOF = WS_PB + (128 << 10), WS_HB = WS_GOF + 8 * HBS;
__device__ __forceinline__ u32 rp_line(int e, int r, int w) { return WS_PB + (u32)((e * 8 + r) * 8 + w) * 384u; }
#define Q1S 2048
constexpr u32 WS_GQ1 = WS_GOF + (192 << 10);
constexpr u32 WS_END = WS_GQ1 + 256 * 2048;
static_assert(WS_END <= (2u << 20) && WS_CLR == 256 * 4096, "layout");
__device__ __forceinline__ u32 go_slot(int w) { return (u32)(w >> 2) * 16384u + (u32)(w & 3) * 2048u; }
__device__ __forceinline__ u32 gy_row(int i) { return go_slot(i); }
__device__ __forceinline__ u32 gy_chunk(int u) {
  const u32 i = __umul24(u, 205) >> 11;
  return (i >> 2) * 8192u + __umul24(i, 2048 - 160) + (u32)u * 16u;
}
__device__ __forceinline__ u32 st_line(int g) { return A_GH + 768u + (u32)(g >> 1) * 16384u + (u32)(g & 1) * 128u; }
__device__ __forceinline__ u32 gy_rep(int r) { return (u32)(r & 3) * AREA + (u32)(r >> 2) * SIDE; }

constexpr int L_ZERO = 0;
constexpr int L_CNT = 16;
constexpr int L_ONE = 32;
constexpr int L_TAG = 48;
constexpr int L_ST = 64;
constexpr int L_VOTE = L_ST;
constexpr int L_ANC = 192;
constexpr int L_CLK = 200;
constexpr int L_SUM = 208;
constexpr int L_AN0 = 216;
constexpr int L_AR = 224;
constexpr int L_Q = 256;
constexpr int L_CS = 640;
constexpr int L_HB = L_CS + 768;
constexpr int L_ACT = L_HB + D * 2;
constexpr int L_RED = L_ACT + 2 * 384;
constexpr int L_P = L_RED + 32 * 144;
constexpr int P_W = 1280, P_U = 592, P_PAR = 288;
constexpr int OM_S = 416;
constexpr int L_OM = L_P + 8 * P_W;
constexpr int L_LN = L_OM;
constexpr int L_LAND = (L_OM + 24 * OM_S + 512 + 255) / 256 * 256;
constexpr int L_WQ = L_LAND + 8 * 2048;
constexpr int L_WB = L_WQ + 6 * 5120;
constexpr int L_W2 = L_WB + 768 * 64;
constexpr int L_LAND2 = L_WB + 640 * 64 - L_LAND;
constexpr int L_PF = L_W2 + 192 * 64;
constexpr int L_TAB = L_PF + 256;
constexpr int L_TOTAL = L_TAB + 8 * 2048;
static_assert(L_LAND2 % 256 == 0 && L_LAND % 256 == 0 && L_WB % 64 == 0 && L_P % 16 == 0 && L_WQ % 16 == 0 && L_HB % 16 == 0 && L_RED % 16 == 0, "alignment");

struct Params {
  const u16* x0;
  const u16* kc;
  const u16* vc;
  const u16* wq;
  const u16* wo;
  const u16* w1;
  const u16* w3;
  const u16* w2;
  u16* out;
  u32* ws;
};

__device__ __forceinline__ u32 cvt2(float lo, float hi) {
  return __builtin_bit_cast(u32, __builtin_convertvector(f32x2{lo, hi}, bf16x2));
}
__device__ __forceinline__ float bflo(u32 w) { return __uint_as_float(w << 16); }
__device__ __forceinline__ float bfhi(u32 w) { return __uint_as_float(w & 0xffff0000u); }
__device__ __forceinline__ float bf16r(float x) { return bflo(cvt2(x, x)); }

template <int CTRL>
__device__ __forceinline__ float dpp(float x) {
  return __builtin_bit_cast(float, __builtin_amdgcn_mov_dpp(__builtin_bit_cast(int, x), CTRL, 0xf, 0xf, true));
}
constexpr int XOR1 = 0xB1, XOR2 = 0x4E, XOR7 = 0x141, ROR8 = 0x128;
__device__ __forceinline__ float fmx(float a, float b) {
  float r;
  asm("v_max_f32 %0, %1, %2" : "=v"(r) : "v"(a), "v"(b));
  return r;
}
__device__ __forceinline__ float xrow16_max(float x) {
  auto s = __builtin_amdgcn_permlane16_swap(__float_as_uint(x), __float_as_uint(x), false, false);
  x = fmx(__uint_as_float(s[0]), __uint_as_float(s[1]));
  auto t = __builtin_amdgcn_permlane32_swap(__float_as_uint(x), __float_as_uint(x), false, false);
  return fmx(__uint_as_float(t[0]), __uint_as_float(t[1]));
}
__device__ __forceinline__ float xrow16_sum(float x) {
  auto s = __builtin_amdgcn_permlane16_swap(__float_as_uint(x), __float_as_uint(x), false, false);
  x = __uint_as_float(s[0]) + __uint_as_float(s[1]);
  auto t = __builtin_amdgcn_permlane32_swap(__float_as_uint(x), __float_as_uint(x), false, false);
  return __uint_as_float(t[0]) + __uint_as_float(t[1]);
}
__device__ __forceinline__ float sum8(float v) {
  v += dpp<XOR1>(v);
  v += dpp<XOR2>(v);
  v += dpp<XOR7>(v);
  return v;
}
__device__ __forceinline__ float max8(float v) {
  asm("s_nop 1\n\tv_max_f32_dpp %0, %0, %0 quad_perm:[1,0,3,2] row_mask:0xf bank_mask:0xf\n\t"
      "s_nop 1\n\tv_max_f32_dpp %0, %0, %0 quad_perm:[2,3,0,1] row_mask:0xf bank_mask:0xf\n\t"
      "s_nop 1\n\tv_max_f32_dpp %0, %0, %0 row_half_mirror row_mask:0xf bank_mask:0xf"
      : "+v"(v));
  return v;
}
__device__ __forceinline__ float max_ror8(float v) {
  asm("s_nop 1\n\tv_max_f32_dpp %0, %0, %0 row_ror:8 row_mask:0xf bank_mask:0xf" : "+v"(v));
  return v;
}
__device__ __forceinline__ float wave_sum(float v) {
  v = sum8(v);
  v += dpp<ROR8>(v);
  return xrow16_sum(v);
}
__device__ __forceinline__ float tsum8(f32x4 a) {
  auto s0 = __builtin_amdgcn_permlane32_swap(__float_as_uint(a[0]), __float_as_uint(a[1]), false, false);
  auto s1 = __builtin_amdgcn_permlane32_swap(__float_as_uint(a[2]), __float_as_uint(a[3]), false, false);
  const float r0 = __uint_as_float(s0[0]) + __uint_as_float(s0[1]);
  const float r1 = __uint_as_float(s1[0]) + __uint_as_float(s1[1]);
  auto t = __builtin_amdgcn_permlane16_swap(__float_as_uint(r0), __float_as_uint(r1), false, false);
  float r = __uint_as_float(t[0]) + __uint_as_float(t[1]);
  r += dpp<ROR8>(r);
  return r;
}

__device__ __forceinline__ u32 now() { return (u32)__builtin_amdgcn_s_memtime(); }
__device__ __forceinline__ void wait_until(u32 t) {
#pragma unroll 1
  while ((int)(now() - t) < -160) __builtin_amdgcn_s_sleep(1);
#pragma unroll 1
  while ((int)(now() - t) < 0) {
  }
}
__device__ __forceinline__ u32 rtnow() { return (u32)__builtin_amdgcn_s_memrealtime(); }
template <int AN = L_ANC>
__device__ __forceinline__ void wait_tt(u32 a) {
  asm volatile("s_mov_b32 s80, %0\n\tv_mov_b32 v62, %1\n\ts_call_b64 s[78:79], .Lmk_wait"
               :
               : "is"(a), "n"(AN)
               : "s78", "s79", "s80", "s81", "s82", "s83", "s84", "s85", "s86", "s87", "s88", "s89", "s90", "s91", "s92", "s93", "s94",
                 "s95", "s96", "s97", "s98", "s99", "v62", "v63", "scc", "memory");
}
__device__ __forceinline__ void lds_wait(volatile LAS u32* p, u32 want) {
  u32 spins = 0;
#pragma unroll 1
  while (__builtin_amdgcn_readfirstlane(*p) != want) {
    if (++spins > (SPIN_CAP << 6)) break;
    __builtin_amdgcn_s_sleep(1);
  }
  asm volatile("" ::: "memory");
}
__device__ __forceinline__ void lds_add(unsigned char* smem, int off, u32 v) {
  __hip_atomic_fetch_add((LAS u32*)(smem + off), v, __ATOMIC_RELAXED, __HIP_MEMORY_SCOPE_WORKGROUP);
}

__device__ __forceinline__ int fresh(int t) {
  asm volatile("" : "+v"(t));
  return t;
}
__device__ __forceinline__ int fresh6(int t) {
  t = fresh(t);
  __builtin_assume((unsigned)t < 64u);
  return t;
}
__device__ __forceinline__ int lane_id() {
  int t;
  asm volatile("v_mbcnt_lo_u32_b32 %0, -1, 0\n\tv_mbcnt_hi_u32_b32 %0, -1, %0" : "=v"(t));
  __builtin_assume((unsigned)t < 64u);
  return t;
}
__device__ __forceinline__ f32x4 zero4() {
  f32x2 a, b;
  asm volatile("v_mov_b64 %0, 0\n\tv_mov_b64 %1, 0" : "=v"(a), "=v"(b));
  return f32x4{a[0], a[1], b[0], b[1]};
}
__device__ __forceinline__ void bar() { asm volatile("s_waitcnt lgkmcnt(0)\n\ts_barrier" ::: "memory"); }

__device__ __forceinline__ u32x2 trread(unsigned char* smem, int off) {
  return __builtin_bit_cast(u32x2, __builtin_amdgcn_ds_read_tr16_b64_v4i16((LAS s16x4*)(smem + off)));
}
__device__ __forceinline__ u32x4 wfrag(unsigned char* smem, int lb, int off) {
  const u32x2 a = trread(smem, lb + off), b = trread(smem, lb + off + 256);
  return u32x4{a[0], a[1], b[0], b[1]};
}
__device__ __forceinline__ int wlane(int lane, int cb) {
  const int i = lane & 15, G = lane >> 4;
  return (8 * G + (i >> 2)) * 64 + (i & 3) * 8 + ((cb ^ (G & 1)) * 32);
}
__device__ __forceinline__ u32x4 afrag(unsigned char* smem, int a) { return *reinterpret_cast<const u32x4*>(smem + a); }
__device__ __forceinline__ f32x4 mfma32(u32x4 w, u32x4 a, f32x4 c) {
  return __builtin_amdgcn_mfma_f32_16x16x32_bf16(__builtin_bit_cast(bf16x8, w), __builtin_bit_cast(bf16x8, a), c, 0, 0, 0);
}

__device__ __forceinline__ void dma_piece(const u16* src, int ld, unsigned char* smem, int ldsoff, int p, int lane) {
  const int row = 16 * p + (lane >> 2), part = lane & 3;
  const int cb = (part >> 1) ^ (lane >> 5);
  const u32 voff = (u32)(row * ld + cb * 16 + (part & 1) * 8) * 2u;
  const u32 dst = (u32)(uintptr_t)(LAS unsigned char*)smem + (u32)(ldsoff + p * 1024);
  u32 keep;
  asm volatile("s_mov_b32 %0, m0\n\ts_mov_b32 m0, %2\n\ts_nop 2\n\tglobal_load_lds_dwordx4 %1, %3 nt\n\ts_mov_b32 m0, %0"
               : "=&s"(keep)
               : "v"(voff), "s"(dst), "s"(src)
               : "memory");
}

__device__ __forceinline__ float sum4(f32x4 d) { return (d[0] + d[1]) + (d[2] + d[3]); }
template <int AUX = 16>
__device__ __forceinline__ void store16(__amdgpu_buffer_rsrc_t rs, u32 off, u32x4 v) {
  __builtin_amdgcn_raw_buffer_store_b128(v, rs, off, 0, AUX);
}

struct G80 {
  u32x4 w[6];
  int ra, rb, rc, l0, l2;
};
__device__ __forceinline__ void g80_load(G80& a, unsigned char* smem) {
  a.w[0] = wfrag(smem, a.l0, 0);
  a.w[1] = wfrag(smem, a.l0 ^ 32, 0);
  a.w[2] = wfrag(smem, a.l0, 2048);
  a.w[3] = wfrag(smem, a.l0 ^ 32, 2048);
  a.w[4] = wfrag(smem, a.l2, 0);
  a.w[5] = wfrag(smem, a.l2 ^ 32, 0);
}
__device__ __forceinline__ void g80_addr(G80& a, int lane, int wb, int xs, int land, int one) {
  const int m = lane & 15, G = lane >> 4;
  a.l0 = fresh(wb + wlane(lane, 0));
  a.l2 = fresh(a.l0 + 4096 - (G >> 1) * 1024);
  const bool vec = m <= 8;
  const int st = vec ? 64 : 0;
  const int sa = vec ? (m == 8 ? xs : land + 256 * m) + 16 * G : (m == 15 ? one : L_ZERO);
  const int sw = m < 8 ? (m & 3) * 64 : 0;
  a.ra = fresh(sa ^ sw);
  a.rb = fresh((sa + st) ^ sw);
  a.rc = fresh(G < 2 ? (sa + 2 * st) ^ sw : L_TAG);
}
template <bool X4, bool NT = false>
__device__ __forceinline__ void ctl_load(u32x4 rq, u32 voff, u32 lds) {
  u32 keep;
  if (NT)
    asm volatile("s_mov_b32 %0, m0\n\ts_mov_b32 m0, %3\n\ts_nop 2\n\tbuffer_load_dwordx4 %1, %2, 0 offen sc1 nt lds\n\ts_mov_b32 m0, %0"
                 : "=&s"(keep) : "v"(voff), "s"(rq), "s"(lds) : "memory");
  else if (X4)
    asm volatile("s_mov_b32 %0, m0\n\ts_mov_b32 m0, %3\n\ts_nop 2\n\tbuffer_load_dwordx4 %1, %2, 0 offen sc1 lds\n\ts_mov_b32 m0, %0"
                 : "=&s"(keep) : "v"(voff), "s"(rq), "s"(lds) : "memory");
  else
    asm volatile("s_mov_b32 %0, m0\n\ts_mov_b32 m0, %3\n\ts_nop 2\n\tbuffer_load_dword %1, %2, 0 offen sc1 lds\n\ts_mov_b32 m0, %0"
                 : "=&s"(keep) : "v"(voff), "s"(rq), "s"(lds) : "memory");
}
__device__ __forceinline__ u32 stamp_poll(unsigned char* smem, u32x4 rq, u32 so, u32 te) {
  u32 sv, budget = SPIN_CAP;
#pragma unroll 1
  for (;;) {
    asm volatile("buffer_load_dword %0, %1, %2, 0 offen sc1\n\ts_waitcnt vmcnt(0)" : "=&v"(sv) : "v"(so), "s"(rq) : "memory");
    if (__builtin_amdgcn_ballot_w64((sv & 1u) == 0u) == 0ull || budget == 0u) break;
    --budget;
    __builtin_amdgcn_s_sleep(1);
  }
  const u32 nw = te + 0x800000u;
  const float age = (float)((nw - (sv >> 1)) & 0xffffffu);
  const u32 an = nw - (u32)-max8(-age);
  *(volatile LAS u32x2*)(smem + L_ANC) = u32x2{an, (u32)max8((float)(sv >> 25))};
  return __builtin_amdgcn_readfirstlane(an);
}
#define MF "v_mfma_f32_16x16x32_bf16 "
#define F0 "v[52:55]"
#define F1 "v[56:59]"
#define F2 "v[60:63]"
#define V52 "v52", "v53", "v54", "v55", "v56", "v57", "v58", "v59", "v60", "v61", "v62", "v63"
#define V40 "v40", "v41", "v42", "v43", "v44", "v45", "v46", "v47", "v48", "v49", "v50", "v51"
#define V36 "v36", "v37", "v38", "v39"
#define V32 "v32", "v33", "v34", "v35"
#define S80 "s80", "s81", "s82", "s83", "s84", "s85", "s86", "s87", "s88", "s89", "s90", "s91", "vcc", "scc", "memory"
#define AND3(a, b, c) "v_bitop3_b32 %[t], " a ", " b ", " c " bitop3:0x80\n\t"
#define ALL " row_mask:0xf bank_mask:0xf\n\t"
#define Q1 " quad_perm:[1,0,3,2]" ALL
#define Q2 " quad_perm:[2,3,0,1]" ALL
#define HM " row_half_mirror" ALL
#define R8 " row_ror:8" ALL
#define PK(a, b) "v_pk_add_f32 v[" #a ":" #a "+1], v[" #a ":" #a "+1], v[" #b ":" #b "+1]\n\t"
#define CV "v_cvt_pk_bf16_f32 "
constexpr u32 OOB = 0x80000000u;
#define POLL_HEAD                                                                                                     \
  "s_mov_b32 m0, %[land]\n\ts_mov_b32 s88, 0x10000\n\ts_mov_b32 s80, 0xff00ff\n\ts_mov_b32 s81, s80\n\t"               \
  "s_mov_b64 s[84:85], s[82:83]\n\ts_mov_b64 s[86:87], s[82:83]\n\t"
#define POLL_MASK                                                                                                     \
  "s_sub_u32 s88, s88, 1\n\ts_cbranch_scc1 3f\n\ts_or_b32 s84, vcc_lo, vcc_hi\n\ts_lshr_b32 s85, s84, 16\n\t"          \
  "s_or_b32 s84, s84, s85\n\ts_lshr_b32 s85, s84, 8\n\ts_or_b32 s84, s84, s85\n\t"                                     \
  "s_bitreplicate_b64_b32 s[84:85], s84\n\ts_bitreplicate_b64_b32 s[84:85], s84\n\t"                                   \
  "s_bitreplicate_b64_b32 s[84:85], s84\n\ts_bitreplicate_b64_b32 s[86:87], s85\n\t"                                   \
  "s_bitreplicate_b64_b32 s[84:85], s84\n\ts_and_b64 s[84:85], s[84:85], s[82:83]\n\t"                                 \
  "s_and_b64 s[86:87], s[86:87], s[82:83]\n\t"
#define POLL_LOAD                                                                                                     \
  "s_mov_b64 exec, s[84:85]\n\tbuffer_load_dwordx4 %[vo], %[rq], 0 offen sc1 lds\n\t"                                  \
  "s_mov_b64 exec, s[86:87]\n\tbuffer_load_dwordx4 %[vo], %[rq], 0 offen offset:1024 sc1 lds\n\ts_mov_b64 exec, -1\n\t"
#define POLL_READ "s_waitcnt vmcnt(0)\n\t" POLL_RD
#define POLL_RD \
  "ds_read_b128 " F0 ", %[ra]\n\tds_read_b128 " F1 ", %[rb]\n\tds_read_b128 " F2 ", %[rc]\n\t"
#define USED10 "s_mov_b32 s82, 0x30ff03ff\n\ts_mov_b32 s83, 0xff30ff03\n\t"
#define POLL_FIRST                                                                                           \
  "s_mov_b32 m0, %[land]\n\ts_mov_b32 exec_lo, 0x30ff03ff\n\ts_mov_b32 exec_hi, 0xff30ff03\n\t"               \
  "s_waitcnt lgkmcnt(0)\n\ts_barrier\n\t"                                                                    \
  "buffer_load_dwordx4 %[vo], %[rq], 0 offen sc1 lds\n\tbuffer_load_dwordx4 %[vo], %[rq], 0 offen offset:1024 sc1 lds\n\t" \
  "s_mov_b64 exec, -1\n\t"
#define WIN6(a) [w0] "v"(a[0]), [w1] "v"(a[1]), [w2] "v"(a[2]), [w3] "v"(a[3]), [w4] "v"(a[4]), [w5] "v"(a[5])
__device__ __forceinline__ f32x4 quad4(float r) { return f32x4{r, dpp<0x55>(r), dpp<0xAA>(r), dpp<0xFF>(r)}; }

__device__ __forceinline__ int q_addr(int d) {
  const int w = d >> 1;
  return L_Q + ((w & 1) * 48 + (w >> 1)) * 4 + (d & 1) * 2;
}

__device__ __forceinline__ void q1_finish(unsigned char* smem, int lane, int wave) {
  const int e = wave * 32 + (lane & 7) * 4 + ((lane >> 4) & 1) * 2 + (lane >> 5);
  asm volatile("" ::: "memory");
  const u32x4 v = afrag(smem, L_LAND + wave * 2048 + lane * 16);
  const float t = tsum8(f32x4{bflo(v[0]), bflo(v[1]), bflo(v[2]), bflo(v[3])});
  *reinterpret_cast<u16*>(smem + q_addr(e)) = (u16)cvt2(t, t);
  *reinterpret_cast<float*>(smem + L_CS + e * 4) = tsum8(f32x4{bfhi(v[0]), bfhi(v[1]), bfhi(v[2]), bfhi(v[3])}) * (-SCL / D);
}
__device__ __forceinline__ void x_request(u32x4 rq, int lane, u32 src, int land) {
  asm volatile("s_mov_b32 m0, %2\n\ts_nop 0\n\tbuffer_load_dwordx4 %0, %1, 0 offen sc1 lds\n\t"
               "buffer_load_dwordx4 %0, %1, 0 offen offset:1024 sc1 lds"
               :
               : "v"(src + ((lane * 16) ^ ((lane >> 4) * 64))), "s"(rq), "s"(land)
               : "memory");
}
template <bool POLL>
__device__ __forceinline__ void x_update(unsigned char* smem, u32x4 rq, int lane, u32 src, int land, int i) {
  const int l = lane < 40 ? lane : 0;
  const u32 off = land + 4 * l;
  u32 v[8];
  u32 budget = SPIN_CAP;
#pragma unroll 1
  for (;;) {
    if (POLL) asm volatile("s_waitcnt vmcnt(0)" ::: "memory");
#pragma unroll
    for (int k = 0; k < 8; ++k) v[k] = *reinterpret_cast<const u32*>(smem + (off ^ ((k & 3) * 64)) + 256 * k);
    const u32 a = v[0] & v[1] & v[2] & v[3] & v[4] & v[5] & v[6] & v[7] & 1u;
    if (!POLL || __builtin_amdgcn_ballot_w64(a == 0u) == 0ull || budget == 0u) break;
    --budget;
    x_request(rq, lane, src, land);
  }
  float lo = 0.f, hi = 0.f;
#pragma unroll
  for (int k = 0; k < 8; ++k) {
    lo += bflo(v[k]);
    hi += bfhi(v[k]);
  }
  u32* px = reinterpret_cast<u32*>(smem + L_HB + 160 * i + 4 * l);
  const u32 xo = *px, y = cvt2(lo, hi);
  if (lane < 40) *px = cvt2(bflo(xo) + bflo(y), bfhi(xo) + bfhi(y));
}
template <int N>
__device__ __forceinline__ void col_poll(u32x4 rq, const u32 (&off)[N], float (&y)[N]) {
  u32x2 v[N];
  u32 budget = SPIN_CAP;
#pragma unroll 1
  for (;;) {
    u32 a = 1u;
#pragma unroll
    for (int k = 0; k < N; ++k)
      asm volatile("buffer_load_dwordx2 %0, %1, %2, 0 offen sc1" : "=v"(v[k]) : "v"(off[k]), "s"(rq) : "memory");
    asm volatile("s_waitcnt vmcnt(0)" ::: "memory");
#pragma unroll
    for (int k = 0; k < N; ++k) {
      asm volatile("" : "+v"(v[k]));
      a &= v[k][0] & v[k][1];
    }
    if (__builtin_amdgcn_ballot_w64(a == 0u) == 0ull || budget == 0u) break;
    --budget;
  }
#pragma unroll
  for (int k = 0; k < N; ++k) y[k] = bf16r(tsum8(f32x4{bflo(v[k][0]), bfhi(v[k][0]), bflo(v[k][1]), bfhi(v[k][1])}));
}

#define SJ 31
#define DWIN 250
#define DEQ 0.9f
#define PFB 16
#define AUXQ 18
#define UQ 1
#define UAD 4
#define UB 3
#define UL 5
#define UG 6
#define UK 7
#define UC 8
#define UW 8
#define UF 10
#define UX 11
#define TSTEP 240
#define TSV 236
#define USW 4
#define TSQ 192
#define WOFS 21
#define WBK 60
#define NINO 4
#define NINF 1
#define NINS 2
#define ORDO 0x543210u
#define ORDF 0x34210u
#define NPRE 2
#define NPF 2
#define TPF 220
#define T_FW 10
#define T_FD 12
#define WU0 3
#define WSP 2
#define WPC 100
#define NUN 1
static_assert(NINS + NPF < 5 && NINS <= NINO && NINO + NPRE == 6 && NINF + NPF < 5 && NPF == NPRE, "counts");
static_assert(WU0 + NINO * WSP <= UX + 1, "pieces before the scores");
#define TPV 220
#define TSE TSV
constexpr int wcnt(int u) { return u < WU0 || u >= WU0 + NINO * WSP ? 0 : (20 + ((u - WU0) / WSP < NINF ? 12 : ((u - WU0) / WSP < NINS ? 2 : 0))) / WSP; }
#define PQ(u) ((((u) < USW ? TSTEP : TSV) * (9600 + wcnt(u) * WPC)) / 12800)
#define ROW0 ((wave * 8 + c) * 8)
#define GST 512
#define ROT h
#define PACE_ASM                                                                                                     \
  "1:\n\ts_memrealtime s[98:99]\n\ts_waitcnt lgkmcnt(0)\n\ts_lshl_b32 s98, s98, 3\n\ts_sub_i32 s98, s98, %[tn]\n\t"  \
  "s_cmp_lt_i32 s98, 0\n\ts_cbranch_scc0 2f\n\ts_sleep 1\n\ts_branch 1b\n2:\n\ts_add_i32 %[tn], %[tn], %[ts]\n\t"
__device__ __forceinline__ void pace(u32& tnext, u32 tstep) {
  asm volatile(PACE_ASM : [tn] "+s"(tnext) : [ts] "s"(tstep) : "scc", "s98", "s99");
}
constexpr int kreg(int i) { return i % 3 < 2 ? 64 + 8 * (i / 3) + 4 * (i % 3) : 128 + 4 * (i / 3); }
constexpr int vreg(int i) { return 160 + 24 * (i / 6) + 8 * (i % 3) + 4 * ((i / 3) & 1); }
constexpr int treg(int t) { return t < 24 ? kreg(t) : vreg(t - 24); }
template <int V>
struct Cn {
  static constexpr int v = V;
};
template <int A, int B, class Fn>
__device__ __forceinline__ void slots(Fn& f) {
  if constexpr (A < B) {
    f(Cn<A>{});
    slots<A + 1, B>(f);
  }
}
#define PRT 200
#define STR2(x) #x
#define STR(x) STR2(x)
#define PRIO_ASM                                                                                                     \
  "s_setprio 0\n\ts_cmpk_lt_i32 s98, " STR(PRT) "\n\ts_cbranch_scc1 4f\n\ts_setprio 1\n\ts_cmpk_lt_i32 s98, 3*" STR(PRT) "\n\t"  \
  "s_cbranch_scc1 4f\n\ts_setprio 2\n\ts_cmpk_lt_i32 s98, 6*" STR(PRT) "\n\ts_cbranch_scc1 4f\n\ts_setprio 3\n4:\n\t"
#define LD1(r) "global_load_dwordx4 v[%[" #r "]:%[" #r "]+3], %[p], %[sb] offset:"
#define PACE_B PACE_ASM
#define BURST(u, voff, sbase)                                                                                       \
  asm volatile(PACE_B PRIO_ASM LD1(a) "0 nt\n\t" LD1(b) "128 nt\n\t" LD1(c) "256 nt\n\t"                                    \
               "s_waitcnt vmcnt(%[w]+2)\n\ts_cmp_gt_u32 %[ex], 1\n\ts_cbranch_scc1 3f\n\t"                          \
               "s_waitcnt vmcnt(%[w]+1)\n\ts_cmp_gt_u32 %[ex], 0\n\ts_cbranch_scc1 3f\n\ts_waitcnt vmcnt(%[w])\n3:"  \
               : [tn] "+s"(tnext)                                                                                   \
               : [a] "n"(treg(3 * (u))), [b] "n"(treg(3 * (u) + 1)), [c] "n"(treg(3 * (u) + 2)), [p] "v"(voff),     \
                 [sb] "s"(sbase), [ts] "s"(tstep), [ex] "s"(exa), [w] "n"(6)                                        \
               : "scc", "s96", "s97", "s98", "s99");                                                                              \
  exa = exl;                                                                                                        \
  exl = 0u
#define LOADW(t, voff, sbase)                                                                                       \
  asm volatile(PACE_ASM PRIO_ASM LD1(a) "%[im] nt\n\ts_waitcnt vmcnt(9)"                                                    \
               : [tn] "+s"(tnext)                                                                                   \
               : [a] "n"(treg(t)), [p] "v"(voff), [sb] "s"(sbase), [im] "n"(128 * ((t) % 3)), [ts] "s"(tstep)       \
               : "scc", "s98", "s99")

#define HS_WAIT(COND)                                                                                \
  _Pragma("unroll 1") for (;;) {                                                                     \
    gd = CTL[512 + lane0] - CTL[576 + lane0];                                                        \
    const u64 ch = __builtin_amdgcn_ballot_w64(gd != 0u) & 0x1ffull;                                 \
    if (ch > 0xffull || (COND) || tmo == 0u) break;                                                  \
    if ((tmo & 3u) == 0u && lane0 == 0 && cnt[1] >= 7u)                                              \
      __builtin_amdgcn_raw_buffer_store_b32(hb, rs, WS_HB + h * HBS + c * 4, 0, 16);                 \
    hb += 4u;                                                                                        \
    --tmo;                                                                                           \
    __builtin_amdgcn_s_sleep(3);                                                                     \
    ctl_load<false>(rq, hso, (u32)(smb + L_CTL + 2048));                                             \
    asm volatile("s_waitcnt vmcnt(0)" ::: "memory");                                                 \
  }

struct Att {
  int qad[6], idxq, wa, ra, idx, oa, sa, la, ma, qw;
  u32 dst;
};
template <int K>
__device__ __forceinline__ void att_table(unsigned char* smem, int lane, int wave, u32 go) {
  const int n = lane & 15, G = lane >> 4;
  const int seg = n & 1, pr = (n >> 1) & 1, tq = n >> 2, xy = G >> 1;
  const int qa = L_Q + (pr * 48 + seg * 8 + (G & 1) * 4) * 4;
  u32 t[16];
#pragma unroll
  for (int k = 0; k < 4; ++k) t[k] = tq == k ? qa + xy * 64 : L_ZERO;
#pragma unroll
  for (int k = 0; k < 2; ++k) t[4 + k] = tq == 2 * k + xy ? qa + 128 : L_ZERO;
  t[6] = 0x4444 + pr * 0xAAAA;
  t[7] = L_P + wave * P_W + (n & 1) * P_PAR + ((((G & 1) * 2 + ((n >> 2) & 1)) * 4 + (n >> 3) * 2 + (G >> 1)) * 16) + (n & 2) * 4;
  t[8] = L_P + wave * P_W + ((n >> 2) & 1) * P_U + (n >> 3) * P_PAR + G * 64;
  t[9] = (n & 3) * 0x5555;
  t[10] = L_OM + (wave * 2 + (n >> 3)) * OM_S + (n & 7) * 16 + (G & 1) * 8;
  t[11] = L_OM + (16 + (wave >> 2) * 8) * OM_S + (wave & 3) * 128 + lane * 2;
  t[12] = wave == 3 ? L_OM + 16 * OM_S + lane * 8 : L_OM + ((G & 1) * 4 + tq) * OM_S + wave * 128 + xy * 32 + (n & 3) * 8;
  t[13] = wave == 3 || n == xy ? L_ONE : L_ZERO;
  const int d0 = wave * 64 + n * 16 + G * 4;
  t[14] = (go + (wave < 3 ? (d0 >= 96 ? 2048 - 192 : 0) + d0 * 2 : (lane & 1) * 2048 + 192 + xy * 8)) & 0xffffu;
  t[15] = wave < 6 ? q_addr(wave * 32 + (lane & 7) * 4 + ((lane >> 4) & 1) * 2 + (lane >> 5)) : L_SUM;
  *reinterpret_cast<u32x4*>(smem + L_TAB + wave * 2048 + K * 1024 + lane * 16) =
      u32x4{t[8 * K] | (t[8 * K + 4] << 16), t[8 * K + 1] | (t[8 * K + 5] << 16), t[8 * K + 2] | (t[8 * K + 6] << 16),
            t[8 * K + 3] | (t[8 * K + 7] << 16)};
}
__device__ __forceinline__ void att_setup(Att& r, unsigned char* smem, int lane, int wave) {
  const u32x4 a = afrag(smem, L_TAB + wave * 2048 + lane * 16), b = afrag(smem, L_TAB + wave * 2048 + 1024 + lane * 16);
  r.qw = fresh(b[3] >> 16);
#pragma unroll
  for (int k = 0; k < 4; ++k) r.qad[k] = fresh(a[k] & 0xffffu);
  r.qad[4] = fresh(a[0] >> 16);
  r.qad[5] = fresh(a[1] >> 16);
  r.idxq = fresh(a[2] >> 16);
  r.wa = fresh(a[3] >> 16);
  r.ra = fresh(b[0] & 0xffffu);
  r.idx = fresh(b[1] & 0xffffu);
  r.oa = fresh(b[2] & 0xffffu);
  r.la = fresh(b[3] & 0xffffu);
  r.sa = fresh(b[0] >> 16);
  r.ma = fresh(b[1] >> 16);
  r.dst = fresh((wave < 3 ? lane & 12 : ~lane & 30) ? OOB : b[2] >> 16);
}
#define SM "v_smfmac_f32_16x16x64_bf16 "
#define QNOP "s_nop 0\n\t"
#define OEARLY 0xff
#define SAFEB "0x42200000"

using u32x16 = __attribute__((ext_vector_type(16))) unsigned;
using u32x32 = __attribute__((ext_vector_type(32))) unsigned;
template <int OFF>
__device__ __forceinline__ u16* karg() {
  u64 p;
  asm volatile("s_load_dwordx2 %0, %1, %2\n\ts_waitcnt lgkmcnt(0)" : "=s"(p) : "s"(__builtin_amdgcn_kernarg_segment_ptr()), "n"(OFF));
  return (u16*)p;
}
template <class T>
__device__ __forceinline__ T* mkptr(u32 lo, u32 hi) {
  return (T*)(((u64)hi << 32) | lo);
}

__global__ __launch_bounds__(NT, 2) void mega_kernel(Params) {
  __shared__ __attribute__((aligned(16))) unsigned char smem[L_TOTAL];
  u32x16 ka;
  u32x4 kb4;
  u32 rtt;
  u64 ts64;
  {
    static_assert(AREA == 1 << 18 && A_GY == 1 << 17 && SIDE == 1 << 13, "clear addresses");
    asm volatile("s_load_dwordx16 s[36:51], %[kp], 0x0\n\ts_load_dwordx4 s[52:55], %[kp], 0x40\n\t"
                 "s_getpc_b64 s[58:59]\n.Lmk_pc:\n\t"
                 "s_mov_b32 s62, m0\n\ts_mov_b32 m0, %[dst]\n\t"
                 "s_cmpk_lt_u32 %[b], %[pfb]\n\ts_cselect_b32 s57, 0x140, 0\n\t"
                 "v_lshlrev_b32 v1, 7, %[tid]\n\tv_min_u32 v1, .Lmk_code_end-.Lmk_pc-4, v1\n\t"
                 "v_cmp_gt_u32 vcc, s57, %[tid]\n\ts_mov_b64 exec, vcc\n\t"
                 "global_load_lds_dword v1, s[58:59]\n\ts_mov_b64 exec, -1\n\t"
                 "v_readfirstlane_b32 s56, %[tid]\n\t"
                 "v_lshrrev_b32 v1, 2, %[tid]\n\tv_cmp_eq_u32 vcc, 3, v1\n\tv_mov_b32 v3, 0x3f803f80\n\ts_lshr_b32 s57, %[b], 5\n\t"
                 "v_cndmask_b32 v2, 0, 1, vcc\n\tv_cmp_eq_u32 vcc, 2, v1\n\tv_lshlrev_b32 v1, 2, %[tid]\n\ts_bfe_u32 s60, %[b], 0x20003\n\t"
                 "v_cndmask_b32 v2, v2, v3, vcc\n\tv_cmp_eq_u32 vcc, 52, %[tid]\n\tv_mov_b32 v3, %[scl]\n\t"
                 "v_min_u32 v1, 0xfc, v1\n\tv_cndmask_b32 v2, v2, v3, vcc\n\t"
                 "ds_write_b32 v1, v2 offset:%[sm]\n\t"
                 "v_lshlrev_b32 v2, 4, %[tid]\n\tv_and_b32 v1, 0x3ff, v2\n\t"
                 "s_cmpk_ge_u32 s56, 448\n\ts_cbranch_scc1 1f\n\t"
                 "s_mul_i32 s57, s57, 0xf0000\n\ts_and_b32 s61, %[b], 7\n\ts_lshl2_add_u32 s60, s61, s60\n\t"
                 "v_bfe_u32 v3, %[tid], 2, 4\n\ts_mul_i32 s60, s60, 0x180\n\tv_mul_u32_u24 v3, 0x3000, v3\n\t"
                 "s_add_u32 s57, s57, s56\n\tv_bitop3_b32 v2, v2, %[tid], 32 bitop3:0x78\n\ts_add_u32 s57, s57, s60\n\t"
                 "v_and_or_b32 v3, v2, 48, v3\n\ts_mul_i32 s60, s56, 80\n\tv_add_u32 v3, s57, v3\n\t"
                 "s_add_u32 s60, s60, %[lwq]\n\ts_cmpk_eq_u32 s56, 384\n\ts_cselect_b64 vcc, -1, 0\n\ts_cselect_b32 m0, %[lhb], s60\n\t"
                 "v_cndmask_b32 v3, v3, v1, vcc\n\ts_mov_b32 %[rtt], 0\n\t"
                 "s_waitcnt lgkmcnt(0)\n\ts_cselect_b64 s[58:59], s[36:37], s[42:43]\n\ts_barrier\n\t"
                 "global_load_lds_dwordx4 v3, s[58:59] nt\n\t"
                 "s_memrealtime %[ts]\n\ts_branch 2f\n"
                 "1:\n\ts_lshl_b32 s57, s57, 17\n\ts_lshl_b32 s60, s60, 13\n\ts_and_b32 s61, %[b], 15\n\ts_or_b32 s57, s57, s60\n\t"
                 "s_lshl_b32 s61, s61, 9\n\t"
                 "v_mov_b64 v[4:5], 0\n\ts_or_b32 s57, s57, s61\n\tv_mov_b64 v[6:7], 0\n\t"
                 "v_or_b32 v1, s57, v1\n\tv_xor_b32 v2, 0x2000, v1\n\ts_mov_b32 exec_hi, 0\n\t"
                 "s_waitcnt lgkmcnt(0)\n\t"
                 "s_add_u32 s58, s54, 0x1fffff\n\ts_addc_u32 s59, s55, 0\n\ts_and_b32 s58, s58, 0xffe00000\n\t"
                 "global_store_dwordx4 v1, v[4:7], s[58:59] sc1\n\tglobal_store_dwordx4 v2, v[4:7], s[58:59] sc1\n\t"
                 "s_barrier\n\ts_mov_b64 exec, -1\n\ts_memrealtime %[ts]\n\t"
                 "s_waitcnt vmcnt(1)\n\ts_memtime s[58:59]\n\ts_waitcnt vmcnt(0)\n\ts_memtime s[60:61]\n\ts_waitcnt lgkmcnt(0)\n\t"
                 "s_sub_u32 %[rtt], s60, s58\n"
                 "2:\n\ts_mov_b32 m0, s62\n\ts_waitcnt lgkmcnt(0)"
                 : "=&{s[36:51]}"(ka), "=&{s[52:55]}"(kb4), [rtt] "=&s"(rtt), [ts] "=&s"(ts64)
                 : [kp] "s"(__builtin_amdgcn_kernarg_segment_ptr()), [tid] "v"(threadIdx.x), [b] "s"(blockIdx.x), [dst] "n"(L_PF),
                   [scl] "n"(__builtin_bit_cast(u32, SCL)), [sm] "n"(0), [lwq] "n"(L_WQ), [lhb] "n"(L_HB), [pfb] "n"(PFB)
                 : "memory", "vcc", "scc", "v1", "v2", "v3", "v4", "v5", "v6", "v7", "s56", "s57", "s58", "s59", "s60", "s61", "s62");
  }
  const u32 tstart = (u32)ts64;
  const int tid = threadIdx.x, lane0 = tid & 63;
  const int wave = __builtin_amdgcn_readfirstlane(tid >> 6);
  const int b = blockIdx.x;
  const int g = b & 7, j = b >> 3, hsub = j & 3, c = j >> 2, h = g * 4 + hsub;
  const bool isO = j < 20;
  const bool leader = b == 0 && wave == 6;
  const u32 fslot = (u32)((j & 1) * 16384 + ((j - 20) >> 1) * 128);
  u32 tmo = SPIN_CAP;
  volatile LAS u32* cnt = (volatile LAS u32*)(smem + L_CNT);
  Params P;
  P.x0 = mkptr<const u16>(ka[0], ka[1]);
  P.kc = mkptr<const u16>(ka[2], ka[3]);
  P.vc = mkptr<const u16>(ka[4], ka[5]);
  P.wq = mkptr<const u16>(ka[6], ka[7]);
  P.wo = mkptr<const u16>(ka[8], ka[9]);
  P.w1 = mkptr<const u16>(ka[10], ka[11]);
  P.w3 = mkptr<const u16>(ka[12], ka[13]);
  P.ws = mkptr<u32>(kb4[2], kb4[3]);
  u32* const wsb = (u32*)(((uintptr_t)P.ws + 0x1fffffu) & ~(uintptr_t)0x1fffffu);
  const __amdgpu_buffer_rsrc_t rs = __builtin_amdgcn_make_buffer_rsrc(wsb, 0, (int)WS_END, 0x00020000);
  const u32x4 rq = {(u32)(uintptr_t)wsb, (u32)((uintptr_t)wsb >> 32) & 0xffffu, WS_END, 0x00020000u};

  u32 to2;
  u32 tref = 0u, ar = 0u;
  u32x32 kv0, kv1, kv2, kv3, kv4, kv5;
  asm volatile(""
               : "={v[64:95]}"(kv0), "={v[96:127]}"(kv1), "={v[128:159]}"(kv2), "={v[160:191]}"(kv3), "={v[192:223]}"(kv4),
                 "={v[224:255]}"(kv5));
  const int smb = (int)(uintptr_t)(LAS unsigned char*)smem;
  const u32 gop = A_GO + go_slot(hsub * 2) + c * 256;
  u32 tstep = 4 * PQ(0);
  u32 tnext = tstart * 8u + 5 * TSQ + (u32)(hsub * PQ(0) + wave * WOFS);
  const size_t rowbase = (size_t)h * SK + (SK - WIN) + ROW0;
  const u16* vb = P.vc + rowbase * DH;
  const int rot = ROT;
  const u16* ws0 = isO ? P.wo + (size_t)(768 * g) * D + 32 * j : ((j & 1) ? P.w3 : P.w1) + 192 * g + 32 * ((j - 20) >> 1);
  const u16* w2s = mkptr<const u16>(ka[14], ka[15]) + (size_t)(192 * g) * D + 32 * j;
  const int nin = isO ? NINO : ((((uintptr_t)ws0 >> 7) & 7) == 3 ? NINS : NINF);
  const u32 word = (((uintptr_t)ws0 >> 6) & 1) ? (isO ? ORDO : ORDF) : 0x543210u;
  int wn = 0;
  u32 hb = 0u;
  u32 exa = 0u, exl = 0u;
  {
    constexpr int L_CTL = L_LAND + 6 * 2048;
    volatile LAS u32* const CTL = (volatile LAS u32*)(smem + L_CTL);
    u32 gd;
    const u32 lho = WS_HB + (lane0 >> 1) * HBS + (lane0 & 1) * 16;
    const u32 hso = lane0 < 8 ? WS_HB + (u32)h * HBS + lane0 * 4 : WS_GOF + g * HBS;
    if (wave < 6) {
      const u16* s0 = P.wq + (size_t)(80 * c) * (H * DH) + h * DH + wave * 32;
      u32 tq = tstart * 8u + TSQ + (u32)wave * (TSTEP / 8);
#pragma unroll 1
      for (int p = 1; p < 5; ++p) {
        pace(tq, TSQ);
        dma_piece(s0, H * DH, smem, L_WQ + wave * 5120, p, lane0);
      }
    } else if (wave == 6) {
      u32 keep;
      asm volatile("s_mov_b32 %0, m0\n\ts_mov_b32 m0, %3\n\ts_nop 2\n\tglobal_load_lds_dword %1, %2 offset:1024\n\ts_mov_b32 m0, %0"
                   : "=&s"(keep)
                   : "v"(lane0 * 4), "s"(P.x0), "s"((u32)(smb + L_HB))
                   : "memory");
      if (j == SJ) store16(rs, rp_line(0, lane0 >> 3, g) + (lane0 & 7) * 16, u32x4{tstart, tstart, tstart, tstart});
      if (leader) {
        ctl_load<true>(rq, lho, (u32)(smb + L_CTL));
        ctl_load<false>(rq, WS_GOF + (lane0 & 7) * HBS, (u32)(smb + L_CTL + 2560));
      }
      asm volatile("s_waitcnt vmcnt(0)" ::: "memory");
      const u32x4 lv = afrag(smem, L_HB + lane0 * 16);
      const u32 xb = *reinterpret_cast<const u32*>(smem + L_HB + 1024 + lane0 * 4);
      float v[10];
#pragma unroll
      for (int k = 0; k < 4; ++k) {
        v[2 * k] = bflo(lv[k]);
        v[2 * k + 1] = bfhi(lv[k]);
      }
      v[8] = bflo(xb);
      v[9] = bfhi(xb);
      float sm = 0.f;
#pragma unroll
      for (int k = 0; k < 10; ++k) sm += v[k];
      const float mean = wave_sum(sm) * (1.0f / D);
      float q = 0.f;
#pragma unroll
      for (int k = 0; k < 10; ++k) q = fmaf(v[k] - mean, v[k] - mean, q);
      const float var = wave_sum(q) * (1.0f / D);
      const float mb = bf16r(mean);
      const float ri = __builtin_amdgcn_rcpf(bf16r(__builtin_amdgcn_sqrtf(bf16r(bf16r(var) + 1e-5f))));
      u32 hp[5];
#pragma unroll
      for (int k = 0; k < 5; ++k) {
        const u32 cc = cvt2(v[2 * k] - mb, v[2 * k + 1] - mb);
        hp[k] = cvt2(bflo(cc) * ri, bfhi(cc) * ri);
      }
      *reinterpret_cast<u32x4*>(smem + L_LN + lane0 * 16) = u32x4{hp[0], hp[1], hp[2], hp[3]};
      *reinterpret_cast<u32*>(smem + L_LN + 1024 + lane0 * 4) = hp[4];
      asm volatile("s_waitcnt lgkmcnt(0)" ::: "memory");
      *(volatile LAS u32*)(smem + L_VOTE + 4) = 1u;
    } else {
      ctl_load<false>(rq, hso, (u32)(smb + L_CTL + 2304));
      hb = rtt > PTHR ? 1u << ((g >> 2) ^ (j & 1)) : 0u;
    }
    if (wave == 1) *(volatile LAS u32x2*)(smem + L_CLK) = u32x2{now(), rtnow()};
    *reinterpret_cast<u32x4*>(smem + L_P + wave * P_W + lane0 * 16) = u32x4{0u, 0u, 0u, 0u};
    *reinterpret_cast<u32x4*>(smem + L_P + wave * P_W + 256 + lane0 * 16) = u32x4{0u, 0u, 0u, 0u};
    const u16* kb = P.kc + rowbase * DH;
    const u32 vo = (lane0 >> 3) * 384 + 16 * (lane0 & 7);
    const u32 kw = (u32)(smb + L_W2 + wave * 1024 + lane0 * 16), kr = (u32)(smb + L_W2 + wave * 1024 + (lane0 & 7) * 128 + (lane0 & 8) * 8 + (lane0 >> 4) * 16);
#define KPERM(i)                                                                          \
  asm volatile("ds_write_b128 %[w], v[%[r0]:%[r1]]\n\tds_read_b128 v[%[r0]:%[r1]], %[r]" \
               :                                                                          \
               : [w] "v"(kw), [r] "v"(kr), [r0] "n"(kreg(i)), [r1] "n"(kreg(i) + 3)       \
               : "memory")
#define WNEXT                                                                                  \
  dma_piece(ws0, isO ? D : F, smem, L_WB, wave * (isO ? 6 : 5) + ((word >> (4 * wn)) & 7), fresh6(lane0)); \
  wn = wn == (isO ? 5 : 4) ? 0 : wn + 1;
    const u32 qo = WS_GQ1 + (u32)(h * 8 + (lane0 >> 3)) * Q1S + wave * 128 + (lane0 & 7) * 16;
    auto step = [&](auto uc) __attribute__((always_inline)) {
      constexpr int u = decltype(uc)::v;
      if (u == UAD + 1) tstep += __builtin_amdgcn_readfirstlane(cnt[2]);
      BURST(u, vo, (u < 8 ? kb : vb) + ((u + rot) & 7) * GST * DH);
      if (wcnt(u + 1) != wcnt(u) || u + 1 == USW) {
        tnext += hsub * (PQ(u + 1) - PQ(u));
        tstep += 4 * (PQ(u + 1) - PQ(u));
      }
      if (u >= 2 && u < 10) {
        KPERM((3 * u + 18) % 24);
        KPERM((3 * u + 19) % 24);
        KPERM((3 * u + 20) % 24);
      }
      if (u == UAD - 3 && wave == 7) {
        ctl_load<false>(rq, rp_line(0, g, lane0 & 7), (u32)(smb + L_CTL + 2816));
        ++exa;
      }
      if (u == 0) {
        att_table<0>(smem, fresh6(lane0), wave, gop);
        att_table<1>(smem, fresh6(lane0), wave, gop);
      }
      if (u == UQ) {
        if (wave < 6) {
          const int lane = fresh6(lane0), m = lane & 15, G = lane >> 4;
          const int q_l0 = L_WQ + wave * 5120 + wlane(lane, 0), q_l1 = L_WQ + wave * 5120 + wlane(lane, 1);
          const int q_bk = 4096 - (G >> 1) * 1024;
          const int st = m == 8 ? 64 : 0;
          const int sa = m == 8 ? L_LN + 160 * c + 16 * G : (m == 15 ? L_ONE : L_ZERO);
          const u32 q_dst = WS_GQ1 + (u32)(h * 8 + c) * Q1S + wave * 128 + (lane >> 5) * 64 + m * 4;
          asm volatile("" ::: "memory");
          lds_wait((volatile LAS u32*)(smem + L_VOTE + 4), 1u);
          const u32x4 f0 = afrag(smem, sa), f1 = afrag(smem, sa + st), f2 = afrag(smem, G < 2 ? sa + 2 * st : L_TAG);
          f32x4 q_d0 = {0.f, 0.f, 0.f, 0.f}, q_d1 = q_d0;
          q_d0 = mfma32(f0, wfrag(smem, q_l0, 0), q_d0);
          q_d1 = mfma32(f0, wfrag(smem, q_l1, 0), q_d1);
          q_d0 = mfma32(f1, wfrag(smem, q_l0, 2048), q_d0);
          q_d1 = mfma32(f1, wfrag(smem, q_l1, 2048), q_d1);
          q_d0 = mfma32(f2, wfrag(smem, q_l0 + q_bk, 0), q_d0);
          q_d1 = mfma32(f2, wfrag(smem, q_l1 + q_bk, 0), q_d1);
          auto sw = __builtin_amdgcn_permlane32_swap(__float_as_uint(sum4(q_d0)), __float_as_uint(sum4(q_d1)), false, false);
          auto sx = __builtin_amdgcn_permlane16_swap(sw[1], sw[1], false, false);
          const f32x4 qd = quad4(__uint_as_float(cvt2(__uint_as_float(sx[0]), __uint_as_float(sx[1]))));
          if ((lane0 & 19) == 0) store16<AUXQ>(rs, q_dst, __builtin_bit_cast(u32x4, qd));
          ++exa;
        }
      }
      if (u == UAD - 1 && wave == 7) {
        asm volatile("" ::: "memory");
        const int d = (int)(CTL[704 + lane0] - tstart);
        const float sd = sum8((u32)(d + DWIN) < 2u * DWIN ? (float)d : 0.f);
        cnt[2] = (u32)(int)__builtin_rintf(sd * (DEQ / (15 - NUN - UAD)));
        int len = 5 * TSQ + 3 * NUN * TSE + 7 * (TSE / 8);
#pragma unroll
        for (int k = 0; k < 16 - NUN; ++k) len += 4 * PQ(k);
        *(volatile LAS u32*)(smem + L_AN0) = tstart * 8u + (u32)(len + (int)sd);
      }
      if ((u == UB && wave < 6) || (u == UB - 1 && wave == 7)) {
        asm volatile("" ::: "memory");
        if (u == UB - 1) {
          hb += (__builtin_amdgcn_readfirstlane(CTL[576 + c]) & ~3u) + 4u;
          *(volatile LAS u32*)(smem + L_VOTE + 8) = hb;
          hb += 4u;
        }
        u32 t0, t1, last;
        asm volatile("s_mov_b64 exec, 1\n\tv_mov_b32 %0, 1\n\tv_mov_b32 %1, %[sm]\n\tds_add_rtn_u32 %0, %1, %0 offset:%[c]\n\t"
                     "ds_read_b32 %1, %1 offset:%[v]\n\ts_waitcnt lgkmcnt(0)\n\tv_readfirstlane_b32 %2, %0\n\t"
                     "s_cmp_eq_u32 %2, 6\n\ts_cbranch_scc0 1f\n\tbuffer_store_dword %1, off, %[rq], %[o] sc1\n1:\n\t"
                     "s_cselect_b32 %2, 1, 0\n\ts_mov_b64 exec, -1"
                     : "=&v"(t0), "=&v"(t1), "=&s"(last)
                     : [sm] "s"(smb), [c] "n"(L_CNT + 4), [v] "n"(L_VOTE + 8), [rq] "s"(rq), [o] "s"(WS_HB + h * HBS + c * 4)
                     : "memory", "scc");
        exa += last;
        exl += last;
      }
      if (u == UL && wave == 7) {
        ctl_load<false>(rq, hso, (u32)(smb + L_CTL + 2048));
        ++exa;
        if (UL + 2 < UK) ++exl;
      }
      if (u == UG && leader) {
        ctl_load<true>(rq, lho, (u32)(smb + L_CTL + 1024));
        ++exa;
      }
      if (u == UK && wave == 7) {
        HS_WAIT((u32)ch == 0xffu)
        if (lane0 == 0) cnt[1] = 15u;
      }
      if (u == UC && wave < 6) {
        lds_wait(cnt + 1, 15u);
        ctl_load<true, true>(rq, qo, (u32)(smb + L_LAND + wave * 2048));
        ++exa;
      }
      if (u == UW && leader) {
        const u32x4 xa = afrag(smem, L_CTL + lane0 * 16);
        u32x4 lv;
#pragma unroll 1
        for (;;) {
          asm volatile("" ::: "memory");
          lv = afrag(smem, L_CTL + 1024 + lane0 * 16);
          const bool same = lv[0] == xa[0] || lv[1] == xa[1] || lv[2] == xa[2] || lv[3] == xa[3];
          if (__builtin_amdgcn_ballot_w64(same) == 0ull || tmo == 0u) break;
          --tmo;
          __builtin_amdgcn_s_sleep(4);
          ctl_load<true>(rq, lho, (u32)(smb + L_CTL + 1024));
          asm volatile("s_waitcnt vmcnt(0)" ::: "memory");
        }
        u32 nv = 256u;
#pragma unroll
        for (int e = 0; e < 4; ++e)
          nv += (u32)__builtin_popcountll(__builtin_amdgcn_ballot_w64((lv[e] & 2u) != 0u)) -
                (u32)__builtin_popcountll(__builtin_amdgcn_ballot_w64((lv[e] & 1u) != 0u));
        if (lane0 < 8) __builtin_amdgcn_raw_buffer_store_b32(CTL[640 + lane0] + (nv > 256u ? 2u : 1u), rs, WS_GOF + lane0 * HBS, 0, 16);
        ++exa;
        ++exl;
      }
      if (u == UX && wave == 7) {
        ctl_load<false>(rq, hso, (u32)(smb + L_CTL + 2048));
        ++exa;
      }
      if (u == UX && wave < 6) {
        const int lane = fresh6(lane0);
        const int ca = L_CS + wave * 128 + (((lane >> 1) ^ (lane >> 5)) & 1) * 64 + (lane & 1) * 32;
        const f32x4 c0 = *reinterpret_cast<const f32x4*>(smem + ca), c1 = *reinterpret_cast<const f32x4*>(smem + ca + 16);
#pragma unroll 1
        for (int p = 0; p < 5; ++p) {
          u32x4* const pw = reinterpret_cast<u32x4*>(smem + L_WQ + wave * 5120 + p * 1024 + lane * 16);
          const u32x4 w = *pw;
#define CW(k, c, e) cvt2(fmaf(bflo(w[k]), SCL, c[e]), fmaf(bfhi(w[k]), SCL, c[e + 1]))
          *pw = u32x4{CW(0, c0, 0), CW(1, c0, 2), CW(2, c1, 0), CW(3, c1, 2)};
        }
      }
      if (u == UF) {
        if (wave < 6) {
          q1_finish(smem, fresh6(lane0), wave);
          asm volatile("s_waitcnt lgkmcnt(0)" ::: "memory");
          if (lane0 == 0) lds_add(smem, L_CNT, 1u);
        }
      }
      if (wcnt(u) != 0 && (u - WU0) / WSP < nin && (wave & (WSP - 1)) == (u - WU0) % WSP) {
        u32 tw = tnext - WBK - hsub * PQ(u + 1);
        pace(tw, 0u);
        WNEXT
        ++exa;
        ++exl;
      }
    };
    static_assert(UQ >= 1 && UQ + 2 <= UB && UB < UL && UL + 2 <= UK && UK <= UC && UG + 2 <= UW && UC + 2 <= UF && UF < UX && UX >= 9 && UX + 2 < 16 - NUN && UAD >= 3 && WU0 >= UB, "order");
    slots<0, UX + 1>(step);
  }

#define lane0 lane_id()
#pragma unroll 1
  for (int half = 0;; ++half) {
    {
      u32x4 O0, O1, O2, S0, S1;
      float sk;
      Att at;
      const int lane = fresh6(lane0);
      const u32 gso = (u32)half * GO_SZ;
      {
        const u32 off = fresh(ar + A_GQ + hsub * 16384 + wave * 128 + (wave < 6 ? (lane >> 3) * 896 + (lane & 7) * 16 : (lane & 7) * 896));
        if (half == 1) {
          if (wave == 7) wait_tt(A_Q);
        } else {
          BURST(UX + 1, (u32)((lane >> 3) * 384 + 16 * (lane & 7)), vb + ((UX + 1 + rot) & 7) * GST * DH);
          lds_wait(cnt, 6u);
        }
        asm volatile("s_cmp_eq_u32 %[hf], 0\n\ts_cbranch_scc1 1f\n\ts_cmp_lt_u32 %[wv], 7\n\ts_cselect_b64 exec, -1, 0\n\ts_barrier\n\t"
                     "buffer_load_dwordx4 v[44:47], %[of], %[rq], 0 offen sc1\n\ts_mov_b64 exec, -1\n\ts_setprio 2\n1:"
                     : "={v[44:47]}"(O1)
                     : [hf] "s"(gso), [wv] "s"(wave), [of] "v"(off), [rq] "s"(rq)
                     : "scc", "memory");
        att_setup(at, smem, lane, wave);
        to2 = T_O2;
#define QK(W, a, k0, k1) \
  "s_waitcnt lgkmcnt(" #W ")\n\t" SM "v[32:35], v[" #a ":" #a "+3], v[" #k0 ":" #k0 "+7], %[ix]\n\t" SM "v[36:39], v[" #a ":" #a "+3], v[" #k1 ":" #k1 "+7], %[ix]\n\t"
#define QLD "buffer_load_dwordx4 v[44:47], %[of], %[rq], 0 offen sc1\n"
#define QWT "s_sub_u32 s88, s88, 1\n\ts_waitcnt vmcnt(0)\n\t"
        u32 t, z;
        asm volatile("v_mov_b64 v[32:33], 0\n\tv_mov_b64 v[34:35], 0\n\tv_mov_b64 v[36:37], 0\n\tv_mov_b64 v[38:39], 0\n\t"
                     "v_mov_b32 %[z], 0\n\ts_cmp_eq_u32 %[hf], 0\n\ts_cbranch_scc1 5f\n\t"
                     "s_mov_b32 s88, 0x10000\n\ts_cmp_lt_u32 %[wv], 6\n\ts_cbranch_scc1 2f\n\t"
                     "s_bitcmp0_b32 %[wv], 0\n\ts_cselect_b64 exec, -1, 0\n\ts_branch 3f\n"
                     "6:\n\t" QLD "3:\n\t" QWT
                     "v_add_f32 v48, v44, v45\n\tv_mul_f32 v49, 0x3acccccd, v47\n\tv_add_f32 v48, v48, v46\n\t" AND3("v44", "v45", "v46")
                     "v_add_f32_dpp v49, v49, v49" Q1 "v_add_f32_dpp v48, v48, v48" Q1 AND3("%[t]", "1", "v47")
                     "v_add_f32_dpp v49, v49, v49" Q2 "v_add_f32_dpp v48, v48, v48" Q2 "v_cmp_eq_u32 vcc, 0, %[t]\n\t"
                     "v_add_f32_dpp v49, v49, v49" HM "v_add_f32_dpp v48, v48, v48" HM
                     "v_mul_f32 v49, -v49, v49\n\tv_fmac_f32 v49, 0x3acccccd, v48\n\t" CV "v48, 0, |v49|\n\tv_add_f32 v48, 0x3727c5ac, v48\n\t"
                     CV "v48, 0, v48\n\tv_sqrt_f32 v48, v48\n\ts_cselect_b64 vcc, 0, vcc\n\t" CV "v48, 0, v48\n\tv_rcp_f32 v48, v48\n\t"
                     "s_cbranch_vccnz 6b\n\tds_write_b32 %[qw], v48\n\ts_cbranch_execnz 4f\n\ts_branch 2f\n"
                     "1:\n\t" QLD "2:\n\t" QWT
                     "v_permlane32_swap_b32 v44, v45\n\tv_permlane32_swap_b32 v46, v47\n\t"
                     "v_add_f32 v48, v44, v45\n\tv_add_f32 v49, v46, v47\n\t" AND3("v44", "v45", "v46") AND3("%[t]", "1", "v47")
                     "v_permlane16_swap_b32 v48, v49\n\tv_add_f32 v48, v48, v49\n\t"
                     "v_cmp_eq_u32 vcc, 0, %[t]\n\ts_nop 0\n\tv_add_f32_dpp v48, v48, v48" R8
                     CV "v49, v48, v48\n\tds_write_b16 %[qw], v49\n\ts_cselect_b64 vcc, 0, vcc\n\t"
                     "s_cbranch_vccnz 1b\n\ts_mov_b64 exec, -1\n4:\n\ts_waitcnt lgkmcnt(0)\n\ts_barrier\n"
                     "5:\n\tds_read_b128 v[40:43], %[q0]\n\tds_read_b128 v[44:47], %[q1]\n\t"
                     "ds_read_b128 v[48:51], %[q2]\n\tds_read_b128 v[52:55], %[q3]\n\t"
                     "ds_read_b128 v[56:59], %[q4]\n\tds_read_b128 v[60:63], %[q5]\n\tds_read_b32 %[sk], %[z] offset:%[su]\n\t"
                     QK(6, 40, 64, 96) QK(5, 44, 72, 104) "ds_read_b128 v[40:43], %[z]\n\t"
                     QK(5, 48, 80, 112) "ds_read_b128 v[44:47], %[z]\n\t" QK(5, 52, 88, 120) "ds_read_b128 v[48:51], %[z]\n\t"
                     QK(5, 56, 128, 144) QK(3, 60, 136, 152)
                     : "=&{v[40:43]}"(O0), "+{v[44:47]}"(O1), "=&{v[48:51]}"(O2), "=&{v[32:35]}"(S0), "=&{v[36:39]}"(S1),
                       [t] "=&v"(t), [z] "=&v"(z), [sk] "=&{v31}"(sk)
                     : [hf] "s"(gso), [wv] "s"(wave), [of] "v"(off), [rq] "s"(rq), [qw] "v"(at.qw), [q0] "v"(at.qad[0]),
                       [q1] "v"(at.qad[1]), [q2] "v"(at.qad[2]), [q3] "v"(at.qad[3]), [q4] "v"(at.qad[4]), [q5] "v"(at.qad[5]),
                       [ix] "v"(at.idxq), [su] "n"(L_SUM), "{v[64:95]}"(kv0), "{v[96:127]}"(kv1), "{v[128:159]}"(kv2)
                     : V52, "s88", "vcc", "scc", "memory");
      }
      u32 hq = gso;
      asm volatile("" : "+s"(hq));
      if (__builtin_expect(hq == 0u, 0)) {
        const u32 tn_ = tnext, ts_ = tstep, ea_ = exa;
        u32 tnext = tn_, tstep = ts_, exa = ea_, exl = 0u;
        int wn = nin;
        const int lv_ = fresh6(lane0);
        const u32 vo = (lv_ >> 3) * 384 + 16 * (lv_ & 7);
#pragma unroll
        for (int u = UX + 2; u < 16 - NUN; ++u) {
          BURST(u, vo, vb + ((u + rot) & 7) * GST * DH);
          if (u == UX + 2 && wave == 6) {
            const u32x2 c0 = *(volatile LAS u32x2*)(smem + L_CLK);
            const float r = (float)(rtnow() - c0[1]) * (24.2f * 512.f) * __builtin_amdgcn_rcpf((float)(now() - c0[0])) - CLKO;
            *(volatile LAS u32*)(smem + L_CLK) = ((u32)__builtin_fminf(__builtin_fmaxf(r, 0.f), 127.f) << 25) | 1u;
            wait_tt<L_ZERO>(tnext - 160u);
          }
          if (u == UX + 2 && wave == 7) {
            constexpr int L_CTL = L_LAND + 6 * 2048;
            volatile LAS u32* const CTL = (volatile LAS u32*)(smem + L_CTL);
            const u32 hso = lv_ < 8 ? WS_HB + (u32)h * HBS + lv_ * 4 : WS_GOF + g * HBS;
            u32 gd, tmo = SPIN_CAP;
            HS_WAIT(false)
            if (lane0 == 8) *(volatile LAS u32*)(smem + L_AR) = (u32)(g & 3) * AREA + (((u32)(g >> 2) ^ (gd >> 1)) & 1u) * SIDE;
          }
        }
        tnext += wave * (TSE / 8 - WOFS) - hsub * PQ(15);
        tstep = TSE;
#pragma unroll
        for (int t = 48 - 3 * NUN; t < 48; ++t) {
          LOADW(t, vo, vb + ((t / 3 + rot) & 7) * GST * DH);
        }
        if (wave == 7) {
          const u32 a7 = *(volatile LAS u32*)(smem + L_AR) + (u32)(j >> 3) * 16384u + (u32)(j & 7) * 1024u;
          const u32x4 zz = __builtin_bit_cast(u32x4, zero4());
          store16(rs, lv_ * 16 + a7 + 2 * 16384, zz);
          store16(rs, lv_ * 16 + a7 + (j < 16 ? 6 : 8) * 16384, zz);
          store16(rs, lv_ * 16 + a7 + 12 * 16384, zz);
        }
#pragma unroll 1
        for (int p = 0; p < (isO ? NPRE : NPF); ++p) {
          pace(tnext, isO ? TPV : TPF);
          WNEXT
        }
      }
#define PW(V, L) "s_waitcnt vmcnt(" #V "+%[no]) lgkmcnt(" #L ")\n\t"
#define PX(o, a, k) SM "v[40+4*" #o ":43+4*" #o "], v[" #a ":" #a "+3], v[160+24*" #k "+8*" #o ":167+24*" #k "+8*" #o "], %[ix]\n\t"
#define ID " quad_perm:[0,1,2,3] row_mask:0xf bank_mask:"
#define OW(o) CV "v[40+2*" #o "], v[40+4*" #o "], v[41+4*" #o "]\n\t" CV "v[41+2*" #o "], v[42+4*" #o "], v[43+4*" #o "]\n\t" \
  "ds_write_b64 %[oa], v[40+2*" #o ":41+2*" #o "] offset:128*" #o "\n\t"
#define TR "ds_read_b64_tr_b16 "
#define AREAD "ds_read_b64 v[46:47], v50 offset:%[lar]\n\t"
#define ACHK "s_waitcnt lgkmcnt(0)\n\tv_cmp_ne_u32 vcc, 0, v47\n\t"
      asm volatile("s_mov_b32 s80, 0xff00ff\n\ts_mov_b32 s81, 0xff00ff00\n\ts_mov_b32 s82, " SAFEB "\n\ts_mov_b32 s83, 1\n\t" QNOP
                   "v_add_f32 v32, v32, v34\n\tv_add_f32 v34, v33, v35\n\t"
                   "v_add_f32_dpp v34, v36, v38" ID "0x3\n\tv_add_f32_dpp v32, v37, v39" ID "0xc\n\t"
                   "s_nop 0\n\tv_add_f32_dpp v32, v34, v32" R8 "v_mul_f32 %[sk], v32, %[sk]\n"
                   "1:\n\tv_exp_f32 v33, %[sk]\n\ts_cmp_lt_u32 %[wv], 4\n\t" CV "v33, v33, 0\n\t"
                   "ds_write2_b32 %[wa], v33, v33 offset1:%[o1]\n\t"
                   "ds_read_b128 v[52:55], %[ra]\n\tds_read_b128 v[56:59], %[ra] offset:16\n\t"
                   "ds_read_b128 v[60:63], %[ra] offset:32\n\tds_read_b128 v[36:39], %[ra] offset:48\n\ts_setprio 0\n\t"
                   "v_cmp_nge_f32 vcc, s82, |%[sk]|\n\tds_write_b16 %[la], v33\n\t"
                   PW(18, 4) PX(0, 52, 0) PX(1, 52, 0) PX(2, 52, 0) PW(12, 1) PX(0, 56, 1) PX(1, 56, 1) PX(2, 56, 1)
                   PW(6, 1) PX(0, 60, 2) PW(2, 1) PX(0, 36, 3) PX(1, 60, 2) PW(1, 1) PX(1, 36, 3) PX(2, 60, 2) PW(0, 1) PX(2, 36, 3)
                   "s_cbranch_vccnz 7f\n2:\n\ts_mov_b64 exec, s[80:81]\n\t" OW(0) OW(1) OW(2)
                   "s_mov_b64 exec, -1\n\tv_mov_b32 v50, 0\n\ts_cbranch_scc0 3f\n\t"
                   "v_mov_b32_dpp v51, %[ma] row_shr:2 row_mask:0xf bank_mask:0xf bound_ctrl:0\n\t"
                   "ds_read_b128 v[32:35], %[ma]\n\tds_read_b128 v[36:39], v51\n\t"
                   "s_cmp_lt_u32 %[wv], 3\n\ts_waitcnt lgkmcnt(0)\n\ts_barrier\n\t"
                   TR "v[52:53], %[sa]\n\t" TR "v[54:55], %[sa] offset:%[os]\n\ts_cbranch_scc0 4f\n\t"
                   TR "v[56:57], %[sa] offset:64\n\t" TR "v[58:59], %[sa] offset:%[os]+64\n\t" AREAD
                   "s_waitcnt lgkmcnt(3)\n\t" MF F2 ", " F0 ", v[32:35], 0\n\ts_waitcnt lgkmcnt(1)\n\t" MF F2 ", " F1 ", v[36:39], " F2 "\n\t"
                   ACHK "v_add_u32 v56, %[ds], v46\n\ts_nop 3\n\ts_cbranch_vccnz 8f\n\t"
                   CV "v52, v60, v61\n\t" CV "v53, v62, v63\n\tv_or_b32 v52, 1, v52\n\tv_or_b32 v53, 1, v53\n\t"
                   "buffer_store_dwordx2 v[52:53], v56, %[rq], %[so] offen sc1\n\ts_branch 9f\n"
                   "7:\n\tv_mov_b32 v52, 0\n\tv_mov_b32 v53, 0xff800000\n\tv_cmp_ne_u32 vcc, s82, v52\n\ts_and_b64 exec, vcc, 1\n\t"
                   "ds_write2_b32 v52, v53, v53 offset0:%[lar]/4+1 offset1:%[lar]/4+2\n\t"
                   "s_cmp_lt_u32 %[wv], 4\n\ts_branch 2b\n"
                   "8:\n\tds_max_f32 v50, %[sk] offset:%[lar]+8\n\ts_mov_b32 s82, 0\n\ts_waitcnt lgkmcnt(0)\n\ts_barrier\n\t"
                   "ds_read_b32 v52, v50 offset:%[lar]+8\n\tds_write_b32 v50, v50 offset:%[lar]+4\n\t"
                   "ds_read_b128 v[40:43], v50\n\tds_read_b128 v[44:47], v50\n\tds_read_b128 v[48:51], v50\n\ts_waitcnt lgkmcnt(0)\n\t"
                   "v_readfirstlane_b32 s83, v52\n\tv_sub_f32 %[sk], %[sk], v52\n\ts_bitset1_b32 s83, 0\n\ts_branch 1b\n"
                   "4:\n\t" AREAD "s_waitcnt lgkmcnt(1)\n\t" MF F2 ", " F0 ", v[32:35], 0\n\t"
                   ACHK "v_add_u32 v56, %[ds], v46\n\tv_mov_b32 v58, s83\n\ts_nop 3\n\t"
                   "v_add_f32 v60, v60, v61\n\tv_add_f32 v62, v62, v63\n\tv_add_f32 v59, v60, v62\n\ts_cbranch_vccnz 8b\n\ts_nop 0\n\t"
                   "v_add_f32_dpp v59, v59, v59 row_bcast:15 row_mask:0xa bank_mask:0xf\n\tv_or_b32 v59, 1, v59\n\t"
                   "buffer_store_dwordx2 v[58:59], v56, %[rq], %[so] offen sc1\n\ts_branch 9f\n"
                   "3:\n\ts_waitcnt lgkmcnt(0)\n\ts_barrier\n\t" AREAD "v_bfrev_b32 v56, 1\n\t" ACHK "s_cbranch_vccnz 8b\n\t"
                   "buffer_store_dword v56, v56, %[rq], 0 offen\n9:"
                   : "+{v[40:43]}"(O0), "+{v[44:47]}"(O1), "+{v[48:51]}"(O2), "+{v[32:35]}"(S0), "+{v[36:39]}"(S1), [sk] "+{v31}"(sk)
                   : [ix] "v"(at.idx), [no] "n"(NPRE), [la] "v"(at.la), [oa] "v"(at.oa), [sa] "v"(at.sa), [ma] "v"(at.ma),
                     [wa] "v"(at.wa), [ra] "v"(at.ra), [o1] "n"(P_U / 4 + 1), [ds] "v"(at.dst), [wv] "s"(wave), [rq] "s"(rq),
                     [so] "s"(gso), [lar] "n"(L_AR), [os] "n"(OM_S * 8), "{v[160:191]}"(kv3), "{v[192:223]}"(kv4), "{v[224:255]}"(kv5)
                   : V52, "s80", "s81", "s82", "s83", "vcc", "scc", "memory");
      tref = now();
      ar = __builtin_amdgcn_readfirstlane(O1[2]);
    }
    const u32 go = ar + A_GO + (u32)half * GO_SZ;
    const u32 gyw = A_GY + (u32)half * GY_PAIR;
    const u32 gy = gyw + ar;
    const u32 gh = go + A_GH;
    const int land = L_LAND + wave * 2048;
    if (isO) {
      {
        const int lane = fresh6(lane0);
        const u32 voff = fresh(go + go_slot(wave) + ((lane * 16) ^ ((lane >> 4) * 64)));
        if (wave == 7) {
          if (half)
            wait_until(tref + to2);
          else
            wait_tt<L_AN0>(B_O);
        }
        if (wave == 6 && half == 0) wait_until(tref + 24 * T_OL);
        asm volatile("s_mov_b32 m0, %2\n\ts_barrier\n\t"
                     "buffer_load_dwordx4 %0, %1, 0 offen sc1 lds\n\tbuffer_load_dwordx4 %0, %1, 0 offen offset:1024 sc1 lds"
                     :
                     : "v"(voff), "s"(rq), "s"(land)
                     : "memory");
        const int l0 = fresh(L_WB + wave * 6144 + wlane(lane, 0)), l1 = l0 ^ 32;
        asm volatile("s_waitcnt vmcnt(3)" ::: "memory");
        u32x4 wf[6];
#pragma unroll
        for (int k = 0; k < 3; ++k) {
          wf[2 * k] = wfrag(smem, l0, 2048 * k);
          wf[2 * k + 1] = wfrag(smem, l1, 2048 * k);
        }
        asm volatile("" ::: "memory");
        const int n = lane & 15, G = lane >> 4;
        const int r0_ = land + (n & 7) * 256 + (n & 3) * 64;
        const int ra = fresh(n < 8 ? r0_ + G * 16 : r0_ ^ 192), rb = fresh((r0_ + G * 16) ^ 64), rc = fresh((r0_ + G * 16) ^ 128);
        const int rw = fresh(L_RED + (lane >> 1) * 80 + wave * 8 + (lane & 1) * 4);
        const int rr = fresh(L_RED + n * 80);
        const u32 dst = fresh(gyw + gy_rep(G) + gy_chunk(4 * j + (n >> 2)) + (n & 3) * 4 + g * 256);
        u32 t;
#define RS(r, e) "v_mul_f32_dpp v" #r ", v41, v" #r " row_newbcast:" #e ALL
        asm volatile("s_mov_b64 s[82:83], -1\n\ts_movk_i32 s89, %[sd]\n\t" POLL_HEAD
                     "s_branch 5f\n"
                     "7:\n\tv_max_f32_dpp v41, v52, v52" Q1 "s_nop 1\n\tv_max_f32_dpp v41, v41, v41" Q2
                     "s_nop 1\n\tv_max_f32_dpp v41, v41, v41" HM "v_sub_f32 v41, v52, v41\n\tv_exp_f32 v41, v41\n\ts_nop 0\n\t"
                     "v_mul_f32 v42, v41, v43\n\ts_nop 1\n\tv_add_f32_dpp v42, v42, v42" Q1 "s_nop 1\n\tv_add_f32_dpp v42, v42, v42" Q2
                     "s_nop 1\n\tv_add_f32_dpp v42, v42, v42" HM "v_mov_b32_dpp v41, v41 row_shl:4 row_mask:0x2 bank_mask:0xf\n\t"
                     "v_rcp_f32 v42, v42\n\ts_nop 0\n\t"
                     RS(44, 8) RS(45, 9) RS(46, 10) RS(47, 11) RS(48, 8) RS(49, 9) RS(50, 10) RS(51, 11) "s_branch 3f\n"
                     "2:\n\tv_cmp_eq_u32 vcc, 0, %[t]\n\ts_cbranch_vccz 7b\n\t" POLL_MASK POLL_LOAD
                     "\n5:\n\tv_mov_b32 v41, 1\n\t" POLL_READ
                     "s_waitcnt lgkmcnt(2)\n\t"
                     MF "v[44:47], " F0 ", %[w0], 0\n\tv_add_f32 v43, v53, v55\n\t"
                     "v_or_b32_dpp v41, v52, v41 quad_perm:[0,1,2,3] row_mask:0xf bank_mask:0xc\n\t"
                     MF "v[48:51], " F0 ", %[w1], 0\n\tv_add_f32_dpp v42, v43, v43" Q1 AND3("v52", "v53", "v54")
                     "s_waitcnt lgkmcnt(1)\n\t"
                     MF "v[44:47], " F1 ", %[w2], v[44:47]\n\tv_add_f32_dpp v42, v42, v42" Q2 AND3("%[t]", "v55", "v56")
                     MF "v[48:51], " F1 ", %[w3], v[48:51]\n\tv_add_f32_dpp v42, v42, v42" HM AND3("%[t]", "v57", "v58")
                     "s_waitcnt lgkmcnt(0)\n\t"
                     MF "v[44:47], " F2 ", %[w4], v[44:47]\n\t" MF "v[48:51], " F2 ", %[w5], v[48:51]\n\tv_rcp_f32 v42, v42\n\t"
                     AND3("%[t]", "v59", "v60") AND3("%[t]", "v61", "v62") AND3("%[t]", "1", "v63")
                     "v_cmp_gt_u32 vcc, v41, %[t]\n\ts_cbranch_vccnz 2b\n"
                     "3:\n\ts_mov_b32 exec_hi, 0\n\ts_cmp_eq_u32 %[wv], 0\n\t" PK(44, 46) PK(48, 50)
                     "v_add_f32 v44, v44, v45\n\tv_add_f32 v48, v48, v49\n\ts_nop 1\n\t"
                     "v_permlane16_swap_b32 v44, v48\n\tv_add_f32 v44, v44, v48\n\tv_mul_f32_dpp v44, v42, v44 row_newbcast:8" ALL
                     "ds_write_b32 %[rw], v44\n\ts_cselect_b64 exec, -1, 0\n\ts_waitcnt lgkmcnt(0)\n\ts_barrier\n\t"
                     "ds_read_b128 " F0 ", %[rr]\n\tds_read_b128 " F1 ", %[rr] offset:16\n\tds_read_b128 " F2 ", %[rr] offset:32\n\t"
                     "ds_read_b128 v[44:47], %[rr] offset:48\n\ts_cbranch_execz 9f\n\t"
                     "s_waitcnt lgkmcnt(2)\n\t" PK(52, 54) PK(56, 58) PK(52, 56) "s_waitcnt lgkmcnt(1)\n\t" PK(52, 60) PK(52, 62)
                     "s_waitcnt lgkmcnt(0)\n\t" PK(52, 44) PK(52, 46)
                     CV "v52, v52, v53\n\tv_or_b32 v52, 1, v52\n\t"
                     "buffer_store_dword v52, %[ds], %[rq], 0 offen sc1\n\tbuffer_store_dword v52, %[ds], %[rq], s89 offen sc1\n"
                     "9:\n\ts_mov_b64 exec, -1"
                     : [t] "=&v"(t)
                     : [ra] "v"(ra), [rb] "v"(rb), [rc] "v"(rc), WIN6(wf), [vo] "v"(voff), [rq] "s"(rq), [land] "s"(land), [rw] "v"(rw),
                       [rr] "v"(rr), [ds] "v"(dst), [wv] "s"(wave), [sd] "n"(SIDE)
                     : V40, V52, S80);
      }
      tref = now();
      if (half == 0) {
        if (wave >= 1 && wave < 4) {
          wait_until(tref + T_W2);
#pragma unroll
          for (int p = 0; p < 4; ++p) dma_piece(w2s, D, smem, L_W2, (wave - 1) * 4 + ((j & W2R) ? 3 - p : p), fresh6(lane0));
          asm volatile("s_waitcnt vmcnt(0)" ::: "memory");
          if (lane0 == 0) lds_add(smem, L_CNT + 12, 1u);
        }
        if (wave == 0) {
          wait_until(tref + 24 * T_LAZ0);
          x_request(rq, fresh6(lane0), gy + gy_row(c), land);
          x_update<true>(smem, rq, fresh6(lane0), gy + gy_row(c), land, c);
        }
      } else if (g == 0 && wave == 2) {
        const int lane = fresh6(lane0);
        const int col = j * 32 + (lane & 7) * 4 + ((lane >> 4) & 1) * 2 + (lane >> 5);
        u32 x0v, dummy;
        u16* const po = karg<64>() + col;
        asm volatile("global_load_ushort %0, %2, off\n\tglobal_load_ushort %1, %3, off\n\ts_waitcnt vmcnt(0)"
                     : "=&v"(x0v), "=&v"(dummy)
                     : "v"(karg<0>() + col), "v"(po)
                     : "memory");
        wait_until(tref + 24 * T_LAZY);
        const u32 o = gy + gy_chunk(4 * j + ((lane >> 1) & 3)) + (lane >> 3) * 256 + (lane & 1) * 8;
        const u32 off[3] = {o - GY_PAIR, o - GY_PAIR + GY_INST, o};
        float y[3];
        col_poll<3>(rq, off, y);
        float x3 = bf16r(bf16r(bf16r(bflo(x0v) + y[0]) + y[1]) + y[2]);
        const u32 off2 = fresh(o + GY_INST);
        asm volatile("" : "+v"(x3));
        wait_tt(A_F);
        u32 t;
        asm volatile("s_mov_b32 s88, 0x10000\n"
                     "1:\n\tbuffer_load_dwordx2 v[40:41], %[of], %[rq], 0 offen sc1\n\ts_sub_u32 s88, s88, 1\n\ts_waitcnt vmcnt(0)\n\t"
                     "v_lshlrev_b32 v42, 16, v40\n\tv_and_b32 v43, 0xffff0000, v40\n\t"
                     "v_lshlrev_b32 v44, 16, v41\n\tv_and_b32 v45, 0xffff0000, v41\n\t"
                     "v_permlane32_swap_b32 v42, v43\n\t" AND3("v40", "1", "v41") "v_permlane32_swap_b32 v44, v45\n\t"
                     "v_add_f32 v42, v42, v43\n\tv_add_f32 v44, v44, v45\n\tv_cmp_eq_u32 vcc, 0, %[t]\n\ts_cselect_b64 vcc, 0, vcc\n\t"
                     "v_permlane16_swap_b32 v42, v44\n\tv_add_f32 v42, v42, v44\n\ts_cbranch_vccnz 1b\n\ts_nop 0\n\t"
                     "v_add_f32_dpp v42, v42, v42" R8 CV "v42, 0, v42\n\tv_add_f32 v42, %[x], v42\n\t" CV "v42, v42, v42\n\t"
                     "global_store_short %[po], v42, off"
                     : [t] "=&v"(t)
                     : [of] "v"(off2), [rq] "s"(rq), [x] "v"(x3), [po] "v"(po)
                     : V40, "s88", "vcc", "scc", "memory");
      }
      if (wave >= 6) {
        const int lane = fresh6(lane0), wv = wave - 6;
        const int n = lane & 15, G = lane >> 4;
        const int l0 = fresh(L_W2 + wlane(lane, wv));
        const int l48 = lane < 48 ? lane : 0;
        const u32 off = fresh(gh + (u32)l48 * 16);
        const int aw = fresh(L_ACT + wv * 384 + l48 * 8);
        const int ab = fresh(L_ACT + wv * 384 + 16 * G);
        const u32 dst = fresh((lane & (half ? 49 : 1)) ? OOB : gyw + GY_INST + gy_rep(G) + gy_chunk(4 * j + wv * 2 + (n >> 3)) + (n & 7) * 2 + g * 256);
        if (half) {
          wait_tt(A_H);
        } else {
          wait_tt<L_AN0>(B_H - B_HC);
          wait_until(tref + 24 * T_HL);
        }
        u32 t;
        u32x4 pa, pb;
#define HLD "buffer_load_dwordx4 v[40:43], %[of], %[rq], 0 offen sc1\n\tbuffer_load_dwordx4 v[44:47], %[of], %[rq], s90 offen sc1\n"
        asm volatile("s_movk_i32 s90, 0x4000\n\tv_mov_b32 v48, 0\n\t" HLD
                     "1:\n\tds_read_b32 %[t], v48 offset:%[c]\n\ts_waitcnt lgkmcnt(0)\n\tv_cmp_ne_u32 vcc, 3, %[t]\n\ts_cbranch_vccnz 1b"
                     : "=&{v[40:43]}"(pa), "=&{v[44:47]}"(pb), [t] "=&v"(t)
                     : [of] "v"(off), [rq] "s"(rq), [c] "n"(L_CNT + 12)
                     : "v48", "s90", "vcc", "memory");
        u32x4 wf[6];
#pragma unroll
        for (int k = 0; k < 6; ++k) wf[k] = wfrag(smem, l0, 2048 * k);
#define HK(W, a, w, c) "s_waitcnt lgkmcnt(" #W ")\n\t" MF "v[36:39], v[" #a ":" #a "+3], %[" #w "], " c "\n\t"
        asm volatile("s_mov_b32 s88, 0x10000\n\ts_movk_i32 s89, %[sd]\n\ts_movk_i32 s90, 0x4000\n\ts_branch 2f\n"
                     "1:\n\ts_waitcnt lgkmcnt(0)\n\t" HLD "2:\n\ts_sub_u32 s88, s88, 1\n\ts_waitcnt vmcnt(0)\n\t"
                     "v_pk_mul_f32 v[48:49], v[40:41], v[44:45]\n\tv_pk_mul_f32 v[50:51], v[42:43], v[46:47]\n\t"
                     CV "v48, v48, v49\n\t" CV "v49, v50, v51\n\tds_write_b64 %[aw], v[48:49]\n\t"
                     "ds_read_b128 v[48:51], %[ab]\n\tds_read_b128 v[52:55], %[ab] offset:64\n\t"
                     "ds_read_b128 v[56:59], %[ab] offset:128\n\tds_read_b128 v[60:63], %[ab] offset:192\n\t"
                     AND3("v40", "v41", "v42") AND3("%[t]", "v43", "v44") AND3("%[t]", "v45", "v46") AND3("%[t]", "1", "v47")
                     "ds_read_b128 v[40:43], %[ab] offset:256\n\tds_read_b128 v[44:47], %[ab] offset:320\n\t"
                     "v_cmp_eq_u32 vcc, 0, %[t]\n\ts_cselect_b64 vcc, 0, vcc\n\ts_cbranch_vccnz 1b\n\t"
                     HK(5, 48, w0, "0") HK(4, 52, w1, "v[36:39]") HK(3, 56, w2, "v[36:39]") HK(2, 60, w3, "v[36:39]")
                     HK(1, 40, w4, "v[36:39]") HK(0, 44, w5, "v[36:39]")
                     "s_nop 7\n\tv_mov_b32_dpp v37, v36" Q1 CV "v36, v36, v37\n\tv_or_b32 v36, 1, v36\n\t"
                     "buffer_store_dword v36, %[ds], %[rq], 0 offen sc1\n\tbuffer_store_dword v36, %[ds], %[rq], s89 offen sc1"
                     : [t] "=&v"(t), "+{v[40:43]}"(pa), "+{v[44:47]}"(pb)
                     : WIN6(wf), [of] "v"(off), [rq] "s"(rq), [aw] "v"(aw), [ab] "v"(ab), [ds] "v"(dst), [sd] "n"(SIDE)
                     : V36, "v48", "v49", "v50", "v51", V52, "s88", "s89", "s90", "vcc", "scc", "memory");
      }
    } else {
      const int lane = fresh6(lane0);
      const int n = lane & 15, G = lane >> 4;
      if (half == 1) {
        x_update<true>(smem, rq, lane, gy - GY_PAIR + GY_INST + gy_row(wave), land + L_LAND2, wave);
      } else {
        int wn = nin + NPF;
#pragma unroll 1
        for (u32 tw = tref + 24 * T_FW; wn != 0; tw += 24 * T_FD) {
          wait_until(tw);
          WNEXT
        }
      }
      G80 a;
      g80_addr(a, lane, L_WB + wave * 5120, L_HB + 160 * wave, land, L_ONE);
      const u32 voff = fresh(gy + gy_row(wave) + ((lane * 16) ^ ((lane >> 4) * 64)));
      const int rw = fresh(L_RED + n * 144 + wave * 16 + G * 4);
      const int sw_ = fresh(n == 8 ? L_ST + wave * 16 + G * 4 : L_ACT + lane * 4);
      const int rr = fresh(L_RED + (lane & 31) * 144 + (lane >> 5) * 64), rs_ = fresh(L_ST + 16 * (lane & 7));
      const u32 dst = fresh(lane < 32 ? gh + fslot + lane * 4 : OOB);
      if (wave == 7) {
        if (half)
          wait_tt(A_Y);
        else
          wait_tt<L_AN0>(B_Y);
      }
      if (wave == 6 && half == 0) wait_until(tref + 24 * T_YL);
      asm volatile(POLL_FIRST "s_waitcnt vmcnt(2)"
                   :
                   : [vo] "v"(voff), [rq] "s"(rq), [land] "s"(land)
                   : "memory");
      g80_load(a, smem);
      u32 t;
#define SH4 " row_shr:4" ALL
      asm volatile(USED10 "s_mov_b32 s89, 0x810000\n\t" POLL_HEAD "s_branch 1f\n2:\n\t" POLL_MASK POLL_LOAD "\n1:\n\t" POLL_READ
                   "s_waitcnt lgkmcnt(2)\n\t"
                   MF "v[40:43], " F0 ", " F0 ", 0\n\t" AND3("v52", "v53", "v54")
                   MF "v[44:47], " F0 ", %[w0], 0\n\t" MF "v[48:51], " F0 ", %[w1], 0\n\t"
                   "s_waitcnt lgkmcnt(1)\n\t"
                   MF "v[40:43], " F1 ", " F1 ", v[40:43]\n\t" AND3("%[t]", "v55", "v56") AND3("%[t]", "v57", "v58")
                   "s_waitcnt lgkmcnt(0)\n\t"
                   MF "v[40:43], " F2 ", " F2 ", v[40:43]\n\t" AND3("%[t]", "v59", "v60") AND3("%[t]", "v61", "v62")
                   MF "v[44:47], " F1 ", %[w2], v[44:47]\n\t" AND3("%[t]", "1", "v63") "v_cmp_eq_u32 vcc, 0, %[t]\n\t"
                   MF "v[48:51], " F1 ", %[w3], v[48:51]\n\ts_and_b64 vcc, vcc, s[80:81]\n\ts_cbranch_scc1 2b\n"
                   "3:\n\t" MF "v[44:47], " F2 ", %[w4], v[44:47]\n\t" PK(40, 42) "v_add_f32 v40, v40, v41\n\t"
                   MF "v[48:51], " F2 ", %[w5], v[48:51]\n\ts_cmp_eq_u32 %[wv], 0\n\t"
                   "v_add_f32_dpp v40, v40, v40" Q1 "s_nop 1\n\tv_add_f32_dpp v40, v40, v40" Q2 PK(44, 46)
                   "v_add_f32 v44, v44, v45\n\tv_add_f32_dpp v41, v40, v40" SH4 PK(48, 50) "v_add_f32 v48, v48, v49\n\t"
                   "v_add_f32_dpp v40, v41, v40" SH4
                   "ds_write2st64_b32 %[rw], v44, v48 offset1:9\n\tds_write_b32 %[sw], v40\n\t"
                   "s_cselect_b64 exec, -1, 0\n\ts_bitcmp1_b32 %[j], 0\n\ts_waitcnt lgkmcnt(0)\n\ts_barrier\n\t"
                   "ds_read_b128 v[40:43], %[rs]\n\tds_read_b128 " F0 ", %[rr]\n\tds_read_b128 " F1 ", %[rr] offset:16\n\t"
                   "ds_read_b128 " F2 ", %[rr] offset:32\n\tds_read_b128 v[44:47], %[rr] offset:48\n\ts_cbranch_execz 9f\n\t"
                   "s_waitcnt lgkmcnt(4)\n\tv_add_f32 v40, v40, v41\n\tv_add_f32 v42, v40, v42\n\tv_add_f32_dpp v43, v43, v43" Q1
                   "s_waitcnt lgkmcnt(2)\n\tv_add_f32_dpp v42, v42, v42" Q1 "v_add_f32_dpp v43, v43, v43" Q2 PK(52, 56)
                   "v_add_f32_dpp v42, v42, v42" Q2 "v_add_f32_dpp v43, v43, v43" HM PK(54, 58) "v_add_f32_dpp v42, v42, v42" HM
                   "v_mul_f32 v43, 0x3acccccd, v43\n\tv_mul_f32 v42, 0x3acccccd, v42\n\tv_fma_f32 v42, -v43, v43, v42\n\t"
                   "s_waitcnt lgkmcnt(0)\n\t" PK(52, 60) PK(54, 62) PK(52, 44) PK(54, 46)
                   CV "v43, 0, v43\n\tv_add_f32 v52, v52, v53\n\tv_fma_f32 v54, -v43, v55, v54\n\t"
                   "v_add_f32 v53, v52, v54\n\tv_add_f32 v52, v52, v54\n\t"
                   CV "v42, 0, |v42|\n\tv_add_f32 v42, 0x3727c5ac, v42\n\t" CV "v42, 0, v42\n\tv_sqrt_f32 v42, v42\n\t"
                   "v_permlane32_swap_b32 v52, v53\n\t" CV "v42, 0, v42\n\tv_rcp_f32 v42, v42\n\tv_add_f32 v52, v52, v53\n\t"
                   "v_mul_f32 v52, v52, v42\n\t" CV "v53, 0, v52\n\tv_mul_f32 v54, 0xbfb8aa3b, v53\n\tv_exp_f32 v54, v54\n\t"
                   "s_cbranch_scc1 8f\n\tv_add_f32 v54, 1.0, v54\n\tv_rcp_f32 v54, v54\n\ts_nop 0\n\tv_mul_f32 v52, v53, v54\n"
                   "8:\n\t" CV "v52, s89, v52\n\tbuffer_store_dword v52, %[ds], %[rq], 0 offen sc1\n"
                   "9:\n\ts_mov_b64 exec, -1"
                   : [t] "=&v"(t)
                   : [ra] "v"(a.ra), [rb] "v"(a.rb), [rc] "v"(a.rc), WIN6(a.w), [vo] "v"(voff), [rq] "s"(rq), [land] "s"(land),
                     [rw] "v"(rw), [sw] "v"(sw_), [rr] "v"(rr), [rs] "v"(rs_), [ds] "v"(dst), [wv] "s"(wave), [j] "s"(j)
                   : V40, V52, S80);
      tref = now();
      if (half == 0 && wave == 1 && j == 20 && lane0 < 8) {
        const u32 nw = rtnow() * 8u, an = *(volatile LAS u32*)(smem + L_AN0);
        const u32 sv = (int)(an + B_H - B_T - nw) >= 0 ? an + B_E : nw + D_E;
        __builtin_amdgcn_raw_buffer_store_b32(((sv & 0xffffffu) << 1) | *(volatile LAS u32*)(smem + L_CLK), rs, gy_rep(fresh6(lane0)) + st_line(g), 0, 16);
      }
#define GET_REF                                                                            \
  const u32 te = __builtin_amdgcn_readfirstlane(*(volatile LAS u32*)(smem + L_AN0)) + B_E;  \
  const u32 so = fresh(ar + st_line(fresh6(lane0) & 7));                                    \
  wait_tt<L_AN0>(B_H - B_A);                                                                \
  if (stamp_poll(smem, rq, so, te) != te) wait_tt(0u);
      if (half == 0 && wave == 7) {
        GET_REF
      } else if (half == 0 && wave != 0) {
#pragma unroll 1
        for (int i = wave;; i = (i + 1) & 7) {
          x_update<false>(smem, rq, fresh6(lane0), 0u, L_LAND + i * 2048, i);
          if (i < 6) break;
        }
      }
    }
    if (half == 1) break;
    {
      if (wave < 7) {
        const u32 gq = ar + A_GQ + hsub * 16384 + c * 896 + wave * 128;
        G80 a;
        u32 t;
        if (wave == 6) {
          const int lane = fresh6(lane0);
          const int n = lane & 15;
          const u32 voff = fresh(gy + GY_INST + gy_row(c) + ((lane * 16) ^ ((lane >> 4) * 64)));
          asm volatile(POLL_FIRST : : [vo] "v"(voff), [rq] "s"(rq), [land] "s"(land) : "memory");
          g80_addr(a, lane, 0, L_HB + 160 * c, land, L_ONE);
          const u32 dst = fresh((gq + (lane >> 4) * 4) | (u32)((n ^ 8) + 15) >> 4 << 31);
          asm volatile(USED10 POLL_HEAD "s_branch 1f\n"
                       "2:\n\t" POLL_MASK POLL_LOAD "\n1:\n\t" POLL_READ
                       "s_waitcnt lgkmcnt(2)\n\t" MF "v[44:47], " F0 ", " F0 ", 0\n\t" AND3("v52", "v53", "v54")
                       "s_waitcnt lgkmcnt(1)\n\t" MF "v[44:47], " F1 ", " F1 ", v[44:47]\n\t" AND3("%[t]", "v55", "v56") AND3("%[t]", "v57", "v58")
                       "s_waitcnt lgkmcnt(0)\n\t" MF "v[44:47], " F2 ", " F2 ", v[44:47]\n\t" AND3("%[t]", "v59", "v60")
                       AND3("%[t]", "v61", "v62") AND3("%[t]", "1", "v63")
                       "v_cmp_eq_u32 vcc, 0, %[t]\n\ts_and_b64 vcc, vcc, s[80:81]\n\ts_cbranch_scc1 2b\n3:\n\ts_nop 1\n\t"
                       PK(44, 46) "v_add_f32 v44, v44, v45\n\ts_nop 1\n\tv_add_f32_dpp v44, v44, v44" Q1
                       "s_nop 1\n\tv_add_f32_dpp v44, v44, v44" Q2 "s_nop 1\n\tv_add_f32_dpp v45, v44, v44" SH4
                       "s_nop 1\n\tv_add_f32_dpp v44, v45, v44" SH4 "v_or_b32 v44, 1, v44\n\t"
                       "buffer_store_dword v44, %[ds], %[rq], 0 offen sc1"
                       : [t] "=&v"(t)
                       : [ra] "v"(a.ra), [rb] "v"(a.rb), [rc] "v"(a.rc), [vo] "v"(voff), [rq] "s"(rq), [land] "s"(land), [ds] "v"(dst)
                       : V40, V52, S80);
        } else {
        const int lane = fresh6(lane0);
        const u32 voff = fresh(gy + GY_INST + gy_row(c) + ((lane * 16) ^ ((lane >> 4) * 64)));
        g80_addr(a, lane, L_WQ + wave * 5120, L_HB + 160 * c, land, L_ZERO);
        g80_load(a, smem);
        const u32 dst = fresh((gq + lane * 4) | (u32)lane >> 5 << 31);
        if (wave == 4) {
          wait_tt<L_AN0>(B_E);
        }
        if (wave == 5 && isO) {
          wait_until(tref + 24 * T_AO);
          GET_REF
        }
        asm volatile(POLL_FIRST USED10 POLL_HEAD "s_branch 1f\n"
                     "2:\n\t" POLL_MASK POLL_LOAD "\n1:\n\t" POLL_READ
                     "s_waitcnt lgkmcnt(2)\n\t"
                     MF "v[44:47], " F0 ", %[w0], 0\n\t" AND3("v52", "v53", "v54") MF "v[48:51], " F0 ", %[w1], 0\n\t"
                     "s_waitcnt lgkmcnt(1)\n\t"
                     MF "v[44:47], " F1 ", %[w2], v[44:47]\n\t" AND3("%[t]", "v55", "v56") AND3("%[t]", "v57", "v58")
                     MF "v[48:51], " F1 ", %[w3], v[48:51]\n\t"
                     "s_waitcnt lgkmcnt(0)\n\t"
                     MF "v[44:47], " F2 ", %[w4], v[44:47]\n\t" MF "v[48:51], " F2 ", %[w5], v[48:51]\n\t"
                     AND3("%[t]", "v59", "v60") AND3("%[t]", "v61", "v62") AND3("%[t]", "1", "v63")
                     "v_cmp_eq_u32 vcc, 0, %[t]\n\ts_and_b64 vcc, vcc, s[80:81]\n\ts_cbranch_scc1 2b\n"
                     "3:\n\ts_nop 0\n\t" PK(44, 46) PK(48, 50) "v_add_f32 v44, v44, v45\n\tv_add_f32 v48, v48, v49\n\ts_nop 1\n\t"
                     "v_permlane16_swap_b32 v44, v48\n\tv_add_f32 v45, v44, v48\n\tv_add_f32 v44, v44, v48\n\ts_nop 1\n\t"
                     "v_permlane32_swap_b32 v44, v45\n\tv_add_f32 v44, v44, v45\n\tv_or_b32 v44, 1, v44\n\t"
                     "buffer_store_dword v44, %[ds], %[rq], 0 offen sc1"
                     : [t] "=&v"(t)
                     : [ra] "v"(a.ra), [rb] "v"(a.rb), [rc] "v"(a.rc), WIN6(a.w), [vo] "v"(voff), [rq] "s"(rq), [land] "s"(land),
                       [ds] "v"(dst)
                     : V40, V52, S80);
        }
      } else {
        bar();
      }
      if (!isO) x_request(rq, fresh6(lane0), gy + GY_INST + gy_row(wave), land + L_LAND2);
    }
  }
#undef RS
#define RS(n) "s_memrealtime s[" #n ":" #n "+1]\n\ts_nop 1\n\t"
#define RD(n) "s_sub_i32 s80, s80, s" #n "\n\t"
  asm volatile("s_branch 9f\n.Lmk_wait:\n\t"
               "ds_read_b64 v[62:63], v62\n\ts_waitcnt lgkmcnt(0)\n\t"
               "v_readfirstlane_b32 s81, v63\n\tv_readfirstlane_b32 s82, v62\n\t"
               "s_add_i32 s82, s82, s80\n\ts_mul_i32 s80, s80, s81\n\ts_lshr_b32 s80, s80, 9\n\ts_add_i32 s80, s80, s82\n\t"
               "s_mul_i32 s81, s81, -3\n\ts_addk_i32 s81, 1549\n"
               "1:\n\ts_memrealtime s[98:99]\n\ts_waitcnt lgkmcnt(0)\n\ts_lshl_b32 s98, s98, 3\n\ts_sub_i32 s98, s80, s98\n\t"
               "s_cmpk_gt_i32 s98, 8*20\n\ts_cbranch_scc1 1b\n\ts_cmpk_lt_i32 s98, 8*9\n\ts_cbranch_scc1 3f\n\t"
               RS(84) RS(86) RS(88) RS(90) RS(92) RS(94) RS(96) "s_memrealtime s[98:99]\n\t"
               "s_getpc_b64 s[82:83]\n2:\n\ts_add_u32 s82, s82, 3f-2b\n\ts_addc_u32 s83, s83, 0\n\ts_waitcnt lgkmcnt(0)\n\t"
               RD(84) RD(86) RD(88) RD(90) RD(92) RD(94) RD(96) RD(98)
               "s_mul_i32 s80, s80, s81\n\ts_ashr_i32 s80, s80, 10\n\ts_sub_i32 s80, s80, 100\n\t"
               "s_max_i32 s80, s80, 0\n\ts_min_i32 s80, s80, 4*26\n\ts_andn2_b32 s80, s80, 3\n\t"
               "s_sub_u32 s82, s82, s80\n\ts_subb_u32 s83, s83, 0\n\ts_setpc_b64 s[82:83]\n\t"
               ".rept 26\n\ts_nop 1\n\t.endr\n3:\n\ts_setpc_b64 s[78:79]\n9:"
               :);
  asm volatile(".Lmk_code_end:");
}

}

extern "C" void kernel_launch(void* const* d_in, const int* in_sizes, int n_in, void* d_out, int out_size, void* d_ws,
                              size_t ws_size, hipStream_t stream) {
  Params P;
  P.x0 = (const u16*)d_in[0];
  P.kc = (const u16*)d_in[1];
  P.vc = (const u16*)d_in[2];
  P.wq = (const u16*)d_in[3];
  P.wo = (const u16*)d_in[4];
  P.w1 = (const u16*)d_in[6];
  P.w3 = (const u16*)d_in[7];
  P.w2 = (const u16*)d_in[8];
  P.out = (u16*)d_out;
  P.ws = (u32*)d_ws;
  mega_kernel<<<NWG, NT, 0, stream>>>(P);
}
```
